```python
import torch

def _softmax(x, axis=-1):
    return torch.softmax(x, dim=axis)
def _gelu(x):
    return torch.nn.functional.gelu(x, approximate="tanh")
def _silu(x):
    return torch.nn.functional.silu(x)
def _sigmoid(x):
    return torch.sigmoid(x)
def _layernorm(x, axis=-1, eps=1e-5):
    m = x.mean(dim=axis, keepdim=True)
    v = x.var(dim=axis, keepdim=True, unbiased=False)
    return (x - m) / torch.sqrt(v + eps)
def _scatter_add(tgt, idx, src, axis=0):
    return tgt.clone().index_add_(axis, idx, src)
def _segment_sum(src, seg, n):
    out = torch.zeros((n,) + src.shape[1:], dtype=src.dtype)
    return out.index_add_(0, seg, src)
def _pad(x, pw):
    flat = tuple(v for pair in reversed(pw) for v in pair)
    return torch.nn.functional.pad(x, flat)

kernel_name = 'standalone__modern_conv_block_convnext__vision'

def setup_inputs():

    _w = (lambda a: (lambda t: t.to(torch.bfloat16) if t.is_floating_point() else t)(torch.as_tensor(a)))
    H = W = 224
    C = 192
    K = 3
    return {
        "x": _w((lambda *s: torch.randn(*s, dtype=torch.bfloat16) * 0.05)(1, H, W, C)),
        "dw": _w((lambda *s: torch.randn(*s, dtype=torch.bfloat16) * 0.05)(K, K, C)),
        "w1": _w((lambda *s: torch.randn(*s, dtype=torch.bfloat16) * 0.05)(C, 4 * C)),
        "b1": _w((lambda *s: torch.randn(*s, dtype=torch.bfloat16) * 0.2)(4 * C)),
        "w2": _w((lambda *s: torch.randn(*s, dtype=torch.bfloat16) * 0.05)(4 * C, C)),
        "b2": _w((lambda *s: torch.randn(*s, dtype=torch.bfloat16) * 0.4)(C)),
        "gamma": _w((lambda *s: torch.randn(*s, dtype=torch.bfloat16) * 0.05)(C)),
    }

def reference(x, dw, w1, b1, w2, b2, gamma):
    B, H, W, C = x.shape
    K = dw.shape[0]
    pad = K // 2
    xp = _pad(x, ((0,0),(pad,pad),(pad,pad),(0,0)))
    out = torch.zeros_like(x)
    for i in range(K):
        for j in range(K):
            out = out + xp[:, i:i+H, j:j+W, :] * dw[i, j][None, None, None, :]
    h = _layernorm(out, axis=-1)
    h = torch.einsum('bhwc,ce->bhwe', h, w1) + b1
    h = _gelu(h)
    h = torch.einsum('bhwe,ec->bhwc', h, w2) + b2
    return x + gamma[None, None, None, :] * h
```

```cpp
#include <hip/hip_runtime.h>
#include <hip/hip_bf16.h>
#include <stdint.h>

__device__ inline float bf2f(uint16_t v) { return __uint_as_float(((uint32_t)v) << 16); }
__device__ inline uint16_t f2bf(float f) { __hip_bfloat16 b = __float2bfloat16(f); return *(uint16_t*)&b; }

__global__ __launch_bounds__(256) void naive_kernel(const uint16_t* x, const uint16_t* dw, const uint16_t* w1, const uint16_t* b1,
    const uint16_t* w2, const uint16_t* b2, const uint16_t* gamma, uint16_t* out) {
  __shared__ float a[8][192];
  __shared__ float h[8][768];
  const int p0 = blockIdx.x * 8;
  const int t = threadIdx.x;
  if (t < 192) {
    for (int p = 0; p < 8; ++p) {
      int pix = p0 + p, y = pix / 224, xx = pix % 224;
      float s = 0.f;
      for (int i = 0; i < 3; ++i) for (int j = 0; j < 3; ++j) {
        int yy = y + i - 1, xj = xx + j - 1;
        if (yy >= 0 && yy < 224 && xj >= 0 && xj < 224) s += bf2f(x[(yy * 224 + xj) * 192 + t]) * bf2f(dw[(i * 3 + j) * 192 + t]);
      }
      a[p][t] = s;
    }
  }
  __syncthreads();
  if (t < 8) {
    float m = 0.f; for (int c = 0; c < 192; ++c) m += a[t][c]; m /= 192.f;
    float v = 0.f; for (int c = 0; c < 192; ++c) { float d = a[t][c] - m; v += d * d; } v /= 192.f;
    float r = 1.f / sqrtf(v + 1e-5f);
    for (int c = 0; c < 192; ++c) a[t][c] = bf2f(f2bf((a[t][c] - m) * r));
  }
  __syncthreads();
  for (int e = t; e < 768; e += 256) {
    float acc[8] = {0};
    for (int c = 0; c < 192; ++c) { float w = bf2f(w1[c * 768 + e]);
      #pragma unroll
      for (int p = 0; p < 8; ++p) acc[p] += a[p][c] * w; }
    for (int p = 0; p < 8; ++p) { float v = acc[p] + bf2f(b1[e]);
      float u = 0.7978845608028654f * (v + 0.044715f * v * v * v);
      h[p][e] = bf2f(f2bf(0.5f * v * (1.f + tanhf(u)))); }
  }
  __syncthreads();
  if (t < 192) {
    float acc[8] = {0};
    for (int e = 0; e < 768; ++e) { float w = bf2f(w2[e * 192 + t]);
      #pragma unroll
      for (int p = 0; p < 8; ++p) acc[p] += h[p][e] * w; }
    for (int p = 0; p < 8; ++p) { int pix = p0 + p;
      out[pix * 192 + t] = f2bf(bf2f(x[pix * 192 + t]) + bf2f(gamma[t]) * (acc[p] + bf2f(b2[t]))); }
  }
}

extern "C" void kernel_launch(void* const* d_in, const int* in_sizes, int n_in,
                              void* d_out, int out_size, void* d_ws, size_t ws_size,
                              hipStream_t stream) {
  naive_kernel<<<50176 / 8, 256, 0, stream>>>((const uint16_t*)d_in[0], (const uint16_t*)d_in[1], (const uint16_t*)d_in[2], (const uint16_t*)d_in[3],
     (const uint16_t*)d_in[4], (const uint16_t*)d_in[5], (const uint16_t*)d_in[6], (uint16_t*)d_out);
}
```

```cpp
#include <hip/hip_runtime.h>
#include <hip/hip_bf16.h>
#include <stdint.h>

#define CLOB_V "v1","v2","v3","v4","v5","v6","v7","v8","v9","v10","v11","v12","v13","v14","v15","v16","v17","v18","v19","v20","v21","v22","v23","v24","v25","v26","v27","v28","v29","v30","v31","v32","v33","v34","v35","v36","v37","v38","v39","v40","v41","v42","v43","v44","v45","v46","v47","v48","v49","v50","v51","v52","v53","v54","v55","v56","v57","v58","v59","v60","v61","v62","v63","v64","v127","v128","v191","v192","v254","v255"
#define CLOB_A "a0","a1","a63","a64","a127","a128","a191","a192","a254","a255"
#define CLOB_S "s4","s5","s6","s7","s8","s9","s10","s11","s12","s13","s14","s15","s16","s17","s18","s19","s20","s21","s22","s23","s24","s25","s26","s27","s28","s29","s30","s31","s33","s34","s35","s36","s37","s38","s39","s40","s41","s42","s43","s44","s45","s46","s47","s48","s49","s50","s51","s52","s53","s54","s55","s56","s57","s58","s59","s60","s61","s62","s63","s64","s65","s66","s67","s68","s69","s70","s71","s72","s73","s74","s75","s76","s77","s78","s79","s80","s81","s82","s83","s84","s85","s86","s87","s88","s89","s90","s91","s92","s93","s94","s95","s96","s97","s98","s99","s100","s101"

__global__ __launch_bounds__(256, 1) void convnext_fused_kernel(const uint16_t* x, const uint16_t* dw, const uint16_t* w1, const uint16_t* b1,
    const uint16_t* w2, const uint16_t* b2, const uint16_t* gamma, uint8_t* ws, uint16_t* out) {
  __shared__ __attribute__((aligned(16))) uint8_t lds[163840];
  const unsigned bid = blockIdx.x;
  const unsigned ldsbase = (unsigned)(size_t)lds;
  unsigned tid = threadIdx.x;
  asm volatile(R"ASM(

L_entry:
s_getpc_b64 s[34:35]
s_lshr_b32 s31, %[bid], 3
v_min_u32 v3, 14, v0
s_mul_i32 s30, s31, 1920
v_lshl_add_u32 v3, v3, 7, s30
s_load_dwordx16 s[44:59], %[ka], 0
v_min_u32 v3, L_end_p-L_entry-192, v3
s_load_dwordx2 s[60:61], %[ka], 64
global_load_dword v251, v3, s[34:35] offset:64
s_and_b32 s30, %[bid], 7
s_add_u32 s31, s31, %[lb]
s_cmp_lt_u32 s31, 14
s_cbranch_scc0 L_plain
s_mul_i32 s42, s30, 14
s_add_u32 s42, s42, s31
s_lshl_b32 s25, s31, 4
s_mul_i32 s24, s30, 13
s_branch L_beg_x
L_plain:
s_mul_i32 s42, s30, 18
s_add_u32 s42, s42, s31
s_sub_u32 s42, s42, 14
s_cmp_lt_u32 s42, 140
s_cbranch_scc1 L_work
s_endpgm
L_work:
s_mul_i32 s24, s42, 147
s_lshr_b32 s24, s24, 11
s_mul_i32 s30, s24, 14
s_sub_u32 s25, s42, s30
s_lshl_b32 s25, s25, 4
s_mul_i32 s24, s24, 12
s_add_u32 s24, s24, 104
s_add_u32 s42, s42, 112
s_getpc_b64 s[34:35]
L_pc:
s_add_u32 s34, s34, L_beg_p-L_pc
s_addc_u32 s35, s35, 0
s_setpc_b64 s[34:35]
.p2align 3
L_beg_x:
v_lshrrev_b32 v1, 6, v0
s_nop 0
v_readfirstlane_b32 s20, v1
s_lshl_b32 s21, s20, 10
s_mul_i32 s27, s24, 224
s_add_u32 s27, s27, s25
s_sub_u32 s27, s27, 225
s_cmp_eq_u32 s25, 0
v_and_b32 v2, 63, v0
s_cselect_b32 s33, 0, 63
s_cmp_eq_u32 s25, 208
v_lshrrev_b32 v5, 3, v2
s_cselect_b32 s33, 17, s33
s_lshl_b32 s31, s20, 3
v_add_u32 v8, s31, v5
s_mov_b32 s62, 206
v_mul_u32_u24 v64, 3641, v8
v_add_u32 v73, 32, v8
v_lshrrev_b32 v65, 16, v64
s_mov_b32 s63, -18
v_mad_i32_i24 v66, v65, s63, v8
v_mad_u32_u24 v67, v65, s62, v8
v_mul_u32_u24 v74, 3641, v73
v_and_b32 v6, 7, v2
v_add_u32 v68, s27, v67
v_and_b32 v69, 6, v66
v_lshrrev_b32 v75, 16, v74
v_add_u32 v83, 64, v8
v_xor_b32 v70, v69, v6
v_mul_i32_i24 v71, 384, v68
v_mad_i32_i24 v76, v75, s63, v73
v_mad_u32_u24 v77, v75, s62, v73
v_mul_u32_u24 v84, 3641, v83
v_cmp_ne_u32 vcc, s33, v66
v_mov_b32 v7, 0x70000000
v_lshl_add_u32 v72, v70, 4, v71
v_add_u32 v78, s27, v77
v_and_b32 v79, 6, v76
v_lshrrev_b32 v85, 16, v84
v_cndmask_b32 v156, v7, v72, vcc
v_xor_b32 v80, v79, v6
v_mul_i32_i24 v81, 384, v78
v_mad_i32_i24 v86, v85, s63, v83
v_mad_u32_u24 v87, v85, s62, v83
v_cmp_ne_u32 vcc, s33, v76
v_lshl_add_u32 v82, v80, 4, v81
v_add_u32 v88, s27, v87
v_and_b32 v89, 6, v86
v_cndmask_b32 v157, v7, v82, vcc
v_xor_b32 v90, v89, v6
v_mul_i32_i24 v91, 384, v88
v_cmp_ne_u32 vcc, s33, v86
v_lshl_add_u32 v92, v90, 4, v91
v_lshlrev_b32 v31, 4, v2
v_cndmask_b32 v158, v7, v92, vcc
s_mov_b32 s10, 19267584
s_mov_b32 s11, 0x20000
s_mov_b32 s70, 3456
s_mov_b32 s71, 0x20000
s_add_u32 m0, s21, 137216
s_waitcnt lgkmcnt(0)
s_mov_b32 s68, s46
s_and_b32 s69, s47, 0xffff
s_nop 0
s_mov_b32 s8, s44
s_and_b32 s9, s45, 0xffff
buffer_load_dwordx4 v31, s[68:71], s21 offen lds
s_cmp_lt_u32 s20, 2
s_cselect_b32 s64, s50, s54
s_cselect_b32 s65, s51, s55
s_cselect_b32 s66, 2, 0
s_cselect_b32 s30, s21, 0
s_cmp_eq_u32 s20, 3
s_cselect_b32 s64, s56, s64
s_cselect_b32 s65, s57, s65
s_lshl_b32 s66, 384, s66
s_and_b32 s65, s65, 0xffff
s_mov_b32 s67, 0x20000
s_add_u32 m0, s21, 141312
s_nop 0
buffer_load_dwordx4 v31, s[64:67], s30 offen lds
s_add_u32 m0, s21, 0
v_add_u32 v64, 96, v8
buffer_load_dwordx4 v156, s[8:11], 0 offen lds
v_add_u32 v74, 128, v8
v_mul_u32_u24 v65, 3641, v64
v_mul_u32_u24 v75, 3641, v74
v_lshrrev_b32 v66, 16, v65
v_lshrrev_b32 v76, 16, v75
v_mad_i32_i24 v67, v66, s63, v64
v_mad_u32_u24 v68, v66, s62, v64
v_and_b32 v70, 6, v67
v_add_u32 v69, s27, v68
v_add_u32 v84, 160, v8
v_xor_b32_e64 v71, v70, v6
s_add_u32 m0, s21, 4096
v_mul_i32_i24 v72, 384, v69
buffer_load_dwordx4 v157, s[8:11], 0 offen lds
v_mad_i32_i24 v77, v76, s63, v74
v_mad_u32_u24 v78, v76, s62, v74
v_mul_u32_u24 v85, 3641, v84
v_cmp_ne_u32 vcc, s33, v67
v_lshl_add_u32 v73, v71, 4, v72
v_add_u32 v79, s27, v78
v_and_b32 v80, 6, v77
v_lshrrev_b32 v86, 16, v85
v_cndmask_b32 v159, v7, v73, vcc
v_xor_b32 v81, v80, v6
v_mul_i32_i24 v82, 384, v79
s_add_u32 m0, s21, 8192
v_mad_i32_i24 v87, v86, s63, v84
buffer_load_dwordx4 v158, s[8:11], 0 offen lds
v_mad_u32_u24 v88, v86, s62, v84
v_cmp_ne_u32 vcc, s33, v77
v_lshl_add_u32 v83, v81, 4, v82
v_add_u32 v89, s27, v88
v_and_b32 v90, 6, v87
v_cndmask_b32 v160, v7, v83, vcc
v_xor_b32_e64 v91, v90, v6
v_mul_i32_i24 v92, 384, v89
v_cmp_ne_u32 vcc, s33, v87
v_lshl_add_u32 v93, v91, 4, v92
s_nop 0
v_cndmask_b32 v161, v7, v93, vcc
s_add_u32 m0, s21, 12288
v_add_u32 v64, 192, v8
buffer_load_dwordx4 v159, s[8:11], 0 offen lds
s_and_b32 s30, s20, 1
v_mul_u32_u24 v65, 3641, v64
v_add_u32 v74, 224, v8
v_lshrrev_b32 v66, 16, v65
s_lshl_b32 s22, s30, 10
s_add_u32 s22, s22, 32768
v_mad_i32_i24 v67, v66, s63, v64
v_mad_u32_u24 v68, v66, s62, v64
v_mul_u32_u24 v75, 3641, v74
s_lshr_b32 s26, s22, 7
v_add_u32 v69, s27, v68
v_and_b32_e64 v70, 6, v67
s_add_u32 m0, s21, 16384
v_lshrrev_b32_e64 v76, 16, v75
buffer_load_dwordx4 v160, s[8:11], 0 offen lds
v_add_u32 v84, s26, v5
v_xor_b32 v71, v70, v6
v_mul_i32_i24 v72, 384, v69
v_mad_i32_i24 v77, v76, s63, v74
v_mad_u32_u24 v78, v76, s62, v74
v_mul_u32_u24 v85, 3641, v84
v_cmp_ne_u32 vcc, s33, v67
v_lshl_add_u32 v73, v71, 4, v72
v_add_u32 v79, s27, v78
v_and_b32_e64 v80, 6, v77
v_lshrrev_b32 v86, 16, v85
v_cndmask_b32 v162, v7, v73, vcc
s_add_u32 m0, s21, 20480
v_xor_b32_e64 v81, v80, v6
buffer_load_dwordx4 v161, s[8:11], 0 offen lds
v_mul_i32_i24 v82, 384, v79
v_mad_i32_i24 v87, v86, s63, v84
v_mad_u32_u24 v88, v86, s62, v84
v_cmp_ne_u32 vcc, s33, v77
v_lshl_add_u32 v83, v81, 4, v82
v_add_u32 v89, s27, v88
v_and_b32 v90, 6, v87
v_cndmask_b32 v163, v7, v83, vcc
v_xor_b32 v91, v90, v6
v_mul_i32_i24 v92, 384, v89
v_cmp_ne_u32 vcc, s33, v87
v_lshl_add_u32 v93, v91, 4, v92
s_nop 0
v_cndmask_b32 v164, v7, v93, vcc
s_add_u32 m0, s21, 24576
v_and_b32_e64 v64, 15, v0
buffer_load_dwordx4 v162, s[8:11], 0 offen lds
v_lshrrev_b32 v70, 4, v0
v_lshrrev_b32 v77, 4, v2
v_mul_u32_u24 v71, 240, v70
v_mul_u32_u24 v72, 20, v64
v_and_b32 v76, 15, v2
v_and_b32 v79, 1, v77
v_add_u32 v73, v71, v72
v_cmp_gt_u32 vcc, 12, v64
v_lshrrev_b32 v84, 1, v76
v_lshlrev_b32_e64 v85, 2, v79
v_add_u32 v74, 157696, v73
v_mov_b32 v75, 163584
v_lshrrev_b32 v78, 5, v2
v_and_b32 v80, 1, v76
v_sub_u32 v86, v84, v85
v_min_u32 v66, 11, v64
v_cndmask_b32 v19, v75, v74, vcc
v_lshlrev_b32 v81, 4, v80
v_mov_b32 v82, 0xffff
v_add_u32_e64 v87, v76, v78
v_add_u32 v92, 2, v76
v_cmp_eq_u32 vcc, 0, v86
v_bfe_u32 v65, v0, 4, 3
v_lshlrev_b32 v67, 5, v66
v_lshlrev_b32 v83, v81, v82
s_add_u32 m0, s21, 28672
v_and_b32_e64 v88, 6, v87
buffer_load_dwordx4 v163, s[8:11], 0 offen lds
v_and_b32_e64 v93, 6, v92
v_lshl_add_u32 v68, v65, 2, v67
v_cndmask_b32 v216, 0, v83, vcc
v_xor_b32 v89, v88, v79
v_xor_b32_e64 v94, v93, v79
s_lshr_b32 s30, s20, 1
v_cmp_eq_u32 vcc, 1, v86
s_mul_i32 s39, s30, 1152
s_mul_i32 s40, s30, 384
s_cmp_eq_u32 s30, 0
v_add_u32 v69, 137216, v68
v_lshlrev_b32 v90, 4, v89
v_lshlrev_b32_e64 v95, 4, v94
s_cselect_b32 s30, 0, 384
v_add_u32 v29, s30, v69
v_cndmask_b32 v217, 0, v83, vcc
v_lshl_add_u32 v91, v87, 7, v90
v_lshl_add_u32 v96, v92, 7, v95
v_cmp_eq_u32 vcc, 2, v86
s_mul_i32 s30, s20, 6912
v_add_u32 v220, s30, v91
v_add_u32 v228, s30, v96
v_lshl_add_u32 v98, v78, 3, v84
s_lshl_b32 s30, s20, 5
v_cndmask_b32 v218, 0, v83, vcc
s_add_u32 m0, s22, 0
v_mul_u32_u24 v97, 2304, v78
buffer_load_dwordx4 v164, s[8:11], 0 offen lds
v_mul_u32_u24 v99, 240, v98
v_xor_b32 v101, s30, v96
v_cmp_eq_u32 vcc, 3, v86
v_add_u32_e64 v224, v97, v228
v_add_u32 v235, 157696, v99
v_xor_b32 v100, s30, v91
v_add_u32 v234, 27648, v101
s_mul_i32 s31, s20, 20
s_mov_b32 s36, 128
s_mov_b32 s37, 256
v_add_u32 v27, s40, v69
v_add_u32 v28, s39, v69
v_cndmask_b32 v219, 0, v83, vcc
v_xor_b32 v221, 32, v220
v_xor_b32 v225, 32, v224
v_xor_b32 v229, 32, v228
v_xor_b32 v222, 64, v220
v_xor_b32 v226, 64, v224
v_xor_b32_e64 v230, 64, v228
v_xor_b32 v223, 96, v220
v_xor_b32 v227, 96, v224
v_xor_b32 v231, 96, v228
v_add_u32 v232, 27648, v100
v_add_u32 v233, v97, v234
v_add_u32 v243, s31, v235
v_mov_b32 v248, v31
v_mov_b32 v249, v0
v_bfe_u32 v80, v249, 3, 3
v_and_b32 v82, 7, v249
v_and_b32 v81, 6, v80
s_mov_b32 s30, 141312
v_lshl_add_u32 v134, v249, 1, s30
v_xor_b32_e64 v83, v81, v82
s_mov_b32 s30, 153600
v_lshl_add_u32 v47, v249, 2, s30
v_lshlrev_b32_e64 v84, 4, v83
v_mul_u32_u24 v85, 1536, v80
s_and_b32 s13, s59, 0xffff
s_mul_i32 s30, s20, 73728
s_mul_i32 s23, s20, 6144
s_mov_b32 s12, s58
s_mov_b32 s14, 590912
s_mov_b32 s15, 0x20000
s_mov_b32 s4, s48
s_and_b32 s5, s49, 0xffff
s_mov_b32 s6, 294912
s_mov_b32 s7, 0x20000
v_add3_u32 v255, v84, v85, s30
v_and_b32_e64 v80, 15, v249
v_bfe_u32 v81, v249, 4, 2
v_add_u32_e64 v82, 1, v80
s_and_b32 s17, s61, 0xffff
v_and_b32_e64 v83, 6, v82
s_mul_i32 s30, s20, 6912
v_xor_b32_e64 v84, v83, v81
s_add_u32 s30, s30, 2304
v_lshlrev_b32 v85, 4, v84
v_cmp_eq_u32 vcc, 0, v249
v_lshl_add_u32 v86, v82, 7, v85
v_mov_b32 v88, 0x70000000
v_add_u32_e64 v236, s30, v86
s_mov_b32 s30, 156672
v_lshl_add_u32 v253, v81, 5, s30
s_lshl_b32 s30, s20, 5
s_and_b32 s30, s30, 64
v_xor_b32 v87, s30, v86
s_lshl_b32 s30, s30, 1
s_and_b32 s31, s20, 1
s_lshl_b32 s31, s31, 4
s_add_u32 s30, s30, s31
s_lshl_b32 s31, s42, 2
v_mov_b32 v89, s31
s_mov_b32 s16, s60
s_mov_b32 s18, 19267584
s_mov_b32 s19, 0x20000
s_mov_b32 s28, 0x409d30fc
s_mov_b32 s29, 0x409d30fc
v_xor_b32_e64 v237, 64, v236
v_add_u32 v252, 29952, v87
v_add_u32 v254, s30, v253
v_cndmask_b32 v250, v88, v89, vcc
v_add_u32_e64 v248, s23, v248
s_mov_b32 s30, 590848
buffer_load_dword v133, off, s[12:15], s30 sc1
s_waitcnt vmcnt(10)
s_barrier
ds_read_b32 v20, v27 offset:0
ds_read_b32 v21, v27 offset:1152
ds_read_b32 v22, v27 offset:2304
ds_read_b32 v23, v28 offset:768
ds_read_b32 v24, v29 offset:3072
ds_read_u16 v128, v134 offset:0
ds_read_u16 v129, v134 offset:512
ds_read_u16 v130, v134 offset:1024
ds_read_u16 v131, v134 offset:2048
ds_read_u16 v132, v134 offset:3072
s_add_u32 m0, s21, 34816
s_nop 0
buffer_load_dwordx4 v156, s[8:11], s36 offen lds
s_waitcnt lgkmcnt(8)
ds_write2_b32 v19, v20, v21 offset0:0 offset1:1
s_waitcnt lgkmcnt(7)
ds_write2_b32 v19, v22, v23 offset0:2 offset1:3
s_waitcnt lgkmcnt(7)
ds_write_b32 v19, v24 offset:16
s_waitcnt lgkmcnt(3)
s_add_u32 m0, s21, 38912
v_lshlrev_b32 v128, 16, v128
v_lshlrev_b32 v129, 16, v129
v_lshlrev_b32 v130, 16, v130
v_lshlrev_b32 v131, 16, v131
v_lshlrev_b32 v132, 16, v132
buffer_load_dwordx4 v157, s[8:11], s36 offen lds
v_mul_f32 v128, 0x3eeff558, v128
v_mul_f32 v129, 0x3eeff558, v129
v_mul_f32 v130, 0x3eeff558, v130
v_mul_f32_e64 v131, v131, v132
s_waitcnt lgkmcnt(0)
s_barrier
ds_write_b32 v47, v128 offset:0
ds_write_b32 v47, v129 offset:1024
ds_write_b32 v47, v130 offset:2048
ds_write_b32 v47, v131 offset:3072
ds_read_b128 v[208:211], v235 offset:0
ds_read2_b32 v[238:239], v243 offset0:0 offset1:1
ds_read2_b32 v[240:241], v243 offset0:2 offset1:3
ds_read_b32 v242, v243 offset:16
s_add_u32 m0, s21, 43008
s_nop 0
buffer_load_dwordx4 v158, s[8:11], s36 offen lds
s_waitcnt lgkmcnt(2)
v_and_b32 v16, v216, v238
v_and_b32 v17, v217, v238
v_and_b32 v18, v218, v238
v_and_b32 v19, v219, v238
v_and_b32 v64, v216, v239
v_and_b32 v65, v217, v239
v_and_b32_e64 v66, v218, v239
s_add_u32 m0, s21, 47104
v_and_b32_e64 v67, v219, v239
buffer_load_dwordx4 v159, s[8:11], s36 offen lds
s_waitcnt lgkmcnt(1)
v_and_b32 v112, v216, v240
v_and_b32 v113, v217, v240
v_and_b32 v114, v218, v240
v_and_b32 v115, v219, v240
v_and_b32 v20, v216, v241
v_and_b32 v21, v217, v241
v_and_b32 v22, v218, v241
s_add_u32 m0, s21, 51200
v_and_b32_e64 v23, v219, v241
buffer_load_dwordx4 v160, s[8:11], s36 offen lds
s_waitcnt lgkmcnt(0)
v_and_b32 v68, v216, v242
v_and_b32 v69, v217, v242
v_and_b32 v70, v218, v242
v_and_b32_e64 v71, v219, v242
ds_read_b128 v[212:215], v235 offset:16
ds_read2_b32 v[238:239], v243 offset0:20 offset1:21
ds_read2_b32 v[240:241], v243 offset0:22 offset1:23
ds_read_b32 v242, v243 offset:96
v_and_b32 v116, v216, v208
v_and_b32 v117, v217, v208
v_and_b32_e64 v118, v218, v208
s_add_u32 m0, s21, 55296
v_and_b32_e64 v119, v219, v208
buffer_load_dwordx4 v161, s[8:11], s36 offen lds
v_and_b32 v24, v216, v209
v_and_b32 v25, v217, v209
v_and_b32 v26, v218, v209
v_and_b32 v27, v219, v209
v_and_b32 v72, v216, v210
v_and_b32 v73, v217, v210
v_and_b32_e64 v74, v218, v210
s_add_u32 m0, s21, 59392
v_and_b32_e64 v75, v219, v210
buffer_load_dwordx4 v162, s[8:11], s36 offen lds
v_and_b32 v120, v216, v211
v_and_b32 v121, v217, v211
v_and_b32 v122, v218, v211
v_and_b32 v123, v219, v211
ds_read_b128 v[208:211], v235 offset:32
s_waitcnt lgkmcnt(4)
v_and_b32 v28, v216, v212
v_and_b32 v29, v217, v212
v_and_b32 v30, v218, v212
s_add_u32 m0, s21, 63488
v_and_b32 v31, v219, v212
buffer_load_dwordx4 v163, s[8:11], s36 offen lds
s_add_u32 m0, s22, 34816
s_nop 0
buffer_load_dwordx4 v164, s[8:11], s36 offen lds
s_waitcnt vmcnt(9)
v_readfirstlane_b32 s43, v133
s_nop 1
s_add_u32 s43, s43, 0x9e3779b9
s_barrier
ds_read_b128 a[156:159], v232 offset:0
ds_read_b128 a[160:163], v232 offset:2304
ds_read_b128 a[164:167], v232 offset:4608
ds_read_b128 a[168:171], v233 offset:0
ds_read_b128 a[172:175], v234 offset:4608
s_waitcnt lgkmcnt(4)
v_mfma_f32_16x16x32_bf16 v[144:147], v[16:19], a[156:159], 0
s_waitcnt lgkmcnt(3)
ds_read_b128 a[196:199], v220 offset:0
ds_read_b128 a[200:203], v220 offset:2304
v_mfma_f32_16x16x32_bf16 v[144:147], v[64:67], a[160:163], v[144:147]
s_waitcnt lgkmcnt(4)
ds_read_b128 a[204:207], v220 offset:4608
ds_read_b128 a[208:211], v224 offset:0
v_mfma_f32_16x16x32_bf16 v[144:147], v[112:115], a[164:167], v[144:147]
s_waitcnt lgkmcnt(5)
ds_read_b128 a[212:215], v224 offset:4608
ds_read_b128 a[216:219], v220 offset:6912
v_mfma_f32_16x16x32_bf16 v[144:147], v[20:23], a[168:171], v[144:147]
s_waitcnt lgkmcnt(6)
ds_read_b128 a[220:223], v224 offset:2304
ds_read_b128 a[224:227], v224 offset:6912
v_mfma_f32_16x16x32_bf16 v[144:147], v[68:71], a[172:175], v[144:147]
s_nop 0
ds_read_b128 a[228:231], v220 offset:9216
ds_read_b128 a[232:235], v228 offset:9216
s_waitcnt lgkmcnt(9)
v_mfma_f32_16x16x32_bf16 v[0:3], v[116:119], a[196:199], 0
s_waitcnt lgkmcnt(8)
ds_read_b128 a[156:159], v221 offset:0
v_and_b32 v16, v216, v213
v_mfma_f32_16x16x32_bf16 v[0:3], v[24:27], a[200:203], v[0:3]
s_waitcnt lgkmcnt(7)
s_add_u32 m0, s21, 69632
ds_read_b128 a[160:163], v221 offset:2304
v_and_b32_e64 v17, v217, v213
v_mfma_f32_16x16x32_bf16 v[0:3], v[72:75], a[204:207], v[0:3]
buffer_load_dwordx4 v156, s[8:11], s37 offen lds
v_and_b32 v18, v218, v213
v_and_b32 v19, v219, v213
v_mfma_f32_16x16x32_bf16 v[0:3], v[120:123], a[208:211], v[0:3]
s_waitcnt lgkmcnt(4)
ds_read_b128 a[164:167], v221 offset:4608
v_and_b32 v64, v216, v214
v_mfma_f32_16x16x32_bf16 v[0:3], v[28:31], a[212:215], v[0:3]
ds_read_b128 a[168:171], v225 offset:0
v_and_b32_e64 v65, v217, v214
v_mfma_f32_16x16x32_bf16 v[48:51], v[116:119], a[200:203], 0
v_and_b32 v66, v218, v214
v_and_b32 v67, v219, v214
v_mfma_f32_16x16x32_bf16 v[48:51], v[24:27], a[204:207], v[48:51]
ds_read_b128 a[172:175], v225 offset:4608
v_and_b32_e64 v112, v216, v215
v_mfma_f32_16x16x32_bf16 v[48:51], v[72:75], a[216:219], v[48:51]
ds_read_b128 a[176:179], v221 offset:6912
v_and_b32_e64 v113, v217, v215
v_mfma_f32_16x16x32_bf16 v[48:51], v[120:123], a[220:223], v[48:51]
v_and_b32 v114, v218, v215
v_and_b32 v115, v219, v215
v_mfma_f32_16x16x32_bf16 v[48:51], v[28:31], a[224:227], v[48:51]
s_add_u32 m0, s21, 73728
ds_read_b128 a[180:183], v225 offset:2304
ds_read_b128 v[212:215], v235 offset:48
v_and_b32_e64 v20, v216, v208
v_mfma_f32_16x16x32_bf16 v[96:99], v[116:119], a[204:207], 0
buffer_load_dwordx4 v157, s[8:11], s37 offen lds
ds_read_b128 a[184:187], v225 offset:6912
v_and_b32_e64 v21, v217, v208
v_mfma_f32_16x16x32_bf16 v[96:99], v[24:27], a[216:219], v[96:99]
s_waitcnt lgkmcnt(4)
v_and_b32 v22, v218, v208
v_and_b32_e64 v23, v219, v208
v_mfma_f32_16x16x32_bf16 v[96:99], v[72:75], a[228:231], v[96:99]
ds_read_b128 a[188:191], v221 offset:9216
v_and_b32_e64 v68, v216, v209
v_mfma_f32_16x16x32_bf16 v[96:99], v[120:123], a[212:215], v[96:99]
ds_read_b128 a[192:195], v229 offset:9216
v_and_b32_e64 v69, v217, v209
v_mfma_f32_16x16x32_bf16 v[96:99], v[28:31], a[232:235], v[96:99]
v_and_b32 v70, v218, v209
v_and_b32 v71, v219, v209
v_mfma_f32_16x16x32_bf16 v[4:7], v[16:19], a[156:159], 0
ds_read_b128 a[196:199], v222 offset:0
v_and_b32_e64 v116, v216, v210
v_mfma_f32_16x16x32_bf16 v[4:7], v[64:67], a[160:163], v[4:7]
ds_read_b128 a[200:203], v222 offset:2304
v_and_b32_e64 v117, v217, v210
v_mfma_f32_16x16x32_bf16 v[4:7], v[112:115], a[164:167], v[4:7]
s_add_u32 m0, s21, 77824
v_and_b32 v118, v218, v210
v_and_b32 v119, v219, v210
v_mfma_f32_16x16x32_bf16 v[4:7], v[20:23], a[168:171], v[4:7]
buffer_load_dwordx4 v158, s[8:11], s37 offen lds
ds_read_b128 a[204:207], v222 offset:4608
v_and_b32_e64 v24, v216, v211
v_mfma_f32_16x16x32_bf16 v[4:7], v[68:71], a[172:175], v[4:7]
ds_read_b128 a[208:211], v226 offset:0
v_and_b32_e64 v25, v217, v211
v_mfma_f32_16x16x32_bf16 v[52:55], v[16:19], a[160:163], 0
v_and_b32_e64 v26, v218, v211
v_and_b32 v27, v219, v211
v_mfma_f32_16x16x32_bf16 v[52:55], v[64:67], a[164:167], v[52:55]
s_waitcnt lgkmcnt(2)
ds_read_b128 a[212:215], v226 offset:4608
ds_read_b128 v[208:211], v235 offset:64
v_and_b32_e64 v72, v216, v212
v_mfma_f32_16x16x32_bf16 v[52:55], v[112:115], a[176:179], v[52:55]
ds_read_b128 a[216:219], v222 offset:6912
v_and_b32_e64 v73, v217, v212
v_mfma_f32_16x16x32_bf16 v[52:55], v[20:23], a[180:183], v[52:55]
v_and_b32 v74, v218, v212
v_and_b32 v75, v219, v212
v_mfma_f32_16x16x32_bf16 v[52:55], v[68:71], a[184:187], v[52:55]
ds_read_b128 a[220:223], v226 offset:2304
v_and_b32_e64 v120, v216, v213
v_mfma_f32_16x16x32_bf16 v[100:103], v[16:19], a[164:167], 0
s_add_u32 m0, s21, 81920
ds_read_b128 a[224:227], v226 offset:6912
v_and_b32_e64 v121, v217, v213
v_mfma_f32_16x16x32_bf16 v[100:103], v[64:67], a[176:179], v[100:103]
buffer_load_dwordx4 v159, s[8:11], s37 offen lds
v_and_b32 v122, v218, v213
v_and_b32 v123, v219, v213
v_mfma_f32_16x16x32_bf16 v[100:103], v[112:115], a[188:191], v[100:103]
ds_read_b128 a[228:231], v222 offset:9216
v_and_b32_e64 v16, v216, v214
v_mfma_f32_16x16x32_bf16 v[100:103], v[20:23], a[172:175], v[100:103]
ds_read_b128 a[232:235], v230 offset:9216
v_and_b32_e64 v17, v217, v214
v_mfma_f32_16x16x32_bf16 v[100:103], v[68:71], a[192:195], v[100:103]
v_and_b32 v18, v218, v214
v_and_b32 v19, v219, v214
v_mfma_f32_16x16x32_bf16 v[8:11], v[116:119], a[196:199], 0
ds_read_b128 a[156:159], v223 offset:0
v_and_b32 v64, v216, v215
v_mfma_f32_16x16x32_bf16 v[8:11], v[24:27], a[200:203], v[8:11]
s_waitcnt lgkmcnt(3)
ds_read_b128 a[160:163], v223 offset:2304
v_and_b32_e64 v65, v217, v215
v_mfma_f32_16x16x32_bf16 v[8:11], v[72:75], a[204:207], v[8:11]
v_and_b32 v66, v218, v215
v_and_b32 v67, v219, v215
v_mfma_f32_16x16x32_bf16 v[8:11], v[120:123], a[208:211], v[8:11]
s_add_u32 m0, s21, 86016
ds_read_b128 a[164:167], v223 offset:4608
ds_read_b128 v[212:215], v235 offset:80
v_and_b32_e64 v112, v216, v208
v_mfma_f32_16x16x32_bf16 v[8:11], v[16:19], a[212:215], v[8:11]
buffer_load_dwordx4 v160, s[8:11], s37 offen lds
ds_read_b128 a[168:171], v227 offset:0
v_and_b32_e64 v113, v217, v208
v_mfma_f32_16x16x32_bf16 v[56:59], v[116:119], a[200:203], 0
v_and_b32 v114, v218, v208
v_and_b32 v115, v219, v208
v_mfma_f32_16x16x32_bf16 v[56:59], v[24:27], a[204:207], v[56:59]
ds_read_b128 a[172:175], v227 offset:4608
v_and_b32_e64 v20, v216, v209
v_mfma_f32_16x16x32_bf16 v[56:59], v[72:75], a[216:219], v[56:59]
ds_read_b128 a[176:179], v223 offset:6912
v_and_b32_e64 v21, v217, v209
v_mfma_f32_16x16x32_bf16 v[56:59], v[120:123], a[220:223], v[56:59]
v_and_b32 v22, v218, v209
v_and_b32 v23, v219, v209
v_mfma_f32_16x16x32_bf16 v[56:59], v[16:19], a[224:227], v[56:59]
ds_read_b128 a[180:183], v227 offset:2304
v_and_b32_e64 v68, v216, v210
v_mfma_f32_16x16x32_bf16 v[104:107], v[116:119], a[204:207], 0
ds_read_b128 a[184:187], v227 offset:6912
v_and_b32 v69, v217, v210
v_mfma_f32_16x16x32_bf16 v[104:107], v[24:27], a[216:219], v[104:107]
s_waitcnt lgkmcnt(3)
s_add_u32 m0, s21, 90112
v_and_b32 v70, v218, v210
v_and_b32 v71, v219, v210
v_mfma_f32_16x16x32_bf16 v[104:107], v[72:75], a[228:231], v[104:107]
buffer_load_dwordx4 v161, s[8:11], s37 offen lds
ds_read_b128 a[188:191], v223 offset:9216
v_and_b32_e64 v116, v216, v211
v_mfma_f32_16x16x32_bf16 v[104:107], v[120:123], a[212:215], v[104:107]
ds_read_b128 a[192:195], v231 offset:9216
v_and_b32_e64 v117, v217, v211
v_mfma_f32_16x16x32_bf16 v[104:107], v[16:19], a[232:235], v[104:107]
v_and_b32 v118, v218, v211
v_and_b32 v119, v219, v211
v_mfma_f32_16x16x32_bf16 v[12:15], v[64:67], a[156:159], 0
v_and_b32 v16, v216, v238
v_and_b32 v17, v217, v238
v_mfma_f32_16x16x32_bf16 v[12:15], v[112:115], a[160:163], v[12:15]
v_and_b32 v18, v218, v238
v_and_b32 v19, v219, v238
v_mfma_f32_16x16x32_bf16 v[12:15], v[20:23], a[164:167], v[12:15]
v_and_b32 v24, v216, v239
v_and_b32 v25, v217, v239
v_mfma_f32_16x16x32_bf16 v[12:15], v[68:71], a[168:171], v[12:15]
v_and_b32 v26, v218, v239
v_and_b32 v27, v219, v239
v_mfma_f32_16x16x32_bf16 v[12:15], v[116:119], a[172:175], v[12:15]
s_add_u32 m0, s21, 94208
v_and_b32 v72, v216, v240
v_and_b32 v73, v217, v240
v_mfma_f32_16x16x32_bf16 v[60:63], v[64:67], a[160:163], 0
buffer_load_dwordx4 v162, s[8:11], s37 offen lds
v_and_b32 v74, v218, v240
v_and_b32 v75, v219, v240
v_mfma_f32_16x16x32_bf16 v[60:63], v[112:115], a[164:167], v[60:63]
s_waitcnt lgkmcnt(0)
v_and_b32 v120, v216, v241
v_and_b32_e64 v121, v217, v241
v_mfma_f32_16x16x32_bf16 v[60:63], v[20:23], a[176:179], v[60:63]
v_and_b32 v122, v218, v241
v_and_b32 v123, v219, v241
v_mfma_f32_16x16x32_bf16 v[60:63], v[68:71], a[180:183], v[60:63]
v_and_b32 v76, v216, v242
v_and_b32 v77, v217, v242
v_mfma_f32_16x16x32_bf16 v[60:63], v[116:119], a[184:187], v[60:63]
v_and_b32 v78, v218, v242
v_and_b32 v79, v219, v242
v_mfma_f32_16x16x32_bf16 v[108:111], v[64:67], a[164:167], 0
ds_read_b128 v[208:211], v235 offset:96
ds_read2_b32 v[238:239], v243 offset0:40 offset1:41
ds_read2_b32 v[240:241], v243 offset0:42 offset1:43
ds_read_b32 v242, v243 offset:176
v_and_b32 v124, v216, v212
v_and_b32 v125, v217, v212
v_mfma_f32_16x16x32_bf16 v[108:111], v[112:115], a[176:179], v[108:111]
v_and_b32 v126, v218, v212
v_and_b32 v127, v219, v212
v_mfma_f32_16x16x32_bf16 v[108:111], v[20:23], a[188:191], v[108:111]
s_add_u32 m0, s21, 98304
v_and_b32 v20, v216, v213
v_and_b32 v21, v217, v213
v_mfma_f32_16x16x32_bf16 v[108:111], v[68:71], a[172:175], v[108:111]
buffer_load_dwordx4 v163, s[8:11], s37 offen lds
v_and_b32 v22, v218, v213
v_and_b32 v23, v219, v213
v_mfma_f32_16x16x32_bf16 v[108:111], v[116:119], a[192:195], v[108:111]
v_and_b32_e64 v68, v216, v214
v_and_b32 v69, v217, v214
s_add_u32 m0, s22, 69632
s_nop 0
buffer_load_dwordx4 v164, s[8:11], s37 offen lds
ds_read_b128 a[12:15], v236 offset:0
ds_read_b128 a[16:19], v236 offset:2304
ds_read_b128 a[20:23], v236 offset:4608
ds_read_b128 a[36:39], v237 offset:0
ds_read_b128 a[40:43], v237 offset:2304
ds_read_b128 a[44:47], v237 offset:4608
ds_read_b128 a[144:147], v252 offset:0
v_pk_mul_f32 v[158:159], v[0:1], v[0:1]
v_pk_add_f32 v[156:157], v[0:1], 0 op_sel_hi:[1,0]
v_pk_mul_f32 v[162:163], v[48:49], v[48:49]
v_pk_add_f32 v[160:161], v[48:49], 0 op_sel_hi:[1,0]
v_pk_mul_f32 v[166:167], v[96:97], v[96:97]
v_pk_add_f32 v[164:165], v[96:97], 0 op_sel_hi:[1,0]
v_pk_fma_f32 v[158:159], v[2:3], v[2:3], v[158:159]
v_pk_add_f32 v[156:157], v[2:3], v[156:157]
v_pk_fma_f32 v[162:163], v[50:51], v[50:51], v[162:163]
v_pk_add_f32 v[160:161], v[50:51], v[160:161]
v_pk_fma_f32 v[166:167], v[98:99], v[98:99], v[166:167]
v_pk_add_f32 v[164:165], v[98:99], v[164:165]
v_pk_fma_f32 v[158:159], v[4:5], v[4:5], v[158:159]
v_pk_add_f32 v[156:157], v[4:5], v[156:157]
v_pk_fma_f32 v[162:163], v[52:53], v[52:53], v[162:163]
v_pk_add_f32 v[160:161], v[52:53], v[160:161]
v_pk_fma_f32 v[166:167], v[100:101], v[100:101], v[166:167]
v_pk_add_f32 v[164:165], v[100:101], v[164:165]
v_pk_fma_f32 v[158:159], v[6:7], v[6:7], v[158:159]
v_pk_add_f32 v[156:157], v[6:7], v[156:157]
v_pk_fma_f32 v[162:163], v[54:55], v[54:55], v[162:163]
v_pk_add_f32 v[160:161], v[54:55], v[160:161]
v_pk_fma_f32 v[166:167], v[102:103], v[102:103], v[166:167]
v_pk_add_f32 v[164:165], v[102:103], v[164:165]
v_pk_fma_f32 v[158:159], v[8:9], v[8:9], v[158:159]
v_pk_add_f32 v[156:157], v[8:9], v[156:157]
v_pk_fma_f32 v[162:163], v[56:57], v[56:57], v[162:163]
v_pk_add_f32 v[160:161], v[56:57], v[160:161]
v_pk_fma_f32 v[166:167], v[104:105], v[104:105], v[166:167]
v_pk_add_f32 v[164:165], v[104:105], v[164:165]
v_pk_fma_f32 v[158:159], v[10:11], v[10:11], v[158:159]
v_pk_add_f32 v[156:157], v[10:11], v[156:157]
v_pk_fma_f32 v[162:163], v[58:59], v[58:59], v[162:163]
v_pk_add_f32 v[160:161], v[58:59], v[160:161]
v_pk_fma_f32 v[166:167], v[106:107], v[106:107], v[166:167]
v_pk_add_f32 v[164:165], v[106:107], v[164:165]
v_pk_fma_f32 v[158:159], v[12:13], v[12:13], v[158:159]
v_pk_add_f32 v[156:157], v[12:13], v[156:157]
v_pk_fma_f32 v[162:163], v[60:61], v[60:61], v[162:163]
v_pk_add_f32 v[160:161], v[60:61], v[160:161]
v_pk_fma_f32 v[166:167], v[108:109], v[108:109], v[166:167]
v_pk_add_f32 v[164:165], v[108:109], v[164:165]
v_pk_fma_f32 v[158:159], v[14:15], v[14:15], v[158:159]
v_pk_add_f32 v[156:157], v[14:15], v[156:157]
v_pk_fma_f32 v[162:163], v[62:63], v[62:63], v[162:163]
v_pk_add_f32 v[160:161], v[62:63], v[160:161]
v_pk_fma_f32 v[166:167], v[110:111], v[110:111], v[166:167]
v_pk_add_f32 v[164:165], v[110:111], v[164:165]
v_pk_mul_f32 v[246:247], v[144:145], v[144:145]
v_pk_add_f32 v[244:245], v[144:145], 0 op_sel_hi:[1,0]
v_pk_fma_f32 v[246:247], v[146:147], v[146:147], v[246:247]
v_pk_add_f32 v[244:245], v[146:147], v[244:245]
v_and_b32 v70, v218, v214
v_and_b32 v71, v219, v214
s_waitcnt vmcnt(9) lgkmcnt(0)
s_barrier
ds_read_b128 a[196:199], v232 offset:34816
ds_read_b128 a[200:203], v232 offset:37120
ds_read_b128 a[204:207], v232 offset:39424
ds_read_b128 a[208:211], v233 offset:34816
ds_read_b128 a[212:215], v234 offset:39424
s_mov_b32 s30, 589824
v_mov_b32 v251, s43
buffer_store_dword v251, v250, s[12:15], s30 offen sc1
s_waitcnt lgkmcnt(4)
v_mfma_f32_16x16x32_bf16 v[148:151], v[16:19], a[196:199], 0
s_waitcnt lgkmcnt(3)
ds_read_b128 a[156:159], v220 offset:34816
ds_read_b128 a[160:163], v220 offset:37120
v_mfma_f32_16x16x32_bf16 v[148:151], v[24:27], a[200:203], v[148:151]
s_waitcnt lgkmcnt(4)
ds_read_b128 a[164:167], v220 offset:39424
ds_read_b128 a[168:171], v224 offset:34816
v_mfma_f32_16x16x32_bf16 v[148:151], v[72:75], a[204:207], v[148:151]
s_waitcnt lgkmcnt(5)
s_add_u32 m0, s23, 104448
s_mov_b32 s26, 0
ds_read_b128 a[172:175], v224 offset:39424
ds_read_b128 a[176:179], v220 offset:41728
v_mfma_f32_16x16x32_bf16 v[148:151], v[120:123], a[208:211], v[148:151]
s_waitcnt lgkmcnt(6)
buffer_load_dwordx4 v255, s[4:7], s26 offen lds
ds_read_b128 a[180:183], v224 offset:37120
ds_read_b128 a[184:187], v224 offset:41728
v_mfma_f32_16x16x32_bf16 v[148:151], v[76:79], a[212:215], v[148:151]
s_mul_i32 s38, s20, 3
ds_read_b128 a[188:191], v220 offset:44032
ds_read_b128 a[192:195], v228 offset:44032
s_waitcnt lgkmcnt(9)
v_mfma_f32_16x16x32_bf16 v[16:19], v[124:127], a[156:159], 0
s_waitcnt lgkmcnt(8)
ds_read_b128 a[196:199], v221 offset:34816
v_and_b32 v116, v216, v215
v_mfma_f32_16x16x32_bf16 v[16:19], v[20:23], a[160:163], v[16:19]
s_waitcnt lgkmcnt(7)
s_add_u32 m0, s23, 105472
s_mov_b32 s26, 12288
ds_read_b128 a[200:203], v221 offset:37120
v_and_b32_e64 v117, v217, v215
v_mfma_f32_16x16x32_bf16 v[16:19], v[68:71], a[164:167], v[16:19]
buffer_load_dwordx4 v255, s[4:7], s26 offen lds
v_and_b32 v118, v218, v215
v_and_b32 v119, v219, v215
s_nop 1
v_mfma_f32_16x16x32_bf16 v[16:19], v[116:119], a[168:171], v[16:19]
s_waitcnt lgkmcnt(4)
ds_read_b128 a[204:207], v221 offset:39424
ds_read_b128 v[212:215], v235 offset:112
v_and_b32 v24, v216, v208
v_and_b32 v25, v217, v208
v_and_b32 v26, v218, v208
v_and_b32 v27, v219, v208
s_nop 1
v_mfma_f32_16x16x32_bf16 v[16:19], v[24:27], a[172:175], v[16:19]
s_add_u32 s38, s38, s24
ds_read_b128 a[208:211], v225 offset:34816
v_and_b32_e64 v72, v216, v209
v_mfma_f32_16x16x32_bf16 v[64:67], v[124:127], a[160:163], 0
s_add_u32 m0, s23, 106496
s_mov_b32 s26, 24576
v_and_b32 v73, v217, v209
v_and_b32 v74, v218, v209
v_mfma_f32_16x16x32_bf16 v[64:67], v[20:23], a[164:167], v[64:67]
buffer_load_dwordx4 v255, s[4:7], s26 offen lds
ds_read_b128 a[212:215], v225 offset:39424
v_and_b32_e64 v75, v219, v209
v_mfma_f32_16x16x32_bf16 v[64:67], v[68:71], a[176:179], v[64:67]
s_mul_i32 s38, s38, 224
ds_read_b128 a[216:219], v221 offset:41728
v_and_b32_e64 v120, v216, v210
v_mfma_f32_16x16x32_bf16 v[64:67], v[116:119], a[180:183], v[64:67]
s_add_u32 s38, s38, s25
v_and_b32 v121, v217, v210
v_and_b32_e64 v122, v218, v210
v_mfma_f32_16x16x32_bf16 v[64:67], v[24:27], a[184:187], v[64:67]
s_add_u32 m0, s23, 107520
s_mov_b32 s26, 36864
ds_read_b128 a[220:223], v225 offset:37120
v_and_b32_e64 v123, v219, v210
v_mfma_f32_16x16x32_bf16 v[112:115], v[124:127], a[164:167], 0
buffer_load_dwordx4 v255, s[4:7], s26 offen lds
ds_read_b128 a[224:227], v225 offset:41728
v_and_b32_e64 v28, v216, v211
v_mfma_f32_16x16x32_bf16 v[112:115], v[20:23], a[176:179], v[112:115]
s_waitcnt lgkmcnt(3)
v_and_b32 v29, v217, v211
v_and_b32_e64 v30, v218, v211
v_mfma_f32_16x16x32_bf16 v[112:115], v[68:71], a[188:191], v[112:115]
s_mul_i32 s44, s38, 384
ds_read_b128 a[228:231], v221 offset:44032
v_and_b32_e64 v31, v219, v211
v_mfma_f32_16x16x32_bf16 v[112:115], v[116:119], a[172:175], v[112:115]
s_add_u32 m0, s23, 108544
s_mov_b32 s26, 49152
ds_read_b128 a[232:235], v229 offset:44032
ds_read_b128 v[208:211], v235 offset:128
v_and_b32_e64 v76, v216, v212
v_mfma_f32_16x16x32_bf16 v[112:115], v[24:27], a[192:195], v[112:115]
buffer_load_dwordx4 v255, s[4:7], s26 offen lds
v_and_b32 v77, v217, v212
v_and_b32 v78, v218, v212
v_mfma_f32_16x16x32_bf16 v[20:23], v[72:75], a[196:199], 0
s_add_u32 s45, s44, 86016
ds_read_b128 a[156:159], v222 offset:34816
v_and_b32_e64 v79, v219, v212
v_mfma_f32_16x16x32_bf16 v[20:23], v[120:123], a[200:203], v[20:23]
s_add_u32 s46, s44, 172032
ds_read_b128 a[160:163], v222 offset:37120
v_and_b32_e64 v24, v216, v213
v_mfma_f32_16x16x32_bf16 v[20:23], v[28:31], a[204:207], v[20:23]
s_add_u32 m0, s23, 109568
s_mov_b32 s26, 61440
v_and_b32 v25, v217, v213
v_and_b32 v26, v218, v213
v_mfma_f32_16x16x32_bf16 v[20:23], v[76:79], a[208:211], v[20:23]
buffer_load_dwordx4 v255, s[4:7], s26 offen lds
ds_read_b128 a[164:167], v222 offset:39424
v_and_b32 v27, v219, v213
s_nop 1
v_mfma_f32_16x16x32_bf16 v[20:23], v[24:27], a[212:215], v[20:23]
s_add_u32 s38, s24, 12
ds_read_b128 a[168:171], v226 offset:34816
v_and_b32 v124, v216, v214
v_mfma_f32_16x16x32_bf16 v[68:71], v[72:75], a[200:203], 0
s_mul_i32 s38, s38, 224
v_and_b32 v125, v217, v214
v_and_b32 v126, v218, v214
v_mfma_f32_16x16x32_bf16 v[68:71], v[120:123], a[204:207], v[68:71]
s_waitcnt lgkmcnt(2)
ds_read_b128 a[172:175], v226 offset:39424
v_and_b32 v127, v219, v214
v_mfma_f32_16x16x32_bf16 v[68:71], v[28:31], a[216:219], v[68:71]
ds_read_b128 a[176:179], v222 offset:41728
v_and_b32_e64 v36, v216, v215
v_mfma_f32_16x16x32_bf16 v[68:71], v[76:79], a[220:223], v[68:71]
s_add_u32 s38, s38, s25
v_and_b32 v37, v217, v215
v_and_b32_e64 v38, v218, v215
v_mfma_f32_16x16x32_bf16 v[68:71], v[24:27], a[224:227], v[68:71]
s_mul_i32 s47, s38, 384
ds_read_b128 a[180:183], v226 offset:37120
v_and_b32_e64 v39, v219, v215
v_mfma_f32_16x16x32_bf16 v[116:119], v[72:75], a[204:207], 0
ds_read_b128 a[184:187], v226 offset:41728
ds_read_b128 v[212:215], v235 offset:144
v_and_b32_e64 v84, v216, v208
v_mfma_f32_16x16x32_bf16 v[116:119], v[120:123], a[216:219], v[116:119]
v_and_b32 v85, v217, v208
v_and_b32 v86, v218, v208
v_mfma_f32_16x16x32_bf16 v[116:119], v[28:31], a[228:231], v[116:119]
s_lshl_b32 s38, s20, 7
ds_read_b128 a[188:191], v222 offset:44032
v_and_b32 v87, v219, v208
v_mfma_f32_16x16x32_bf16 v[116:119], v[76:79], a[212:215], v[116:119]
s_add_u32 s47, s47, s38
ds_read_b128 a[192:195], v230 offset:44032
v_and_b32 v28, v216, v209
v_mfma_f32_16x16x32_bf16 v[116:119], v[24:27], a[232:235], v[116:119]
v_and_b32 v29, v217, v209
v_and_b32 v30, v218, v209
v_mfma_f32_16x16x32_bf16 v[24:27], v[124:127], a[156:159], 0
ds_read_b128 a[196:199], v223 offset:34816
v_and_b32 v31, v219, v209
v_mfma_f32_16x16x32_bf16 v[24:27], v[36:39], a[160:163], v[24:27]
s_waitcnt lgkmcnt(3)
ds_read_b128 a[200:203], v223 offset:37120
v_and_b32_e64 v76, v216, v210
v_mfma_f32_16x16x32_bf16 v[24:27], v[84:87], a[164:167], v[24:27]
s_add_u32 s38, s20, 1
v_and_b32 v77, v217, v210
v_and_b32_e64 v78, v218, v210
v_mfma_f32_16x16x32_bf16 v[24:27], v[28:31], a[168:171], v[24:27]
ds_read_b128 a[204:207], v223 offset:39424
v_and_b32 v79, v219, v210
s_nop 1
v_mfma_f32_16x16x32_bf16 v[24:27], v[76:79], a[172:175], v[24:27]
ds_read_b128 a[208:211], v227 offset:34816
v_and_b32_e64 v132, v216, v211
v_mfma_f32_16x16x32_bf16 v[72:75], v[124:127], a[160:163], 0
s_lshr_b32 s38, s38, 2
v_and_b32 v133, v217, v211
v_and_b32_e64 v134, v218, v211
v_mfma_f32_16x16x32_bf16 v[72:75], v[36:39], a[164:167], v[72:75]
s_mul_i32 s38, s38, 0x70000000
ds_read_b128 a[212:215], v227 offset:39424
v_and_b32_e64 v135, v219, v211
v_mfma_f32_16x16x32_bf16 v[72:75], v[84:87], a[176:179], v[72:75]
ds_read_b128 a[216:219], v223 offset:41728
ds_read_b128 v[208:211], v235 offset:160
v_and_b32_e64 v40, v216, v212
v_mfma_f32_16x16x32_bf16 v[72:75], v[28:31], a[180:183], v[72:75]
v_and_b32 v41, v217, v212
v_and_b32 v42, v218, v212
v_mfma_f32_16x16x32_bf16 v[72:75], v[76:79], a[184:187], v[72:75]
s_add_u32 s47, s47, s38
ds_read_b128 a[220:223], v227 offset:37120
v_and_b32 v43, v219, v212
v_mfma_f32_16x16x32_bf16 v[120:123], v[124:127], a[164:167], 0
ds_read_b128 a[224:227], v227 offset:41728
v_and_b32_e64 v88, v216, v213
v_mfma_f32_16x16x32_bf16 v[120:123], v[36:39], a[176:179], v[120:123]
s_waitcnt lgkmcnt(4)
v_and_b32 v89, v217, v213
v_and_b32_e64 v90, v218, v213
v_mfma_f32_16x16x32_bf16 v[120:123], v[84:87], a[188:191], v[120:123]
ds_read_b128 a[228:231], v223 offset:44032
v_and_b32_e64 v91, v219, v213
v_mfma_f32_16x16x32_bf16 v[120:123], v[28:31], a[172:175], v[120:123]
ds_read_b128 a[232:235], v231 offset:44032
v_and_b32_e64 v36, v216, v214
v_mfma_f32_16x16x32_bf16 v[120:123], v[76:79], a[192:195], v[120:123]
v_and_b32 v37, v217, v214
v_and_b32 v38, v218, v214
v_mfma_f32_16x16x32_bf16 v[28:31], v[132:135], a[196:199], 0
v_and_b32 v39, v219, v214
v_and_b32 v84, v216, v215
v_mfma_f32_16x16x32_bf16 v[28:31], v[40:43], a[200:203], v[28:31]
v_and_b32 v85, v217, v215
v_and_b32 v86, v218, v215
v_mfma_f32_16x16x32_bf16 v[28:31], v[88:91], a[204:207], v[28:31]
v_and_b32 v87, v219, v215
v_and_b32 v136, v216, v238
v_mfma_f32_16x16x32_bf16 v[28:31], v[36:39], a[208:211], v[28:31]
v_and_b32 v137, v217, v238
v_and_b32 v138, v218, v238
v_mfma_f32_16x16x32_bf16 v[28:31], v[84:87], a[212:215], v[28:31]
v_and_b32 v139, v219, v238
v_and_b32 v44, v216, v239
v_mfma_f32_16x16x32_bf16 v[76:79], v[132:135], a[200:203], 0
v_and_b32 v45, v217, v239
v_and_b32 v46, v218, v239
v_mfma_f32_16x16x32_bf16 v[76:79], v[40:43], a[204:207], v[76:79]
s_waitcnt lgkmcnt(0)
v_and_b32 v47, v219, v239
v_and_b32_e64 v92, v216, v240
v_mfma_f32_16x16x32_bf16 v[76:79], v[88:91], a[216:219], v[76:79]
v_and_b32 v93, v217, v240
v_and_b32 v94, v218, v240
v_mfma_f32_16x16x32_bf16 v[76:79], v[36:39], a[220:223], v[76:79]
v_and_b32 v95, v219, v240
v_and_b32 v140, v216, v241
v_mfma_f32_16x16x32_bf16 v[76:79], v[84:87], a[224:227], v[76:79]
v_and_b32 v141, v217, v241
v_and_b32 v142, v218, v241
v_mfma_f32_16x16x32_bf16 v[124:127], v[132:135], a[204:207], 0
v_and_b32 v143, v219, v241
v_and_b32 v132, v216, v242
v_mfma_f32_16x16x32_bf16 v[124:127], v[40:43], a[216:219], v[124:127]
v_and_b32 v133, v217, v242
v_and_b32 v134, v218, v242
v_mfma_f32_16x16x32_bf16 v[124:127], v[88:91], a[228:231], v[124:127]
v_and_b32_e64 v135, v219, v242
ds_read_b128 v[212:215], v235 offset:176
v_and_b32_e64 v40, v216, v208
v_mfma_f32_16x16x32_bf16 v[124:127], v[36:39], a[212:215], v[124:127]
v_and_b32 v41, v217, v208
v_and_b32 v42, v218, v208
v_mfma_f32_16x16x32_bf16 v[124:127], v[84:87], a[232:235], v[124:127]
v_and_b32 v43, v219, v208
v_and_b32 v36, v216, v209
ds_read_b128 a[60:63], v236 offset:34816
ds_read_b128 a[64:67], v236 offset:37120
ds_read_b128 a[68:71], v236 offset:39424
ds_read_b128 a[84:87], v237 offset:34816
ds_read_b128 a[88:91], v237 offset:37120
ds_read_b128 a[92:95], v237 offset:39424
ds_read_b128 a[148:151], v252 offset:34816
v_pk_fma_f32 v[158:159], v[16:17], v[16:17], v[158:159]
v_pk_add_f32 v[156:157], v[16:17], v[156:157]
v_pk_fma_f32 v[162:163], v[64:65], v[64:65], v[162:163]
v_pk_add_f32 v[160:161], v[64:65], v[160:161]
v_pk_fma_f32 v[166:167], v[112:113], v[112:113], v[166:167]
v_pk_add_f32 v[164:165], v[112:113], v[164:165]
v_pk_fma_f32 v[158:159], v[18:19], v[18:19], v[158:159]
v_pk_add_f32 v[156:157], v[18:19], v[156:157]
v_pk_fma_f32 v[162:163], v[66:67], v[66:67], v[162:163]
v_pk_add_f32 v[160:161], v[66:67], v[160:161]
v_pk_fma_f32 v[166:167], v[114:115], v[114:115], v[166:167]
v_pk_add_f32 v[164:165], v[114:115], v[164:165]
v_pk_fma_f32 v[158:159], v[20:21], v[20:21], v[158:159]
v_pk_add_f32 v[156:157], v[20:21], v[156:157]
v_pk_fma_f32 v[162:163], v[68:69], v[68:69], v[162:163]
v_pk_add_f32 v[160:161], v[68:69], v[160:161]
v_pk_fma_f32 v[166:167], v[116:117], v[116:117], v[166:167]
v_pk_add_f32 v[164:165], v[116:117], v[164:165]
v_pk_fma_f32 v[158:159], v[22:23], v[22:23], v[158:159]
v_pk_add_f32 v[156:157], v[22:23], v[156:157]
v_pk_fma_f32 v[162:163], v[70:71], v[70:71], v[162:163]
v_pk_add_f32 v[160:161], v[70:71], v[160:161]
v_pk_fma_f32 v[166:167], v[118:119], v[118:119], v[166:167]
v_pk_add_f32 v[164:165], v[118:119], v[164:165]
v_pk_fma_f32 v[158:159], v[24:25], v[24:25], v[158:159]
v_pk_add_f32 v[156:157], v[24:25], v[156:157]
v_pk_fma_f32 v[162:163], v[72:73], v[72:73], v[162:163]
v_pk_add_f32 v[160:161], v[72:73], v[160:161]
v_pk_fma_f32 v[166:167], v[120:121], v[120:121], v[166:167]
v_pk_add_f32 v[164:165], v[120:121], v[164:165]
v_pk_fma_f32 v[158:159], v[26:27], v[26:27], v[158:159]
v_pk_add_f32 v[156:157], v[26:27], v[156:157]
v_pk_fma_f32 v[162:163], v[74:75], v[74:75], v[162:163]
v_pk_add_f32 v[160:161], v[74:75], v[160:161]
v_pk_fma_f32 v[166:167], v[122:123], v[122:123], v[166:167]
v_pk_add_f32 v[164:165], v[122:123], v[164:165]
v_pk_fma_f32 v[158:159], v[28:29], v[28:29], v[158:159]
v_pk_add_f32 v[156:157], v[28:29], v[156:157]
v_pk_fma_f32 v[162:163], v[76:77], v[76:77], v[162:163]
v_pk_add_f32 v[160:161], v[76:77], v[160:161]
v_pk_fma_f32 v[166:167], v[124:125], v[124:125], v[166:167]
v_pk_add_f32 v[164:165], v[124:125], v[164:165]
v_pk_fma_f32 v[158:159], v[30:31], v[30:31], v[158:159]
v_pk_add_f32 v[156:157], v[30:31], v[156:157]
v_pk_fma_f32 v[162:163], v[78:79], v[78:79], v[162:163]
v_pk_add_f32 v[160:161], v[78:79], v[160:161]
v_pk_fma_f32 v[166:167], v[126:127], v[126:127], v[166:167]
v_pk_add_f32 v[164:165], v[126:127], v[164:165]
v_pk_fma_f32 v[246:247], v[148:149], v[148:149], v[246:247]
v_pk_add_f32 v[244:245], v[148:149], v[244:245]
v_pk_fma_f32 v[246:247], v[150:151], v[150:151], v[246:247]
v_pk_add_f32 v[244:245], v[150:151], v[244:245]
v_and_b32 v37, v217, v209
v_and_b32 v38, v218, v209
v_and_b32 v39, v219, v209
v_subrev_u32 v238, s23, v248
s_lshl_b32 s31, s20, 6
v_lshrrev_b32 v242, 6, v238
v_bfe_u32 v243, v238, 5, 1
v_and_b32 v255, 6, v242
v_lshrrev_b32 v241, 4, v238
v_or_b32 v255, v255, v243
v_and_b32 v241, 48, v241
v_lshlrev_b32_e64 v255, 4, v255
v_add_u32 v241, 153600, v241
v_lshl_add_u32 v255, v242, 7, v255
v_add_u32_e64 v242, s31, v241
s_lshl_b32 s31, s20, 11
s_lshr_b32 s30, s20, 1
v_bfe_u32 v243, v238, 4, 1
v_add_u32_e64 v249, s31, v238
s_lshl_b32 s30, s30, 10
s_and_b32 s31, s20, 1
v_lshl_add_u32 v255, v243, 3, v255
s_lshl_b32 s31, s31, 3
s_add_u32 s30, s30, s31
v_add_u32 v239, 104448, v238
v_add_u32 v255, 104448, v255
v_add_u32 v243, 104448, v248
v_add_u32 v240, s30, v238
s_lshl_b32 s27, s20, 5
s_mov_b32 s33, 0x3727c5ac
v_add_u32 v220, 69632, v220
v_add_u32 v221, 69632, v221
v_add_u32 v222, 69632, v222
v_add_u32 v223, 69632, v223
v_add_u32 v224, 69632, v224
v_add_u32 v225, 69632, v225
v_add_u32 v226, 69632, v226
v_add_u32 v227, 69632, v227
v_add_u32 v228, 69632, v228
v_add_u32 v229, 69632, v229
v_add_u32 v230, 69632, v230
v_add_u32 v231, 69632, v231
v_add_u32 v236, 69632, v236
v_add_u32 v237, 69632, v237
v_add_u32 v232, 69632, v232
v_add_u32 v233, 69632, v233
v_add_u32 v234, 69632, v234
v_add_u32 v252, 69632, v252
s_waitcnt vmcnt(7) lgkmcnt(0)
s_barrier
ds_read_b128 a[156:159], v232 offset:0
ds_read_b128 a[160:163], v232 offset:2304
ds_read_b128 a[164:167], v232 offset:4608
ds_read_b128 a[168:171], v233 offset:0
ds_read_b128 a[172:175], v234 offset:4608
s_waitcnt lgkmcnt(4)
v_mfma_f32_16x16x32_bf16 v[152:155], v[136:139], a[156:159], 0
s_waitcnt lgkmcnt(3)
ds_read_b128 a[196:199], v220 offset:0
ds_read_b128 a[200:203], v220 offset:2304
v_and_b32 v84, v216, v210
v_mfma_f32_16x16x32_bf16 v[152:155], v[44:47], a[160:163], v[152:155]
s_waitcnt lgkmcnt(4)
ds_read_b128 a[204:207], v220 offset:4608
ds_read_b128 a[208:211], v224 offset:0
v_and_b32 v85, v217, v210
v_mfma_f32_16x16x32_bf16 v[152:155], v[92:95], a[164:167], v[152:155]
s_waitcnt lgkmcnt(5)
ds_read_b128 a[212:215], v224 offset:4608
ds_read_b128 a[216:219], v220 offset:6912
v_and_b32 v86, v218, v210
v_mfma_f32_16x16x32_bf16 v[152:155], v[140:143], a[168:171], v[152:155]
s_waitcnt lgkmcnt(6)
ds_read_b128 a[220:223], v224 offset:2304
ds_read_b128 a[224:227], v224 offset:6912
v_and_b32_e64 v87, v219, v210
v_mfma_f32_16x16x32_bf16 v[152:155], v[132:135], a[172:175], v[152:155]
ds_read_b128 a[228:231], v220 offset:9216
ds_read_b128 a[232:235], v228 offset:9216
v_and_b32_e64 v132, v216, v211
s_waitcnt lgkmcnt(9)
v_mfma_f32_16x16x32_bf16 v[32:35], v[40:43], a[196:199], 0
s_waitcnt lgkmcnt(8)
ds_read_b128 a[156:159], v221 offset:0
v_and_b32 v133, v217, v211
v_mfma_f32_16x16x32_bf16 v[32:35], v[36:39], a[200:203], v[32:35]
s_waitcnt lgkmcnt(7)
ds_read_b128 a[160:163], v221 offset:2304
v_fma_f32 v246, v152, v152, v246
v_add_f32 v244, v152, v244
v_and_b32 v134, v218, v211
v_mfma_f32_16x16x32_bf16 v[32:35], v[84:87], a[204:207], v[32:35]
v_fma_f32 v247, v153, v153, v247
v_add_f32 v245, v153, v245
v_and_b32 v135, v219, v211
s_nop 1
v_mfma_f32_16x16x32_bf16 v[32:35], v[132:135], a[208:211], v[32:35]
s_waitcnt lgkmcnt(4)
ds_read_b128 a[164:167], v221 offset:4608
v_fma_f32 v246, v154, v154, v246
v_add_f32_e64 v244, v154, v244
ds_read_b128 v[208:211], v235 offset:192
v_and_b32 v88, v216, v212
v_and_b32 v89, v217, v212
v_and_b32_e64 v90, v218, v212
v_and_b32 v91, v219, v212
s_nop 1
v_mfma_f32_16x16x32_bf16 v[32:35], v[88:91], a[212:215], v[32:35]
ds_read_b128 a[168:171], v225 offset:0
v_fma_f32 v247, v155, v155, v247
v_add_f32 v245, v155, v245
v_and_b32 v136, v216, v213
v_mfma_f32_16x16x32_bf16 v[80:83], v[40:43], a[200:203], 0
v_add_f32 v244, v244, v245
v_add_f32 v246, v246, v247
v_and_b32_e64 v137, v217, v213
v_mfma_f32_16x16x32_bf16 v[80:83], v[36:39], a[204:207], v[80:83]
ds_read_b128 a[172:175], v225 offset:4608
v_permlane32_swap_b32 v244, v246
v_and_b32 v138, v218, v213
v_mfma_f32_16x16x32_bf16 v[80:83], v[84:87], a[216:219], v[80:83]
ds_read_b128 a[176:179], v221 offset:6912
v_add_f32 v244, v244, v246
v_and_b32 v139, v219, v213
v_mfma_f32_16x16x32_bf16 v[80:83], v[132:135], a[220:223], v[80:83]
v_mov_b32 v245, v244
v_and_b32 v44, v216, v214
v_mfma_f32_16x16x32_bf16 v[80:83], v[88:91], a[224:227], v[80:83]
ds_read_b128 a[180:183], v225 offset:2304
v_permlane16_swap_b32 v244, v245
v_and_b32 v45, v217, v214
v_mfma_f32_16x16x32_bf16 v[128:131], v[40:43], a[204:207], 0
ds_read_b128 a[184:187], v225 offset:6912
v_add_f32 v244, v244, v245
v_and_b32 v46, v218, v214
v_mfma_f32_16x16x32_bf16 v[128:131], v[36:39], a[216:219], v[128:131]
s_waitcnt lgkmcnt(3)
v_mov_b32 v245, v244
v_and_b32_e64 v47, v219, v214
v_mfma_f32_16x16x32_bf16 v[128:131], v[84:87], a[228:231], v[128:131]
ds_read_b128 a[188:191], v221 offset:9216
v_permlane32_swap_b32 v244, v245
v_and_b32 v40, v216, v215
v_mfma_f32_16x16x32_bf16 v[128:131], v[132:135], a[212:215], v[128:131]
ds_read_b128 a[192:195], v229 offset:9216
v_and_b32_e64 v41, v217, v215
v_mfma_f32_16x16x32_bf16 v[128:131], v[88:91], a[232:235], v[128:131]
ds_write_b64 v240, v[244:245] offset:0
v_and_b32_e64 v42, v218, v215
v_mfma_f32_16x16x32_bf16 v[36:39], v[136:139], a[156:159], 0
ds_read_b128 a[196:199], v222 offset:0
v_and_b32_e64 v43, v219, v215
v_mfma_f32_16x16x32_bf16 v[36:39], v[44:47], a[160:163], v[36:39]
ds_read_b128 a[200:203], v222 offset:2304
ds_read_b128 v[212:215], v235 offset:208
v_and_b32_e64 v88, v216, v208
v_mfma_f32_16x16x32_bf16 v[36:39], v[40:43], a[164:167], v[36:39]
v_and_b32 v89, v217, v208
v_and_b32 v90, v218, v208
v_and_b32 v91, v219, v208
s_nop 1
v_mfma_f32_16x16x32_bf16 v[36:39], v[88:91], a[168:171], v[36:39]
ds_read_b128 a[204:207], v222 offset:4608
v_and_b32 v92, v216, v209
v_and_b32 v93, v217, v209
v_and_b32_e64 v94, v218, v209
v_and_b32 v95, v219, v209
s_nop 1
v_mfma_f32_16x16x32_bf16 v[36:39], v[92:95], a[172:175], v[36:39]
ds_read_b128 a[208:211], v226 offset:0
v_and_b32_e64 v140, v216, v210
v_mfma_f32_16x16x32_bf16 v[84:87], v[136:139], a[160:163], 0
s_waitcnt vmcnt(0) lgkmcnt(5)
s_barrier
v_and_b32_e64 v141, v217, v210
v_mfma_f32_16x16x32_bf16 v[84:87], v[44:47], a[164:167], v[84:87]
ds_read_b128 a[212:215], v226 offset:4608
ds_read_b128 v[244:247], v238 offset:0
v_and_b32_e64 v142, v218, v210
v_mfma_f32_16x16x32_bf16 v[84:87], v[40:43], a[176:179], v[84:87]
ds_read_b128 a[216:219], v222 offset:6912
ds_read_b64 v[250:251], v238 offset:1024
v_and_b32_e64 v143, v219, v210
v_mfma_f32_16x16x32_bf16 v[84:87], v[88:91], a[180:183], v[84:87]
ds_read_b64_tr_b16 a[96:97], v255 offset:0
v_and_b32_e64 v168, v216, v211
v_mfma_f32_16x16x32_bf16 v[84:87], v[92:95], a[184:187], v[84:87]
ds_read_b128 a[220:223], v226 offset:2304
v_and_b32_e64 v169, v217, v211
v_mfma_f32_16x16x32_bf16 v[132:135], v[136:139], a[164:167], 0
ds_read_b128 a[224:227], v226 offset:6912
v_and_b32_e64 v170, v218, v211
v_mfma_f32_16x16x32_bf16 v[132:135], v[44:47], a[176:179], v[132:135]
ds_read_b64_tr_b16 a[98:99], v255 offset:2048
v_and_b32_e64 v171, v219, v211
v_mfma_f32_16x16x32_bf16 v[132:135], v[40:43], a[188:191], v[132:135]
ds_read_b128 a[228:231], v222 offset:9216
s_waitcnt lgkmcnt(7)
v_add_f32 v244, v244, v246
v_add_f32_e64 v245, v245, v247
ds_read_b128 v[208:211], v235 offset:224
v_and_b32_e64 v44, v216, v212
v_mfma_f32_16x16x32_bf16 v[132:135], v[88:91], a[172:175], v[132:135]
ds_read_b128 a[232:235], v230 offset:9216
ds_read_b64 v[246:247], v238 offset:1032
s_waitcnt lgkmcnt(8)
v_add_f32 v244, v244, v250
v_add_f32 v245, v245, v251
v_and_b32 v45, v217, v212
v_mfma_f32_16x16x32_bf16 v[132:135], v[92:95], a[192:195], v[132:135]
ds_read_b64_tr_b16 a[100:101], v255 offset:4096
v_and_b32_e64 v46, v218, v212
v_mfma_f32_16x16x32_bf16 v[40:43], v[140:143], a[196:199], 0
ds_read_b128 a[156:159], v223 offset:0
v_and_b32_e64 v47, v219, v212
v_mfma_f32_16x16x32_bf16 v[40:43], v[168:171], a[200:203], v[40:43]
ds_read_b128 a[160:163], v223 offset:2304
v_and_b32_e64 v92, v216, v213
v_mfma_f32_16x16x32_bf16 v[40:43], v[44:47], a[204:207], v[40:43]
ds_read_b64_tr_b16 a[102:103], v255 offset:6144
s_waitcnt lgkmcnt(4)
v_add_f32 v244, v244, v246
v_add_f32 v245, v245, v247
v_and_b32 v93, v217, v213
v_and_b32_e64 v94, v218, v213
v_and_b32 v95, v219, v213
s_nop 1
v_mfma_f32_16x16x32_bf16 v[40:43], v[92:95], a[208:211], v[40:43]
ds_read_b128 a[164:167], v223 offset:4608
v_mul_f32 v247, 0xbbaaaaab, v244
v_and_b32 v172, v216, v214
v_and_b32 v173, v217, v214
v_and_b32_e64 v174, v218, v214
v_and_b32 v175, v219, v214
s_nop 1
v_mfma_f32_16x16x32_bf16 v[40:43], v[172:175], a[212:215], v[40:43]
ds_read_b128 a[168:171], v227 offset:0
v_mul_f32 v246, 0x3baaaaab, v245
v_and_b32_e64 v176, v216, v215
v_mfma_f32_16x16x32_bf16 v[88:91], v[140:143], a[200:203], 0
ds_read_b64_tr_b16 a[104:105], v255 offset:8192
v_fma_f32 v246, -v247, v247, v246
v_and_b32_e64 v177, v217, v215
v_mfma_f32_16x16x32_bf16 v[88:91], v[168:171], a[204:207], v[88:91]
ds_read_b128 a[172:175], v227 offset:4608
v_max_f32 v246, 0, v246
v_and_b32 v178, v218, v215
v_mfma_f32_16x16x32_bf16 v[88:91], v[44:47], a[216:219], v[88:91]
ds_read_b128 a[176:179], v223 offset:6912
v_add_f32 v246, s33, v246
v_and_b32 v179, v219, v215
v_mfma_f32_16x16x32_bf16 v[88:91], v[92:95], a[220:223], v[88:91]
ds_read_b64_tr_b16 a[106:107], v255 offset:10240
v_rsq_f32 v246, v246
v_and_b32 v180, v216, v208
v_mfma_f32_16x16x32_bf16 v[88:91], v[172:175], a[224:227], v[88:91]
ds_read_b128 a[180:183], v227 offset:2304
v_mul_f32 v244, 0x3eeff558, v246
v_and_b32_e64 v181, v217, v208
v_mfma_f32_16x16x32_bf16 v[136:139], v[140:143], a[204:207], 0
ds_read_b128 a[184:187], v227 offset:6912
v_mul_f32_e64 v245, v247, v244
v_fma_f32 v144, v144, v244, v245
v_and_b32_e64 v182, v218, v208
v_mfma_f32_16x16x32_bf16 v[136:139], v[168:171], a[216:219], v[136:139]
v_fma_f32 v145, v145, v244, v245
v_fma_f32 v146, v146, v244, v245
v_and_b32_e64 v183, v219, v208
v_mfma_f32_16x16x32_bf16 v[136:139], v[44:47], a[228:231], v[136:139]
ds_read_b128 a[188:191], v223 offset:9216
v_fma_f32 v147, v147, v244, v245
v_fma_f32 v148, v148, v244, v245
v_and_b32_e64 v184, v216, v209
v_mfma_f32_16x16x32_bf16 v[136:139], v[92:95], a[212:215], v[136:139]
ds_read_b128 a[192:195], v231 offset:9216
v_fma_f32 v149, v149, v244, v245
v_fma_f32 v150, v150, v244, v245
v_and_b32_e64 v185, v217, v209
v_mfma_f32_16x16x32_bf16 v[136:139], v[172:175], a[232:235], v[136:139]
v_fma_f32 v151, v151, v244, v245
v_fma_f32 v152, v152, v244, v245
v_and_b32_e64 v186, v218, v209
s_waitcnt lgkmcnt(12)
v_mfma_f32_16x16x32_bf16 v[44:47], v[176:179], a[156:159], 0
s_waitcnt lgkmcnt(2)
ds_read_b64_tr_b16 a[120:121], v255 offset:12288
v_fma_f32 v153, v153, v244, v245
v_fma_f32 v154, v154, v244, v245
v_and_b32_e64 v187, v219, v209
v_mfma_f32_16x16x32_bf16 v[44:47], v[180:183], a[160:163], v[44:47]
ds_read_b64_tr_b16 a[122:123], v255 offset:14336
v_fma_f32 v155, v155, v244, v245
v_cvt_pk_bf16_f32 v144, v144, v145
v_and_b32_e64 v188, v216, v210
v_mfma_f32_16x16x32_bf16 v[44:47], v[184:187], a[164:167], v[44:47]
ds_read_b64_tr_b16 a[124:125], v255 offset:16384
v_cvt_pk_bf16_f32 v145, v146, v147
v_cvt_pk_bf16_f32 v146, v148, v149
v_and_b32 v189, v217, v210
v_and_b32 v190, v218, v210
v_and_b32 v191, v219, v210
s_nop 1
v_mfma_f32_16x16x32_bf16 v[44:47], v[188:191], a[168:171], v[44:47]
ds_read_b64_tr_b16 a[126:127], v255 offset:18432
v_cvt_pk_bf16_f32 v147, v150, v151
v_cvt_pk_bf16_f32 v148, v152, v153
v_and_b32 v192, v216, v211
v_and_b32 v193, v217, v211
v_and_b32_e64 v194, v218, v211
v_and_b32 v195, v219, v211
s_nop 1
v_mfma_f32_16x16x32_bf16 v[44:47], v[192:195], a[172:175], v[44:47]
ds_read_b64_tr_b16 a[128:129], v255 offset:20480
v_cvt_pk_bf16_f32 v149, v154, v155
v_mfma_f32_16x16x32_bf16 v[92:95], v[176:179], a[160:163], 0
ds_write_b64 v240, v[144:145] offset:49152
v_xor_b32_e64 v209, 32, v255
v_mfma_f32_16x16x32_bf16 v[92:95], v[180:183], a[164:167], v[92:95]
ds_write_b64 v240, v[146:147] offset:51200
v_xor_b32_e64 v210, 64, v255
v_mfma_f32_16x16x32_bf16 v[92:95], v[184:187], a[176:179], v[92:95]
ds_write_b64 v240, v[148:149] offset:53248
ds_read_b64_tr_b16 a[130:131], v255 offset:22528
v_mfma_f32_16x16x32_bf16 v[92:95], v[188:191], a[180:183], v[92:95]
ds_read_b128 a[108:111], v236 offset:0
v_xor_b32 v211, 96, v255
v_mfma_f32_16x16x32_bf16 v[92:95], v[192:195], a[184:187], v[92:95]
ds_read_b128 a[112:115], v236 offset:2304
v_xor_b32_e64 v212, s27, v255
v_mfma_f32_16x16x32_bf16 v[140:143], v[176:179], a[164:167], 0
s_nop 0
ds_read_b128 a[116:119], v236 offset:4608
v_mfma_f32_16x16x32_bf16 v[140:143], v[180:183], a[176:179], v[140:143]
s_waitcnt lgkmcnt(5)
ds_read_b128 a[132:135], v237 offset:0
v_mfma_f32_16x16x32_bf16 v[140:143], v[184:187], a[188:191], v[140:143]
ds_read_b128 a[136:139], v237 offset:2304
v_mfma_f32_16x16x32_bf16 v[140:143], v[188:191], a[172:175], v[140:143]
ds_read_b128 a[140:143], v237 offset:4608
v_mfma_f32_16x16x32_bf16 v[140:143], v[192:195], a[192:195], v[140:143]
ds_read_b128 a[152:155], v252 offset:0
ds_read_b128 v[216:219], v241 offset:0
v_pk_fma_f32 v[158:159], v[32:33], v[32:33], v[158:159]
v_pk_add_f32 v[156:157], v[32:33], v[156:157]
v_pk_fma_f32 v[162:163], v[80:81], v[80:81], v[162:163]
v_pk_add_f32 v[160:161], v[80:81], v[160:161]
v_pk_fma_f32 v[166:167], v[128:129], v[128:129], v[166:167]
v_pk_add_f32 v[164:165], v[128:129], v[164:165]
v_pk_fma_f32 v[158:159], v[34:35], v[34:35], v[158:159]
v_pk_add_f32 v[156:157], v[34:35], v[156:157]
v_pk_fma_f32 v[162:163], v[82:83], v[82:83], v[162:163]
v_pk_add_f32 v[160:161], v[82:83], v[160:161]
v_pk_fma_f32 v[166:167], v[130:131], v[130:131], v[166:167]
v_pk_add_f32 v[164:165], v[130:131], v[164:165]
v_pk_fma_f32 v[158:159], v[36:37], v[36:37], v[158:159]
v_pk_add_f32 v[156:157], v[36:37], v[156:157]
v_pk_fma_f32 v[162:163], v[84:85], v[84:85], v[162:163]
v_pk_add_f32 v[160:161], v[84:85], v[160:161]
v_pk_fma_f32 v[166:167], v[132:133], v[132:133], v[166:167]
v_pk_add_f32 v[164:165], v[132:133], v[164:165]
v_pk_fma_f32 v[158:159], v[38:39], v[38:39], v[158:159]
v_pk_add_f32 v[156:157], v[38:39], v[156:157]
v_pk_fma_f32 v[162:163], v[86:87], v[86:87], v[162:163]
v_pk_add_f32 v[160:161], v[86:87], v[160:161]
v_pk_fma_f32 v[166:167], v[134:135], v[134:135], v[166:167]
v_pk_add_f32 v[164:165], v[134:135], v[164:165]
v_pk_fma_f32 v[158:159], v[40:41], v[40:41], v[158:159]
v_pk_add_f32 v[156:157], v[40:41], v[156:157]
v_pk_fma_f32 v[162:163], v[88:89], v[88:89], v[162:163]
v_pk_add_f32 v[160:161], v[88:89], v[160:161]
v_pk_fma_f32 v[166:167], v[136:137], v[136:137], v[166:167]
v_pk_add_f32 v[164:165], v[136:137], v[164:165]
v_pk_fma_f32 v[158:159], v[42:43], v[42:43], v[158:159]
v_pk_add_f32 v[156:157], v[42:43], v[156:157]
v_pk_fma_f32 v[162:163], v[90:91], v[90:91], v[162:163]
v_pk_add_f32 v[160:161], v[90:91], v[160:161]
v_pk_fma_f32 v[166:167], v[138:139], v[138:139], v[166:167]
v_pk_add_f32 v[164:165], v[138:139], v[164:165]
v_pk_fma_f32 v[158:159], v[44:45], v[44:45], v[158:159]
v_pk_add_f32 v[156:157], v[44:45], v[156:157]
v_pk_fma_f32 v[162:163], v[92:93], v[92:93], v[162:163]
v_pk_add_f32 v[160:161], v[92:93], v[160:161]
v_pk_fma_f32 v[166:167], v[140:141], v[140:141], v[166:167]
v_pk_add_f32 v[164:165], v[140:141], v[164:165]
v_pk_fma_f32 v[158:159], v[46:47], v[46:47], v[158:159]
v_pk_add_f32 v[156:157], v[46:47], v[156:157]
v_pk_fma_f32 v[162:163], v[94:95], v[94:95], v[162:163]
v_pk_add_f32 v[160:161], v[94:95], v[160:161]
v_pk_fma_f32 v[166:167], v[142:143], v[142:143], v[166:167]
v_pk_add_f32 v[164:165], v[142:143], v[164:165]
s_waitcnt lgkmcnt(9)
s_barrier
ds_read_b128 a[228:231], v238 offset:49152
ds_read_b128 a[232:235], v238 offset:50176
ds_read_b128 a[236:239], v238 offset:51200
ds_read_b128 a[240:243], v238 offset:52224
ds_read_b128 a[244:247], v238 offset:53248
ds_read_b128 a[248:251], v238 offset:54272
v_add_u32 v240, 157696, v240
v_add_f32 v156, v156, v157
v_add_f32 v158, v158, v159
v_add_f32 v160, v160, v161
v_add_f32 v162, v162, v163
v_add_f32 v164, v164, v165
v_add_f32 v166, v166, v167
v_permlane32_swap_b32 v156, v158
v_permlane32_swap_b32 v160, v162
v_permlane32_swap_b32 v164, v166
v_add_f32 v156, v156, v158
v_add_f32 v160, v160, v162
v_add_f32 v164, v164, v166
v_mov_b32 v165, v164
v_permlane16_swap_b32 v156, v160
s_nop 0
v_permlane16_swap_b32 v164, v165
v_add_f32 v156, v156, v160
v_add_f32 v157, v164, v165
s_nop 1
v_permlane32_swap_b32 v156, v157
s_mov_b32 s33, 0x3727c5ac
v_mul_f32 v159, 0xbbaaaaab, v156
v_mul_f32 v158, 0x3baaaaab, v157
v_fma_f32 v158, -v159, v159, v158
v_max_f32 v158, 0, v158
v_add_f32 v158, s33, v158
v_rsq_f32 v158, v158
s_nop 0
v_mul_f32 v156, 0x3eeff558, v158
v_mul_f32 v157, v159, v156
v_mov_b32 v160, v156
v_mov_b32 v161, v157
s_nop 0
v_permlane16_swap_b32 v156, v160
v_permlane16_swap_b32 v157, v161
v_mov_b32 v164, v156
v_mov_b32 v158, v160
v_mov_b32 v165, v157
v_mov_b32 v159, v161
v_permlane32_swap_b32 v156, v164
v_permlane32_swap_b32 v160, v158
v_permlane32_swap_b32 v157, v165
v_permlane32_swap_b32 v161, v159
v_pk_fma_f32 v[0:1], v[0:1], v[156:157], v[156:157] op_sel:[0,0,1] op_sel_hi:[1,0,1]
v_pk_fma_f32 v[2:3], v[2:3], v[156:157], v[156:157] op_sel:[0,0,1] op_sel_hi:[1,0,1]
v_pk_fma_f32 v[4:5], v[4:5], v[156:157], v[156:157] op_sel:[0,0,1] op_sel_hi:[1,0,1]
v_pk_fma_f32 v[6:7], v[6:7], v[156:157], v[156:157] op_sel:[0,0,1] op_sel_hi:[1,0,1]
v_pk_fma_f32 v[8:9], v[8:9], v[156:157], v[156:157] op_sel:[0,0,1] op_sel_hi:[1,0,1]
v_pk_fma_f32 v[10:11], v[10:11], v[156:157], v[156:157] op_sel:[0,0,1] op_sel_hi:[1,0,1]
v_pk_fma_f32 v[12:13], v[12:13], v[156:157], v[156:157] op_sel:[0,0,1] op_sel_hi:[1,0,1]
v_pk_fma_f32 v[14:15], v[14:15], v[156:157], v[156:157] op_sel:[0,0,1] op_sel_hi:[1,0,1]
v_pk_fma_f32 v[16:17], v[16:17], v[156:157], v[156:157] op_sel:[0,0,1] op_sel_hi:[1,0,1]
v_pk_fma_f32 v[18:19], v[18:19], v[156:157], v[156:157] op_sel:[0,0,1] op_sel_hi:[1,0,1]
v_pk_fma_f32 v[20:21], v[20:21], v[156:157], v[156:157] op_sel:[0,0,1] op_sel_hi:[1,0,1]
v_pk_fma_f32 v[22:23], v[22:23], v[156:157], v[156:157] op_sel:[0,0,1] op_sel_hi:[1,0,1]
v_pk_fma_f32 v[24:25], v[24:25], v[156:157], v[156:157] op_sel:[0,0,1] op_sel_hi:[1,0,1]
v_pk_fma_f32 v[26:27], v[26:27], v[156:157], v[156:157] op_sel:[0,0,1] op_sel_hi:[1,0,1]
v_pk_fma_f32 v[28:29], v[28:29], v[156:157], v[156:157] op_sel:[0,0,1] op_sel_hi:[1,0,1]
v_pk_fma_f32 v[30:31], v[30:31], v[156:157], v[156:157] op_sel:[0,0,1] op_sel_hi:[1,0,1]
v_pk_fma_f32 v[32:33], v[32:33], v[156:157], v[156:157] op_sel:[0,0,1] op_sel_hi:[1,0,1]
v_pk_fma_f32 v[34:35], v[34:35], v[156:157], v[156:157] op_sel:[0,0,1] op_sel_hi:[1,0,1]
v_pk_fma_f32 v[36:37], v[36:37], v[156:157], v[156:157] op_sel:[0,0,1] op_sel_hi:[1,0,1]
v_pk_fma_f32 v[38:39], v[38:39], v[156:157], v[156:157] op_sel:[0,0,1] op_sel_hi:[1,0,1]
v_pk_fma_f32 v[40:41], v[40:41], v[156:157], v[156:157] op_sel:[0,0,1] op_sel_hi:[1,0,1]
v_pk_fma_f32 v[42:43], v[42:43], v[156:157], v[156:157] op_sel:[0,0,1] op_sel_hi:[1,0,1]
v_pk_fma_f32 v[44:45], v[44:45], v[156:157], v[156:157] op_sel:[0,0,1] op_sel_hi:[1,0,1]
v_pk_fma_f32 v[46:47], v[46:47], v[156:157], v[156:157] op_sel:[0,0,1] op_sel_hi:[1,0,1]
v_pk_fma_f32 v[48:49], v[48:49], v[160:161], v[160:161] op_sel:[0,0,1] op_sel_hi:[1,0,1]
v_pk_fma_f32 v[50:51], v[50:51], v[160:161], v[160:161] op_sel:[0,0,1] op_sel_hi:[1,0,1]
v_pk_fma_f32 v[52:53], v[52:53], v[160:161], v[160:161] op_sel:[0,0,1] op_sel_hi:[1,0,1]
v_pk_fma_f32 v[54:55], v[54:55], v[160:161], v[160:161] op_sel:[0,0,1] op_sel_hi:[1,0,1]
v_pk_fma_f32 v[56:57], v[56:57], v[160:161], v[160:161] op_sel:[0,0,1] op_sel_hi:[1,0,1]
v_pk_fma_f32 v[58:59], v[58:59], v[160:161], v[160:161] op_sel:[0,0,1] op_sel_hi:[1,0,1]
v_pk_fma_f32 v[60:61], v[60:61], v[160:161], v[160:161] op_sel:[0,0,1] op_sel_hi:[1,0,1]
v_pk_fma_f32 v[62:63], v[62:63], v[160:161], v[160:161] op_sel:[0,0,1] op_sel_hi:[1,0,1]
v_pk_fma_f32 v[64:65], v[64:65], v[160:161], v[160:161] op_sel:[0,0,1] op_sel_hi:[1,0,1]
v_pk_fma_f32 v[66:67], v[66:67], v[160:161], v[160:161] op_sel:[0,0,1] op_sel_hi:[1,0,1]
v_pk_fma_f32 v[68:69], v[68:69], v[160:161], v[160:161] op_sel:[0,0,1] op_sel_hi:[1,0,1]
v_pk_fma_f32 v[70:71], v[70:71], v[160:161], v[160:161] op_sel:[0,0,1] op_sel_hi:[1,0,1]
v_pk_fma_f32 v[72:73], v[72:73], v[160:161], v[160:161] op_sel:[0,0,1] op_sel_hi:[1,0,1]
v_pk_fma_f32 v[74:75], v[74:75], v[160:161], v[160:161] op_sel:[0,0,1] op_sel_hi:[1,0,1]
v_pk_fma_f32 v[76:77], v[76:77], v[160:161], v[160:161] op_sel:[0,0,1] op_sel_hi:[1,0,1]
v_pk_fma_f32 v[78:79], v[78:79], v[160:161], v[160:161] op_sel:[0,0,1] op_sel_hi:[1,0,1]
v_pk_fma_f32 v[80:81], v[80:81], v[160:161], v[160:161] op_sel:[0,0,1] op_sel_hi:[1,0,1]
v_pk_fma_f32 v[82:83], v[82:83], v[160:161], v[160:161] op_sel:[0,0,1] op_sel_hi:[1,0,1]
v_pk_fma_f32 v[84:85], v[84:85], v[160:161], v[160:161] op_sel:[0,0,1] op_sel_hi:[1,0,1]
v_pk_fma_f32 v[86:87], v[86:87], v[160:161], v[160:161] op_sel:[0,0,1] op_sel_hi:[1,0,1]
v_pk_fma_f32 v[88:89], v[88:89], v[160:161], v[160:161] op_sel:[0,0,1] op_sel_hi:[1,0,1]
v_pk_fma_f32 v[90:91], v[90:91], v[160:161], v[160:161] op_sel:[0,0,1] op_sel_hi:[1,0,1]
v_pk_fma_f32 v[92:93], v[92:93], v[160:161], v[160:161] op_sel:[0,0,1] op_sel_hi:[1,0,1]
v_pk_fma_f32 v[94:95], v[94:95], v[160:161], v[160:161] op_sel:[0,0,1] op_sel_hi:[1,0,1]
s_mov_b32 s30, 589824
buffer_load_dwordx4 v[204:207], v238, s[12:15], s30 offen offset:0 sc1
v_pk_fma_f32 v[96:97], v[96:97], v[164:165], v[164:165] op_sel:[0,0,1] op_sel_hi:[1,0,1]
v_pk_fma_f32 v[98:99], v[98:99], v[164:165], v[164:165] op_sel:[0,0,1] op_sel_hi:[1,0,1]
v_pk_fma_f32 v[100:101], v[100:101], v[164:165], v[164:165] op_sel:[0,0,1] op_sel_hi:[1,0,1]
v_pk_fma_f32 v[102:103], v[102:103], v[164:165], v[164:165] op_sel:[0,0,1] op_sel_hi:[1,0,1]
v_pk_fma_f32 v[104:105], v[104:105], v[164:165], v[164:165] op_sel:[0,0,1] op_sel_hi:[1,0,1]
v_pk_fma_f32 v[106:107], v[106:107], v[164:165], v[164:165] op_sel:[0,0,1] op_sel_hi:[1,0,1]
v_pk_fma_f32 v[108:109], v[108:109], v[164:165], v[164:165] op_sel:[0,0,1] op_sel_hi:[1,0,1]
v_pk_fma_f32 v[110:111], v[110:111], v[164:165], v[164:165] op_sel:[0,0,1] op_sel_hi:[1,0,1]
v_pk_fma_f32 v[112:113], v[112:113], v[164:165], v[164:165] op_sel:[0,0,1] op_sel_hi:[1,0,1]
v_pk_fma_f32 v[114:115], v[114:115], v[164:165], v[164:165] op_sel:[0,0,1] op_sel_hi:[1,0,1]
v_pk_fma_f32 v[116:117], v[116:117], v[164:165], v[164:165] op_sel:[0,0,1] op_sel_hi:[1,0,1]
v_pk_fma_f32 v[118:119], v[118:119], v[164:165], v[164:165] op_sel:[0,0,1] op_sel_hi:[1,0,1]
v_pk_fma_f32 v[120:121], v[120:121], v[164:165], v[164:165] op_sel:[0,0,1] op_sel_hi:[1,0,1]
v_pk_fma_f32 v[122:123], v[122:123], v[164:165], v[164:165] op_sel:[0,0,1] op_sel_hi:[1,0,1]
v_pk_fma_f32 v[124:125], v[124:125], v[164:165], v[164:165] op_sel:[0,0,1] op_sel_hi:[1,0,1]
v_pk_fma_f32 v[126:127], v[126:127], v[164:165], v[164:165] op_sel:[0,0,1] op_sel_hi:[1,0,1]
v_pk_fma_f32 v[128:129], v[128:129], v[164:165], v[164:165] op_sel:[0,0,1] op_sel_hi:[1,0,1]
v_pk_fma_f32 v[130:131], v[130:131], v[164:165], v[164:165] op_sel:[0,0,1] op_sel_hi:[1,0,1]
v_pk_fma_f32 v[132:133], v[132:133], v[164:165], v[164:165] op_sel:[0,0,1] op_sel_hi:[1,0,1]
v_pk_fma_f32 v[134:135], v[134:135], v[164:165], v[164:165] op_sel:[0,0,1] op_sel_hi:[1,0,1]
v_pk_fma_f32 v[136:137], v[136:137], v[164:165], v[164:165] op_sel:[0,0,1] op_sel_hi:[1,0,1]
v_pk_fma_f32 v[138:139], v[138:139], v[164:165], v[164:165] op_sel:[0,0,1] op_sel_hi:[1,0,1]
v_pk_fma_f32 v[140:141], v[140:141], v[164:165], v[164:165] op_sel:[0,0,1] op_sel_hi:[1,0,1]
v_pk_fma_f32 v[142:143], v[142:143], v[164:165], v[164:165] op_sel:[0,0,1] op_sel_hi:[1,0,1]
v_cvt_pk_bf16_f32 v0, v0, v1
v_cvt_pk_bf16_f32 v1, v2, v3
v_cvt_pk_bf16_f32 v2, v4, v5
v_cvt_pk_bf16_f32 v3, v6, v7
v_cvt_pk_bf16_f32 v4, v8, v9
v_cvt_pk_bf16_f32 v5, v10, v11
v_cvt_pk_bf16_f32 v6, v12, v13
v_cvt_pk_bf16_f32 v7, v14, v15
v_cvt_pk_bf16_f32 v8, v16, v17
v_cvt_pk_bf16_f32 v9, v18, v19
v_cvt_pk_bf16_f32 v10, v20, v21
v_cvt_pk_bf16_f32 v11, v22, v23
v_cvt_pk_bf16_f32 v12, v24, v25
v_cvt_pk_bf16_f32 v13, v26, v27
v_cvt_pk_bf16_f32 v14, v28, v29
v_cvt_pk_bf16_f32 v15, v30, v31
v_cvt_pk_bf16_f32 v16, v32, v33
v_cvt_pk_bf16_f32 v17, v34, v35
v_cvt_pk_bf16_f32 v18, v36, v37
v_cvt_pk_bf16_f32 v19, v38, v39
v_cvt_pk_bf16_f32 v20, v40, v41
v_cvt_pk_bf16_f32 v21, v42, v43
v_cvt_pk_bf16_f32 v22, v44, v45
v_cvt_pk_bf16_f32 v23, v46, v47
v_cvt_pk_bf16_f32 v48, v48, v49
v_cvt_pk_bf16_f32 v49, v50, v51
v_cvt_pk_bf16_f32 v50, v52, v53
v_cvt_pk_bf16_f32 v51, v54, v55
v_cvt_pk_bf16_f32 v52, v56, v57
v_cvt_pk_bf16_f32 v53, v58, v59
v_cvt_pk_bf16_f32 v54, v60, v61
v_cvt_pk_bf16_f32 v55, v62, v63
v_cvt_pk_bf16_f32 v56, v64, v65
v_cvt_pk_bf16_f32 v57, v66, v67
v_cvt_pk_bf16_f32 v58, v68, v69
v_cvt_pk_bf16_f32 v59, v70, v71
v_cvt_pk_bf16_f32 v60, v72, v73
v_cvt_pk_bf16_f32 v61, v74, v75
v_cvt_pk_bf16_f32 v62, v76, v77
v_cvt_pk_bf16_f32 v63, v78, v79
v_cvt_pk_bf16_f32 v64, v80, v81
v_cvt_pk_bf16_f32 v65, v82, v83
v_cvt_pk_bf16_f32 v66, v84, v85
v_cvt_pk_bf16_f32 v67, v86, v87
v_cvt_pk_bf16_f32 v68, v88, v89
v_cvt_pk_bf16_f32 v69, v90, v91
v_cvt_pk_bf16_f32 v70, v92, v93
v_cvt_pk_bf16_f32 v71, v94, v95
s_mov_b64 vcc, -1
s_mov_b32 s62, 0
s_mov_b32 s63, s23
s_add_u32 s36, s23, 49152
s_xor_b32 s36, s36, s23
s_mov_b32 s37, 5
s_waitcnt lgkmcnt(6)
v_mfma_f32_16x16x32_bf16 v[24:27], a[96:99], v[0:3], v[216:219]
ds_read_b64_tr_b16 a[204:205], v212 offset:0
v_bfe_u32 v144, v238, 4, 4
v_cvt_pk_bf16_f32 v96, v96, v97
v_cvt_pk_bf16_f32 v97, v98, v99
v_mfma_f32_16x16x32_bf16 v[24:27], a[100:103], v[4:7], v[24:27]
ds_read_b64_tr_b16 a[206:207], v212 offset:2048
v_bfe_u32 v145, v238, 8, 2
v_cvt_pk_bf16_f32 v98, v100, v101
v_cvt_pk_bf16_f32 v99, v102, v103
v_mfma_f32_16x16x32_bf16 v[24:27], a[104:107], v[8:11], v[24:27]
ds_read_b64_tr_b16 a[208:209], v212 offset:4096
ds_read_b64_tr_b16 a[180:181], v209 offset:0
v_cvt_pk_bf16_f32 v100, v104, v105
v_cvt_pk_bf16_f32 v101, v106, v107
v_mfma_f32_16x16x32_bf16 v[24:27], a[120:123], v[12:15], v[24:27]
ds_read_b64_tr_b16 a[210:211], v212 offset:6144
ds_read_b64_tr_b16 a[182:183], v209 offset:2048
v_cvt_pk_bf16_f32 v102, v108, v109
v_mfma_f32_16x16x32_bf16 v[24:27], a[124:127], v[16:19], v[24:27]
ds_read_b64_tr_b16 a[212:213], v212 offset:8192
ds_read_b64_tr_b16 a[184:185], v209 offset:4096
v_cvt_pk_bf16_f32 v103, v110, v111
v_cvt_pk_bf16_f32 v104, v112, v113
v_mfma_f32_16x16x32_bf16 v[24:27], a[128:131], v[20:23], v[24:27]
s_nop 0
ds_read_b64_tr_b16 a[214:215], v212 offset:10240
s_waitcnt lgkmcnt(11)
ds_read_b64_tr_b16 a[186:187], v209 offset:6144
v_cvt_pk_bf16_f32 v105, v114, v115
v_cvt_pk_bf16_f32 v106, v116, v117
v_mfma_f32_16x16x32_bf16 v[28:31], a[128:131], v[68:71], v[216:219]
ds_read_b64_tr_b16 a[216:217], v212 offset:12288
ds_read_b64_tr_b16 a[188:189], v209 offset:8192
v_cvt_pk_bf16_f32 v107, v118, v119
v_mfma_f32_16x16x32_bf16 v[28:31], a[124:127], v[64:67], v[28:31]
s_nop 0
ds_read_b64_tr_b16 a[218:219], v212 offset:14336
s_waitcnt lgkmcnt(11)
ds_read_b64_tr_b16 a[190:191], v209 offset:10240
v_cvt_pk_bf16_f32 v108, v120, v121
v_cvt_pk_bf16_f32 v109, v122, v123
v_mfma_f32_16x16x32_bf16 v[28:31], a[120:123], v[60:63], v[28:31]
ds_read_b64_tr_b16 a[220:221], v212 offset:16384
ds_read_b64_tr_b16 a[192:193], v209 offset:12288
v_cvt_pk_bf16_f32 v110, v124, v125
v_cvt_pk_bf16_f32 v111, v126, v127
v_mfma_f32_16x16x32_bf16 v[28:31], a[104:107], v[56:59], v[28:31]
s_nop 0
ds_read_b64_tr_b16 a[222:223], v212 offset:18432
s_waitcnt lgkmcnt(11)
ds_read_b64_tr_b16 a[194:195], v209 offset:14336
v_cvt_pk_bf16_f32 v112, v128, v129
v_cvt_pk_bf16_f32 v113, v130, v131
v_mfma_f32_16x16x32_bf16 v[28:31], a[100:103], v[52:55], v[28:31]
ds_read_b64_tr_b16 a[224:225], v212 offset:20480
ds_read_b64_tr_b16 a[196:197], v209 offset:16384
v_cvt_pk_bf16_f32 v114, v132, v133
v_mfma_f32_16x16x32_bf16 v[28:31], a[96:99], v[48:51], v[28:31]
s_nop 0
ds_read_b64_tr_b16 a[226:227], v212 offset:22528
s_waitcnt lgkmcnt(11)
ds_read_b64_tr_b16 a[198:199], v209 offset:18432
v_cvt_pk_bf16_f32 v115, v134, v135
v_cvt_pk_bf16_f32 v116, v136, v137
v_mfma_f32_16x16x32_bf16 v[32:35], a[96:99], v[96:99], v[216:219]
ds_read_b128 v[220:223], v242 offset:0
ds_read_b64_tr_b16 a[200:201], v209 offset:20480
v_cvt_pk_bf16_f32 v117, v138, v139
v_cvt_pk_bf16_f32 v118, v140, v141
v_mfma_f32_16x16x32_bf16 v[32:35], a[100:103], v[100:103], v[32:35]
s_nop 0
ds_read_b64_tr_b16 a[202:203], v209 offset:22528
s_waitcnt lgkmcnt(11)
ds_read_b128 v[216:219], v241 offset:64
v_cvt_pk_bf16_f32 v119, v142, v143
v_mfma_f32_16x16x32_bf16 v[32:35], a[104:107], v[104:107], v[32:35]
v_lshrrev_b32_e64 v150, 1, v144
v_lshlrev_b32 v151, 1, v145
s_waitcnt vmcnt(0)
v_cmp_eq_u32 s[34:35], s43, v204
v_mfma_f32_16x16x32_bf16 v[32:35], a[120:123], v[108:111], v[32:35]
v_and_b32 v146, 1, v144
v_sub_u32 v152, v150, v151
v_cmp_eq_u32 s[64:65], s43, v205
v_mfma_f32_16x16x32_bf16 v[32:35], a[124:127], v[112:115], v[32:35]
v_lshlrev_b32_e64 v147, 4, v146
v_mov_b32 v148, 0x3f80
v_cmp_eq_u32 s[66:67], s43, v206
v_mfma_f32_16x16x32_bf16 v[32:35], a[128:131], v[116:119], v[32:35]
s_waitcnt lgkmcnt(2)
v_cmp_eq_u32 vcc, 0, v152
v_lshlrev_b32 v149, v147, v148
s_and_b32 s30, s20, 1
v_cmp_eq_u32 s[68:69], s43, v207
v_mfma_f32_16x16x32_bf16 v[224:227], a[204:207], a[228:231], v[220:223]
v_cndmask_b32 v192, 0, v149, vcc
v_cmp_eq_u32 vcc, 1, v152
s_sub_u32 s30, s30, 1
s_not_b32 s31, s30
v_mfma_f32_16x16x32_bf16 v[224:227], a[208:211], a[232:235], v[224:227]
s_and_b64 s[34:35], s[34:35], s[64:65]
v_cndmask_b32 v193, 0, v149, vcc
v_mov_b32_e64 v194, 0
v_mfma_f32_16x16x32_bf16 v[224:227], a[212:215], a[236:239], v[224:227]
s_and_b64 s[66:67], s[66:67], s[68:69]
v_mov_b32 v196, 0
v_mov_b32_e64 v198, v192
v_mfma_f32_16x16x32_bf16 v[224:227], a[216:219], a[240:243], v[224:227]
s_and_b64 s[34:35], s[34:35], s[66:67]
v_mov_b32 v195, 0
v_mov_b32_e64 v197, 0
v_mfma_f32_16x16x32_bf16 v[224:227], a[220:223], a[244:247], v[224:227]
s_or_b32 s34, s34, 0x0fffffff
s_bitset1_b32 s35, 31
v_mov_b32 v199, v193
v_and_b32_e64 v200, s30, v192
v_mfma_f32_16x16x32_bf16 v[224:227], a[224:227], a[248:251], v[224:227]
s_waitcnt lgkmcnt(0)
s_cmp_eq_u64 s[34:35], -1
s_cbranch_scc0 L_slow_x
L_fast_x:
v_and_b32 v201, s30, v193
v_and_b32 v202, s31, v192
v_and_b32 v203, s31, v193
v_pk_fma_f32 v[86:87], v[24:25], v[24:25], s[28:29]
v_pk_fma_f32 v[84:85], v[26:27], v[26:27], s[28:29]
v_pk_fma_f32 v[90:91], v[28:29], v[28:29], s[28:29]
v_pk_fma_f32 v[88:89], v[30:31], v[30:31], s[28:29]
v_pk_fma_f32 v[94:95], v[32:33], v[32:33], s[28:29]
v_pk_fma_f32 v[92:93], v[34:35], v[34:35], s[28:29]
v_pk_fma_f32 v[234:235], v[224:225], v[224:225], s[28:29]
v_pk_fma_f32 v[232:233], v[226:227], v[226:227], s[28:29]
v_pk_mul_f32 v[86:87], v[24:25], v[86:87] neg_lo:[1,0] neg_hi:[1,0]
v_pk_mul_f32 v[84:85], v[26:27], v[84:85] neg_lo:[1,0] neg_hi:[1,0]
v_pk_mul_f32 v[90:91], v[28:29], v[90:91] neg_lo:[1,0] neg_hi:[1,0]
v_pk_mul_f32 v[88:89], v[30:31], v[88:89] neg_lo:[1,0] neg_hi:[1,0]
v_pk_mul_f32 v[94:95], v[32:33], v[94:95] neg_lo:[1,0] neg_hi:[1,0]
v_pk_mul_f32 v[92:93], v[34:35], v[92:93] neg_lo:[1,0] neg_hi:[1,0]
v_pk_mul_f32 v[234:235], v[224:225], v[234:235] neg_lo:[1,0] neg_hi:[1,0]
v_pk_mul_f32 v[232:233], v[226:227], v[232:233] neg_lo:[1,0] neg_hi:[1,0]
v_mfma_f32_16x16x32_bf16 v[36:39], a[180:183], v[0:3], v[216:219]
s_add_u32 m0, s23, 129024
s_add_u32 s26, s62, 49152
ds_read_b128 a[204:207], v253 offset:0
v_exp_f32_e64 v88, v88
v_mfma_f32_16x16x32_bf16 v[36:39], a[184:187], v[4:7], v[36:39]
buffer_load_dwordx4 v248, s[12:15], s26 offen offset:0 sc1 lds
v_mfma_f32_16x16x32_bf16 v[36:39], a[188:191], v[8:11], v[36:39]
ds_read_b64_tr_b16 a[156:157], v210 offset:0
ds_read_b128 a[208:211], v253 offset:16
v_mfma_f32_16x16x32_bf16 v[36:39], a[192:195], v[12:15], v[36:39]
ds_read_b64_tr_b16 a[158:159], v210 offset:2048
v_exp_f32_e64 v89, v89
v_mfma_f32_16x16x32_bf16 v[36:39], a[196:199], v[16:19], v[36:39]
ds_read_b64_tr_b16 a[160:161], v210 offset:4096
v_exp_f32_e64 v94, v94
v_mfma_f32_16x16x32_bf16 v[36:39], a[200:203], v[20:23], v[36:39]
ds_read_b64_tr_b16 a[162:163], v210 offset:6144
v_exp_f32_e64 v95, v95
v_mfma_f32_16x16x32_bf16 v[40:43], a[200:203], v[68:71], v[216:219]
ds_read_b64_tr_b16 a[164:165], v210 offset:8192
v_exp_f32_e64 v92, v92
v_mfma_f32_16x16x32_bf16 v[40:43], a[196:199], v[64:67], v[40:43]
ds_read_b64_tr_b16 a[166:167], v210 offset:10240
v_exp_f32_e64 v93, v93
v_mfma_f32_16x16x32_bf16 v[40:43], a[192:195], v[60:63], v[40:43]
ds_read_b64_tr_b16 a[168:169], v210 offset:12288
v_exp_f32_e64 v234, v234
v_mfma_f32_16x16x32_bf16 v[40:43], a[188:191], v[56:59], v[40:43]
ds_read_b64_tr_b16 a[170:171], v210 offset:14336
v_exp_f32_e64 v235, v235
v_mfma_f32_16x16x32_bf16 v[40:43], a[184:187], v[52:55], v[40:43]
ds_read_b64_tr_b16 a[172:173], v210 offset:16384
v_exp_f32_e64 v232, v232
v_mfma_f32_16x16x32_bf16 v[40:43], a[180:183], v[48:51], v[40:43]
ds_read_b64_tr_b16 a[174:175], v210 offset:18432
v_exp_f32_e64 v233, v233
v_mfma_f32_16x16x32_bf16 v[44:47], a[180:183], v[96:99], v[216:219]
ds_read_b64_tr_b16 a[176:177], v210 offset:20480
v_mfma_f32_16x16x32_bf16 v[44:47], a[184:187], v[100:103], v[44:47]
ds_read_b64_tr_b16 a[178:179], v210 offset:22528
ds_read_b128 v[216:219], v241 offset:128
v_mfma_f32_16x16x32_bf16 v[44:47], a[188:191], v[104:107], v[44:47]
buffer_load_dwordx4 v248, s[12:15], s26 offen offset:1024 sc1 lds
v_mfma_f32_16x16x32_bf16 v[44:47], a[192:195], v[108:111], v[44:47]
buffer_load_dwordx4 v248, s[12:15], s26 offen offset:2048 sc1 lds
v_mfma_f32_16x16x32_bf16 v[44:47], a[196:199], v[112:115], v[44:47]
s_add_u32 m0, s23, 132096
s_add_u32 s26, s62, 52224
v_exp_f32 v86, v86
v_mfma_f32_16x16x32_bf16 v[44:47], a[200:203], v[116:119], v[44:47]
s_waitcnt lgkmcnt(2)
buffer_load_dwordx4 v248, s[12:15], s26 offen offset:0 sc1 lds
v_mfma_f32_16x16x32_bf16 a[0:3], v[192:195], a[12:15], a[204:207]
buffer_load_dwordx4 v248, s[12:15], s26 offen offset:1024 sc1 lds
v_mfma_f32_16x16x32_bf16 a[12:15], v[196:199], a[12:15], a[208:211]
buffer_load_dwordx4 v248, s[12:15], s26 offen offset:2048 sc1 lds
v_mfma_f32_16x16x32_bf16 a[4:7], v[192:195], a[16:19], a[204:207]
v_exp_f32_e64 v87, v87
v_mfma_f32_16x16x32_bf16 a[16:19], v[196:199], a[16:19], a[208:211]
v_exp_f32_e64 v84, v84
v_mfma_f32_16x16x32_bf16 a[8:11], v[192:195], a[20:23], a[204:207]
v_exp_f32_e64 v85, v85
v_mfma_f32_16x16x32_bf16 a[20:23], v[196:199], a[20:23], a[208:211]
s_waitcnt lgkmcnt(0)
v_exp_f32 v90, v90
v_exp_f32 v91, v91
v_add_f32 v86, 1.0, v86
v_add_f32_e64 v87, 1.0, v87
v_pk_add_f32 v[84:85], v[84:85], 1.0 op_sel_hi:[1,0]
v_pk_add_f32 v[90:91], v[90:91], 1.0 op_sel_hi:[1,0]
v_pk_add_f32 v[88:89], v[88:89], 1.0 op_sel_hi:[1,0]
v_pk_add_f32 v[94:95], v[94:95], 1.0 op_sel_hi:[1,0]
v_pk_add_f32 v[92:93], v[92:93], 1.0 op_sel_hi:[1,0]
v_pk_add_f32 v[234:235], v[234:235], 1.0 op_sel_hi:[1,0]
v_pk_add_f32 v[232:233], v[232:233], 1.0 op_sel_hi:[1,0]
v_pk_fma_f32 v[122:123], v[36:37], v[36:37], s[28:29]
v_pk_fma_f32 v[120:121], v[38:39], v[38:39], s[28:29]
v_pk_fma_f32 v[126:127], v[40:41], v[40:41], s[28:29]
v_pk_fma_f32 v[124:125], v[42:43], v[42:43], s[28:29]
v_pk_fma_f32 v[130:131], v[44:45], v[44:45], s[28:29]
v_pk_fma_f32 v[128:129], v[46:47], v[46:47], s[28:29]
v_pk_mul_f32 v[122:123], v[36:37], v[122:123] neg_lo:[1,0] neg_hi:[1,0]
v_pk_mul_f32 v[120:121], v[38:39], v[120:121] neg_lo:[1,0] neg_hi:[1,0]
v_pk_mul_f32 v[126:127], v[40:41], v[126:127] neg_lo:[1,0] neg_hi:[1,0]
v_pk_mul_f32 v[124:125], v[42:43], v[124:125] neg_lo:[1,0] neg_hi:[1,0]
v_pk_mul_f32 v[130:131], v[44:45], v[130:131] neg_lo:[1,0] neg_hi:[1,0]
v_pk_mul_f32 v[128:129], v[46:47], v[128:129] neg_lo:[1,0] neg_hi:[1,0]
v_mfma_f32_16x16x32_bf16 v[72:75], a[156:159], v[0:3], v[216:219]
s_add_u32 m0, s63, 0
s_add_u32 s26, s62, 24576
ds_read_b128 a[204:207], v253 offset:128
v_exp_f32 v125, v125
v_mfma_f32_16x16x32_bf16 v[72:75], a[160:163], v[4:7], v[72:75]
buffer_load_dwordx4 v248, s[12:15], s26 offen offset:0 sc1 lds
v_rcp_f32_e64 v88, v88
v_mfma_f32_16x16x32_bf16 v[72:75], a[164:167], v[8:11], v[72:75]
ds_read_b64_tr_b16 a[180:181], v211 offset:0
ds_read_b128 a[208:211], v253 offset:144
v_rcp_f32_e64 v89, v89
v_mfma_f32_16x16x32_bf16 v[72:75], a[168:171], v[12:15], v[72:75]
ds_read_b64_tr_b16 a[182:183], v211 offset:2048
v_exp_f32_e64 v130, v130
v_mfma_f32_16x16x32_bf16 v[72:75], a[172:175], v[16:19], v[72:75]
ds_read_b64_tr_b16 a[184:185], v211 offset:4096
ds_read_b128 a[252:255], v254 offset:0
v_rcp_f32_e64 v94, v94
v_mfma_f32_16x16x32_bf16 v[72:75], a[176:179], v[20:23], v[72:75]
ds_read_b64_tr_b16 a[186:187], v211 offset:6144
v_exp_f32_e64 v131, v131
v_mfma_f32_16x16x32_bf16 v[76:79], a[176:179], v[68:71], v[216:219]
ds_read_b64_tr_b16 a[188:189], v211 offset:8192
v_exp_f32_e64 v128, v128
v_mfma_f32_16x16x32_bf16 v[76:79], a[172:175], v[64:67], v[76:79]
ds_read_b64_tr_b16 a[190:191], v211 offset:10240
v_exp_f32_e64 v129, v129
v_mfma_f32_16x16x32_bf16 v[76:79], a[168:171], v[60:63], v[76:79]
ds_read_b64_tr_b16 a[192:193], v211 offset:12288
v_rcp_f32_e64 v86, v86
v_mfma_f32_16x16x32_bf16 v[76:79], a[164:167], v[56:59], v[76:79]
ds_read_b64_tr_b16 a[194:195], v211 offset:14336
v_rcp_f32_e64 v87, v87
v_mfma_f32_16x16x32_bf16 v[76:79], a[160:163], v[52:55], v[76:79]
ds_read_b64_tr_b16 a[196:197], v211 offset:16384
v_rcp_f32_e64 v84, v84
v_mfma_f32_16x16x32_bf16 v[76:79], a[156:159], v[48:51], v[76:79]
ds_read_b64_tr_b16 a[198:199], v211 offset:18432
v_rcp_f32_e64 v85, v85
v_mfma_f32_16x16x32_bf16 v[80:83], a[156:159], v[96:99], v[216:219]
ds_read_b64_tr_b16 a[200:201], v211 offset:20480
v_rcp_f32_e64 v90, v90
v_mfma_f32_16x16x32_bf16 v[80:83], a[160:163], v[100:103], v[80:83]
ds_read_b64_tr_b16 a[202:203], v211 offset:22528
s_waitcnt lgkmcnt(11)
ds_read_b128 v[216:219], v241 offset:192
v_rcp_f32 v95, v95
v_mfma_f32_16x16x32_bf16 v[80:83], a[164:167], v[104:107], v[80:83]
buffer_load_dwordx4 v248, s[12:15], s26 offen offset:1024 sc1 lds
v_rcp_f32_e64 v92, v92
v_mfma_f32_16x16x32_bf16 v[80:83], a[168:171], v[108:111], v[80:83]
buffer_load_dwordx4 v248, s[12:15], s26 offen offset:2048 sc1 lds
v_rcp_f32_e64 v93, v93
v_mfma_f32_16x16x32_bf16 v[80:83], a[172:175], v[112:115], v[80:83]
s_add_u32 m0, s63, 3072
s_add_u32 s26, s62, 27648
v_exp_f32 v122, v122
v_rcp_f32 v234, v234
v_mfma_f32_16x16x32_bf16 v[80:83], a[176:179], v[116:119], v[80:83]
buffer_load_dwordx4 v248, s[12:15], s26 offen offset:0 sc1 lds
v_rcp_f32_e64 v235, v235
v_mfma_f32_16x16x32_bf16 a[24:27], v[192:195], a[36:39], a[204:207]
buffer_load_dwordx4 v248, s[12:15], s26 offen offset:1024 sc1 lds
v_rcp_f32_e64 v232, v232
v_mfma_f32_16x16x32_bf16 a[36:39], v[196:199], a[36:39], a[208:211]
v_exp_f32 v123, v123
v_rcp_f32 v233, v233
v_mfma_f32_16x16x32_bf16 a[28:31], v[192:195], a[40:43], a[204:207]
buffer_load_dwordx4 v248, s[12:15], s26 offen offset:2048 sc1 lds
v_mfma_f32_16x16x32_bf16 a[40:43], v[196:199], a[40:43], a[208:211]
v_exp_f32_e64 v120, v120
v_mfma_f32_16x16x32_bf16 a[32:35], v[192:195], a[44:47], a[204:207]
v_exp_f32_e64 v121, v121
v_mfma_f32_16x16x32_bf16 a[44:47], v[196:199], a[44:47], a[208:211]
s_waitcnt lgkmcnt(0)
v_exp_f32 v126, v126
v_mfma_f32_16x16x32_bf16 a[144:147], v[200:203], a[144:147], a[252:255]
v_exp_f32 v127, v127
v_exp_f32 v124, v124
v_rcp_f32_e64 v91, v91
v_pk_add_f32 v[122:123], v[122:123], 1.0 op_sel_hi:[1,0]
v_pk_add_f32 v[120:121], v[120:121], 1.0 op_sel_hi:[1,0]
v_pk_add_f32 v[126:127], v[126:127], 1.0 op_sel_hi:[1,0]
v_pk_add_f32 v[124:125], v[124:125], 1.0 op_sel_hi:[1,0]
v_pk_add_f32 v[130:131], v[130:131], 1.0 op_sel_hi:[1,0]
v_pk_add_f32 v[128:129], v[128:129], 1.0 op_sel_hi:[1,0]
v_pk_mul_f32 v[86:87], v[24:25], v[86:87]
v_pk_mul_f32 v[84:85], v[26:27], v[84:85]
v_pk_mul_f32 v[90:91], v[28:29], v[90:91]
v_pk_mul_f32 v[88:89], v[30:31], v[88:89]
v_pk_mul_f32 v[94:95], v[32:33], v[94:95]
v_pk_mul_f32 v[92:93], v[34:35], v[92:93]
v_pk_mul_f32 v[234:235], v[224:225], v[234:235]
v_pk_mul_f32 v[232:233], v[226:227], v[232:233]
v_pk_fma_f32 v[134:135], v[72:73], v[72:73], s[28:29]
v_pk_fma_f32 v[132:133], v[74:75], v[74:75], s[28:29]
v_pk_fma_f32 v[138:139], v[76:77], v[76:77], s[28:29]
v_pk_fma_f32 v[136:137], v[78:79], v[78:79], s[28:29]
v_pk_fma_f32 v[142:143], v[80:81], v[80:81], s[28:29]
v_pk_fma_f32 v[140:141], v[82:83], v[82:83], s[28:29]
v_pk_mul_f32 v[134:135], v[72:73], v[134:135] neg_lo:[1,0] neg_hi:[1,0]
v_pk_mul_f32 v[132:133], v[74:75], v[132:133] neg_lo:[1,0] neg_hi:[1,0]
v_pk_mul_f32 v[138:139], v[76:77], v[138:139] neg_lo:[1,0] neg_hi:[1,0]
v_pk_mul_f32 v[136:137], v[78:79], v[136:137] neg_lo:[1,0] neg_hi:[1,0]
v_pk_mul_f32 v[142:143], v[80:81], v[142:143] neg_lo:[1,0] neg_hi:[1,0]
v_pk_mul_f32 v[140:141], v[82:83], v[140:141] neg_lo:[1,0] neg_hi:[1,0]
s_waitcnt vmcnt(6)
s_barrier
v_mfma_f32_16x16x32_bf16 v[144:147], a[180:183], v[0:3], v[216:219]
ds_read_b128 a[204:207], v253 offset:256
v_cvt_pk_bf16_f32 v168, v86, v87
v_cvt_pk_bf16_f32 v236, v234, v235
v_mfma_f32_16x16x32_bf16 v[144:147], a[184:187], v[4:7], v[144:147]
ds_read_b128 a[156:159], v239 offset:24576
v_cvt_pk_bf16_f32 v169, v84, v85
v_cvt_pk_bf16_f32 v237, v232, v233
v_mfma_f32_16x16x32_bf16 v[144:147], a[188:191], v[8:11], v[144:147]
ds_read_b128 a[208:211], v253 offset:272
v_cvt_pk_bf16_f32 v172, v90, v91
v_rcp_f32_e64 v130, v130
v_mfma_f32_16x16x32_bf16 v[144:147], a[192:195], v[12:15], v[144:147]
ds_read_b128 a[160:163], v239 offset:25600
v_cvt_pk_bf16_f32 v173, v88, v89
v_rcp_f32_e64 v131, v131
v_mfma_f32_16x16x32_bf16 v[144:147], a[196:199], v[16:19], v[144:147]
ds_read_b128 a[212:215], v253 offset:384
v_cvt_pk_bf16_f32 v176, v94, v95
v_rcp_f32_e64 v128, v128
v_mfma_f32_16x16x32_bf16 v[144:147], a[200:203], v[20:23], v[144:147]
ds_read_b128 a[164:167], v239 offset:26624
v_cvt_pk_bf16_f32 v177, v92, v93
v_rcp_f32_e64 v129, v129
v_mfma_f32_16x16x32_bf16 v[148:151], a[200:203], v[68:71], v[216:219]
s_add_u32 m0, s63, 24576
ds_read_b128 a[216:219], v253 offset:400
ds_write_b64 v240, v[236:237] offset:0
v_mfma_f32_16x16x32_bf16 v[148:151], a[196:199], v[64:67], v[148:151]
s_add_u32 s26, s62, 73728
ds_read_b128 a[168:171], v239 offset:27648
v_rcp_f32_e64 v121, v121
v_mfma_f32_16x16x32_bf16 v[148:151], a[192:195], v[60:63], v[148:151]
buffer_load_dwordx4 v248, s[12:15], s26 offen offset:0 sc1 lds
v_mfma_f32_16x16x32_bf16 v[148:151], a[188:191], v[56:59], v[148:151]
ds_read_b128 a[172:175], v239 offset:28672
v_rcp_f32_e64 v126, v126
v_mfma_f32_16x16x32_bf16 v[148:151], a[184:187], v[52:55], v[148:151]
buffer_load_dwordx4 v248, s[12:15], s26 offen offset:1024 sc1 lds
v_mfma_f32_16x16x32_bf16 v[148:151], a[180:183], v[48:51], v[148:151]
ds_read_b128 a[176:179], v239 offset:29696
v_rcp_f32_e64 v127, v127
v_mfma_f32_16x16x32_bf16 v[152:155], a[180:183], v[96:99], v[216:219]
buffer_load_dwordx4 v248, s[12:15], s26 offen offset:2048 sc1 lds
v_mfma_f32_16x16x32_bf16 v[152:155], a[184:187], v[100:103], v[152:155]
ds_read_b128 v[216:219], v241 offset:256
v_rcp_f32_e64 v124, v124
v_mfma_f32_16x16x32_bf16 v[152:155], a[188:191], v[104:107], v[152:155]
v_exp_f32_e64 v134, v134
v_mfma_f32_16x16x32_bf16 v[152:155], a[192:195], v[108:111], v[152:155]
v_exp_f32_e64 v135, v135
v_mfma_f32_16x16x32_bf16 v[152:155], a[196:199], v[112:115], v[152:155]
s_add_u32 m0, s63, 27648
v_exp_f32 v132, v132
v_mfma_f32_16x16x32_bf16 v[152:155], a[200:203], v[116:119], v[152:155]
s_waitcnt lgkmcnt(1)
s_add_u32 s26, s62, 76800
v_exp_f32_e64 v133, v133
v_mfma_f32_16x16x32_bf16 a[48:51], v[192:195], a[60:63], a[204:207]
buffer_load_dwordx4 v248, s[12:15], s26 offen offset:0 sc1 lds
v_mfma_f32_16x16x32_bf16 a[60:63], v[196:199], a[60:63], a[208:211]
v_exp_f32_e64 v138, v138
v_mfma_f32_16x16x32_bf16 a[52:55], v[192:195], a[64:67], a[204:207]
v_exp_f32_e64 v139, v139
v_mfma_f32_16x16x32_bf16 a[64:67], v[196:199], a[64:67], a[208:211]
v_exp_f32_e64 v136, v136
v_mfma_f32_16x16x32_bf16 a[56:59], v[192:195], a[68:71], a[204:207]
v_exp_f32_e64 v137, v137
v_mfma_f32_16x16x32_bf16 a[68:71], v[196:199], a[68:71], a[208:211]
buffer_load_dwordx4 v248, s[12:15], s26 offen offset:1024 sc1 lds
v_mfma_f32_16x16x32_bf16 a[72:75], v[192:195], a[84:87], a[212:215]
v_exp_f32_e64 v142, v142
v_mfma_f32_16x16x32_bf16 a[84:87], v[196:199], a[84:87], a[216:219]
v_exp_f32_e64 v143, v143
v_mfma_f32_16x16x32_bf16 a[76:79], v[192:195], a[88:91], a[212:215]
v_exp_f32_e64 v140, v140
v_mfma_f32_16x16x32_bf16 a[88:91], v[196:199], a[88:91], a[216:219]
v_exp_f32_e64 v141, v141
v_mfma_f32_16x16x32_bf16 a[80:83], v[192:195], a[92:95], a[212:215]
v_rcp_f32_e64 v122, v122
v_mfma_f32_16x16x32_bf16 a[92:95], v[196:199], a[92:95], a[216:219]
s_waitcnt lgkmcnt(0)
v_rcp_f32 v123, v123
v_rcp_f32 v120, v120
v_rcp_f32 v125, v125
v_pk_add_f32 v[134:135], v[134:135], 1.0 op_sel_hi:[1,0]
v_pk_add_f32 v[132:133], v[132:133], 1.0 op_sel_hi:[1,0]
v_pk_add_f32 v[138:139], v[138:139], 1.0 op_sel_hi:[1,0]
v_pk_add_f32 v[136:137], v[136:137], 1.0 op_sel_hi:[1,0]
v_pk_add_f32 v[142:143], v[142:143], 1.0 op_sel_hi:[1,0]
v_pk_add_f32 v[140:141], v[140:141], 1.0 op_sel_hi:[1,0]
v_pk_mul_f32 v[122:123], v[36:37], v[122:123]
v_pk_mul_f32 v[120:121], v[38:39], v[120:121]
v_pk_mul_f32 v[126:127], v[40:41], v[126:127]
v_pk_mul_f32 v[124:125], v[42:43], v[124:125]
v_pk_mul_f32 v[130:131], v[44:45], v[130:131]
v_pk_mul_f32 v[128:129], v[46:47], v[128:129]
v_pk_fma_f32 v[158:159], v[144:145], v[144:145], s[28:29]
v_pk_fma_f32 v[156:157], v[146:147], v[146:147], s[28:29]
v_pk_fma_f32 v[162:163], v[148:149], v[148:149], s[28:29]
v_pk_fma_f32 v[160:161], v[150:151], v[150:151], s[28:29]
v_pk_fma_f32 v[166:167], v[152:153], v[152:153], s[28:29]
v_pk_fma_f32 v[164:165], v[154:155], v[154:155], s[28:29]
v_pk_mul_f32 v[158:159], v[144:145], v[158:159] neg_lo:[1,0] neg_hi:[1,0]
v_pk_mul_f32 v[156:157], v[146:147], v[156:157] neg_lo:[1,0] neg_hi:[1,0]
v_pk_mul_f32 v[162:163], v[148:149], v[162:163] neg_lo:[1,0] neg_hi:[1,0]
v_pk_mul_f32 v[160:161], v[150:151], v[160:161] neg_lo:[1,0] neg_hi:[1,0]
v_pk_mul_f32 v[166:167], v[152:153], v[166:167] neg_lo:[1,0] neg_hi:[1,0]
v_pk_mul_f32 v[164:165], v[154:155], v[164:165] neg_lo:[1,0] neg_hi:[1,0]
v_mfma_f32_16x16x32_bf16 v[24:27], a[156:159], v[0:3], v[216:219]
ds_read_b128 a[252:255], v254 offset:256
s_add_u32 s30, s20, 163072
v_mov_b32 v250, 0
v_exp_f32 v167, v167
v_mfma_f32_16x16x32_bf16 v[24:27], a[160:163], v[4:7], v[24:27]
ds_read_b128 a[204:207], v243 offset:24576
ds_read_b128 a[180:183], v239 offset:30720
v_exp_f32_e64 v164, v164
v_mfma_f32_16x16x32_bf16 v[24:27], a[164:167], v[8:11], v[24:27]
buffer_load_dwordx4 v248, s[12:15], s26 offen offset:2048 sc1 lds
v_exp_f32_e64 v165, v165
v_mfma_f32_16x16x32_bf16 v[24:27], a[168:171], v[12:15], v[24:27]
ds_read_b128 a[208:211], v243 offset:25600
ds_read_b128 a[184:187], v239 offset:31744
v_rcp_f32_e64 v134, v134
v_mfma_f32_16x16x32_bf16 v[24:27], a[172:175], v[16:19], v[24:27]
s_add_u32 m0, s23, 104448
v_mov_b32_e64 v251, s30
v_mov_b32 v252, 163072
v_rcp_f32_e64 v135, v135
v_mfma_f32_16x16x32_bf16 v[24:27], a[176:179], v[20:23], v[24:27]
s_add_u32 s26, s62, 98304
ds_read_b128 a[212:215], v243 offset:26624
ds_read_b128 a[188:191], v239 offset:32768
v_rcp_f32_e64 v132, v132
v_mfma_f32_16x16x32_bf16 v[28:31], a[176:179], v[68:71], v[216:219]
buffer_load_dwordx4 v248, s[12:15], s26 offen offset:0 sc1 lds
v_rcp_f32_e64 v133, v133
v_mfma_f32_16x16x32_bf16 v[28:31], a[172:175], v[64:67], v[28:31]
ds_read_b128 a[216:219], v243 offset:27648
ds_read_b128 a[192:195], v239 offset:33792
v_rcp_f32_e64 v138, v138
v_mfma_f32_16x16x32_bf16 v[28:31], a[168:171], v[60:63], v[28:31]
ds_write_b8 v251, v250
v_cvt_pk_bf16_f32 v170, v122, v123
v_rcp_f32_e64 v139, v139
v_mfma_f32_16x16x32_bf16 v[28:31], a[164:167], v[56:59], v[28:31]
ds_read_b128 a[220:223], v243 offset:28672
ds_read_b128 a[196:199], v239 offset:34816
v_rcp_f32_e64 v136, v136
v_mfma_f32_16x16x32_bf16 v[28:31], a[160:163], v[52:55], v[28:31]
buffer_load_dwordx4 v248, s[12:15], s26 offen offset:1024 sc1 lds
v_rcp_f32_e64 v137, v137
v_mfma_f32_16x16x32_bf16 v[28:31], a[156:159], v[48:51], v[28:31]
ds_read_b128 a[224:227], v243 offset:29696
ds_read_b128 a[200:203], v239 offset:35840
v_rcp_f32_e64 v142, v142
v_mfma_f32_16x16x32_bf16 v[32:35], a[156:159], v[96:99], v[216:219]
ds_write_b8 v251, v250 offset:4
v_cvt_pk_bf16_f32 v171, v120, v121
v_rcp_f32 v143, v143
v_mfma_f32_16x16x32_bf16 v[32:35], a[160:163], v[100:103], v[32:35]
s_waitcnt lgkmcnt(11)
ds_read_b128 v[220:223], v242 offset:256
ds_read_b128 v[216:219], v241 offset:320
v_rcp_f32_e64 v140, v140
v_mfma_f32_16x16x32_bf16 v[32:35], a[164:167], v[104:107], v[32:35]
v_mov_b32_e64 v250, 1
s_mov_b32 s33, 0x01010101
v_cvt_pk_bf16_f32 v174, v126, v127
v_rcp_f32_e64 v141, v141
v_mfma_f32_16x16x32_bf16 v[32:35], a[168:171], v[108:111], v[32:35]
buffer_load_dwordx4 v248, s[12:15], s26 offen offset:2048 sc1 lds
v_mfma_f32_16x16x32_bf16 v[32:35], a[172:175], v[112:115], v[32:35]
v_cvt_pk_bf16_f32 v175, v124, v125
v_cvt_pk_bf16_f32 v178, v130, v131
v_mfma_f32_16x16x32_bf16 v[32:35], a[176:179], v[116:119], v[32:35]
s_waitcnt lgkmcnt(1)
s_add_u32 m0, s23, 107520
v_cvt_pk_bf16_f32 v179, v128, v129
v_exp_f32 v161, v161
v_mfma_f32_16x16x32_bf16 v[224:227], a[204:207], a[228:231], v[220:223]
s_add_u32 s26, s62, 101376
v_exp_f32_e64 v158, v158
v_mfma_f32_16x16x32_bf16 v[224:227], a[208:211], a[232:235], v[224:227]
buffer_load_dwordx4 v248, s[12:15], s26 offen offset:0 sc1 lds
v_mfma_f32_16x16x32_bf16 v[224:227], a[212:215], a[236:239], v[224:227]
v_exp_f32_e64 v159, v159
v_mfma_f32_16x16x32_bf16 v[224:227], a[216:219], a[240:243], v[224:227]
v_exp_f32_e64 v156, v156
v_mfma_f32_16x16x32_bf16 v[224:227], a[220:223], a[244:247], v[224:227]
v_exp_f32_e64 v157, v157
v_mfma_f32_16x16x32_bf16 v[224:227], a[224:227], a[248:251], v[224:227]
v_exp_f32_e64 v162, v162
v_mfma_f32_16x16x32_bf16 a[148:151], v[200:203], a[148:151], a[252:255]
s_waitcnt lgkmcnt(0)
v_exp_f32 v163, v163
v_exp_f32 v160, v160
v_exp_f32 v166, v166
v_pk_add_f32 v[158:159], v[158:159], 1.0 op_sel_hi:[1,0]
v_pk_add_f32 v[156:157], v[156:157], 1.0 op_sel_hi:[1,0]
v_pk_add_f32 v[162:163], v[162:163], 1.0 op_sel_hi:[1,0]
v_pk_add_f32 v[160:161], v[160:161], 1.0 op_sel_hi:[1,0]
v_pk_add_f32 v[166:167], v[166:167], 1.0 op_sel_hi:[1,0]
v_pk_add_f32 v[164:165], v[164:165], 1.0 op_sel_hi:[1,0]
v_pk_mul_f32 v[134:135], v[72:73], v[134:135]
v_pk_mul_f32 v[132:133], v[74:75], v[132:133]
v_pk_mul_f32 v[138:139], v[76:77], v[138:139]
v_pk_mul_f32 v[136:137], v[78:79], v[136:137]
v_pk_mul_f32 v[142:143], v[80:81], v[142:143]
v_pk_mul_f32 v[140:141], v[82:83], v[140:141]
v_pk_fma_f32 v[86:87], v[24:25], v[24:25], s[28:29]
v_pk_fma_f32 v[84:85], v[26:27], v[26:27], s[28:29]
v_pk_fma_f32 v[90:91], v[28:29], v[28:29], s[28:29]
v_pk_fma_f32 v[88:89], v[30:31], v[30:31], s[28:29]
v_pk_fma_f32 v[94:95], v[32:33], v[32:33], s[28:29]
v_pk_fma_f32 v[92:93], v[34:35], v[34:35], s[28:29]
v_pk_fma_f32 v[234:235], v[224:225], v[224:225], s[28:29]
v_pk_fma_f32 v[232:233], v[226:227], v[226:227], s[28:29]
v_pk_mul_f32 v[86:87], v[24:25], v[86:87] neg_lo:[1,0] neg_hi:[1,0]
v_pk_mul_f32 v[84:85], v[26:27], v[84:85] neg_lo:[1,0] neg_hi:[1,0]
v_pk_mul_f32 v[90:91], v[28:29], v[90:91] neg_lo:[1,0] neg_hi:[1,0]
v_pk_mul_f32 v[88:89], v[30:31], v[88:89] neg_lo:[1,0] neg_hi:[1,0]
v_pk_mul_f32 v[94:95], v[32:33], v[94:95] neg_lo:[1,0] neg_hi:[1,0]
v_pk_mul_f32 v[92:93], v[34:35], v[92:93] neg_lo:[1,0] neg_hi:[1,0]
v_pk_mul_f32 v[234:235], v[224:225], v[234:235] neg_lo:[1,0] neg_hi:[1,0]
v_pk_mul_f32 v[232:233], v[226:227], v[232:233] neg_lo:[1,0] neg_hi:[1,0]
v_mfma_f32_16x16x32_bf16 v[36:39], a[180:183], v[0:3], v[216:219]
ds_read_b128 a[204:207], v253 offset:512
v_cvt_pk_bf16_f32 v180, v134, v135
v_rcp_f32_e64 v164, v164
v_mfma_f32_16x16x32_bf16 v[36:39], a[184:187], v[4:7], v[36:39]
ds_read_b128 a[156:159], v239 offset:36864
v_cvt_pk_bf16_f32 v181, v132, v133
v_rcp_f32_e64 v165, v165
v_mfma_f32_16x16x32_bf16 v[36:39], a[188:191], v[8:11], v[36:39]
ds_read_b128 a[208:211], v253 offset:528
v_cvt_pk_bf16_f32 v184, v138, v139
v_exp_f32_e64 v234, v234
v_mfma_f32_16x16x32_bf16 v[36:39], a[192:195], v[12:15], v[36:39]
ds_read_b128 a[160:163], v239 offset:37888
v_cvt_pk_bf16_f32 v185, v136, v137
v_exp_f32_e64 v235, v235
v_mfma_f32_16x16x32_bf16 v[36:39], a[196:199], v[16:19], v[36:39]
ds_read_b128 a[212:215], v253 offset:640
v_cvt_pk_bf16_f32 v188, v142, v143
v_exp_f32_e64 v232, v232
v_mfma_f32_16x16x32_bf16 v[36:39], a[200:203], v[20:23], v[36:39]
ds_read_b128 a[164:167], v239 offset:38912
v_cvt_pk_bf16_f32 v189, v140, v141
v_exp_f32_e64 v233, v233
v_mfma_f32_16x16x32_bf16 v[40:43], a[200:203], v[68:71], v[216:219]
ds_read_b128 a[216:219], v253 offset:656
v_rcp_f32_e64 v157, v157
v_mfma_f32_16x16x32_bf16 v[40:43], a[196:199], v[64:67], v[40:43]
ds_read_b128 a[168:171], v239 offset:39936
v_rcp_f32_e64 v162, v162
v_mfma_f32_16x16x32_bf16 v[40:43], a[192:195], v[60:63], v[40:43]
ds_read_b128 a[252:255], v254 offset:512
v_rcp_f32_e64 v163, v163
v_mfma_f32_16x16x32_bf16 v[40:43], a[188:191], v[56:59], v[40:43]
ds_read_b128 a[172:175], v239 offset:40960
v_rcp_f32_e64 v160, v160
v_mfma_f32_16x16x32_bf16 v[40:43], a[184:187], v[52:55], v[40:43]
buffer_load_dwordx4 v248, s[12:15], s26 offen offset:1024 sc1 lds
v_mfma_f32_16x16x32_bf16 v[40:43], a[180:183], v[48:51], v[40:43]
ds_read_b128 a[176:179], v239 offset:41984
v_rcp_f32_e64 v161, v161
v_mfma_f32_16x16x32_bf16 v[44:47], a[180:183], v[96:99], v[216:219]
buffer_load_dwordx4 v248, s[12:15], s26 offen offset:2048 sc1 lds
v_mfma_f32_16x16x32_bf16 v[44:47], a[184:187], v[100:103], v[44:47]
s_xor_b32 s63, s63, s36
s_add_u32 m0, s63, 0
s_add_u32 s26, s62, 122880
ds_read_b128 v[216:219], v241 offset:384
v_rcp_f32_e64 v166, v166
v_mfma_f32_16x16x32_bf16 v[44:47], a[188:191], v[104:107], v[44:47]
buffer_load_dwordx4 v248, s[12:15], s26 offen offset:0 sc1 lds
v_mfma_f32_16x16x32_bf16 v[44:47], a[192:195], v[108:111], v[44:47]
v_exp_f32_e64 v86, v86
v_mfma_f32_16x16x32_bf16 v[44:47], a[196:199], v[112:115], v[44:47]
v_exp_f32_e64 v87, v87
v_mfma_f32_16x16x32_bf16 v[44:47], a[200:203], v[116:119], v[44:47]
s_waitcnt lgkmcnt(1)
v_exp_f32 v84, v84
v_mfma_f32_16x16x32_bf16 a[96:99], v[192:195], a[108:111], a[204:207]
buffer_load_dwordx4 v248, s[12:15], s26 offen offset:1024 sc1 lds
v_mfma_f32_16x16x32_bf16 a[108:111], v[196:199], a[108:111], a[208:211]
v_exp_f32_e64 v85, v85
v_mfma_f32_16x16x32_bf16 a[100:103], v[192:195], a[112:115], a[204:207]
v_exp_f32_e64 v90, v90
v_mfma_f32_16x16x32_bf16 a[112:115], v[196:199], a[112:115], a[208:211]
v_exp_f32_e64 v91, v91
v_mfma_f32_16x16x32_bf16 a[104:107], v[192:195], a[116:119], a[204:207]
v_exp_f32_e64 v88, v88
v_mfma_f32_16x16x32_bf16 a[116:119], v[196:199], a[116:119], a[208:211]
v_exp_f32_e64 v89, v89
v_mfma_f32_16x16x32_bf16 a[120:123], v[192:195], a[132:135], a[212:215]
buffer_load_dwordx4 v248, s[12:15], s26 offen offset:2048 sc1 lds
v_mfma_f32_16x16x32_bf16 a[132:135], v[196:199], a[132:135], a[216:219]
v_exp_f32_e64 v94, v94
v_mfma_f32_16x16x32_bf16 a[124:127], v[192:195], a[136:139], a[212:215]
v_exp_f32_e64 v95, v95
v_mfma_f32_16x16x32_bf16 a[136:139], v[196:199], a[136:139], a[216:219]
v_exp_f32_e64 v92, v92
v_mfma_f32_16x16x32_bf16 a[128:131], v[192:195], a[140:143], a[212:215]
v_exp_f32_e64 v93, v93
v_mfma_f32_16x16x32_bf16 a[140:143], v[196:199], a[140:143], a[216:219]
v_rcp_f32_e64 v158, v158
v_mfma_f32_16x16x32_bf16 a[152:155], v[200:203], a[152:155], a[252:255]
s_waitcnt vmcnt(15) lgkmcnt(0)
s_barrier
v_rcp_f32 v159, v159
v_rcp_f32 v156, v156
v_rcp_f32_e64 v167, v167
v_pk_add_f32 v[86:87], v[86:87], 1.0 op_sel_hi:[1,0]
v_pk_add_f32 v[84:85], v[84:85], 1.0 op_sel_hi:[1,0]
v_pk_add_f32 v[90:91], v[90:91], 1.0 op_sel_hi:[1,0]
v_pk_add_f32 v[88:89], v[88:89], 1.0 op_sel_hi:[1,0]
v_pk_add_f32 v[94:95], v[94:95], 1.0 op_sel_hi:[1,0]
v_pk_add_f32 v[92:93], v[92:93], 1.0 op_sel_hi:[1,0]
v_pk_add_f32 v[234:235], v[234:235], 1.0 op_sel_hi:[1,0]
v_pk_add_f32 v[232:233], v[232:233], 1.0 op_sel_hi:[1,0]
v_pk_mul_f32 v[158:159], v[144:145], v[158:159]
v_pk_mul_f32 v[156:157], v[146:147], v[156:157]
v_pk_mul_f32 v[162:163], v[148:149], v[162:163]
v_pk_mul_f32 v[160:161], v[150:151], v[160:161]
v_pk_mul_f32 v[166:167], v[152:153], v[166:167]
v_pk_mul_f32 v[164:165], v[154:155], v[164:165]
v_pk_fma_f32 v[122:123], v[36:37], v[36:37], s[28:29]
v_pk_fma_f32 v[120:121], v[38:39], v[38:39], s[28:29]
v_pk_fma_f32 v[126:127], v[40:41], v[40:41], s[28:29]
v_pk_fma_f32 v[124:125], v[42:43], v[42:43], s[28:29]
v_pk_fma_f32 v[130:131], v[44:45], v[44:45], s[28:29]
v_pk_fma_f32 v[128:129], v[46:47], v[46:47], s[28:29]
v_pk_mul_f32 v[122:123], v[36:37], v[122:123] neg_lo:[1,0] neg_hi:[1,0]
v_pk_mul_f32 v[120:121], v[38:39], v[120:121] neg_lo:[1,0] neg_hi:[1,0]
v_pk_mul_f32 v[126:127], v[40:41], v[126:127] neg_lo:[1,0] neg_hi:[1,0]
v_pk_mul_f32 v[124:125], v[42:43], v[124:125] neg_lo:[1,0] neg_hi:[1,0]
v_pk_mul_f32 v[130:131], v[44:45], v[130:131] neg_lo:[1,0] neg_hi:[1,0]
v_pk_mul_f32 v[128:129], v[46:47], v[128:129] neg_lo:[1,0] neg_hi:[1,0]
v_mfma_f32_16x16x32_bf16 v[72:75], a[156:159], v[0:3], v[216:219]
s_add_u32 m0, s63, 3072
ds_read_b128 a[180:183], v238 offset:0
v_cvt_pk_bf16_f32 v182, v158, v159
v_rcp_f32_e64 v235, v235
v_mfma_f32_16x16x32_bf16 v[72:75], a[160:163], v[4:7], v[72:75]
s_add_u32 s26, s62, 125952
v_exp_f32 v122, v122
v_rcp_f32 v232, v232
v_mfma_f32_16x16x32_bf16 v[72:75], a[164:167], v[8:11], v[72:75]
buffer_load_dwordx4 v248, s[12:15], s26 offen offset:0 sc1 lds
v_rcp_f32_e64 v233, v233
v_mfma_f32_16x16x32_bf16 v[72:75], a[168:171], v[12:15], v[72:75]
ds_read_b128 a[184:187], v238 offset:1024
v_cvt_pk_bf16_f32 v183, v156, v157
v_mfma_f32_16x16x32_bf16 v[72:75], a[172:175], v[16:19], v[72:75]
v_exp_f32_e64 v123, v123
v_mfma_f32_16x16x32_bf16 v[72:75], a[176:179], v[20:23], v[72:75]
v_exp_f32_e64 v120, v120
v_mfma_f32_16x16x32_bf16 v[76:79], a[176:179], v[68:71], v[216:219]
ds_read_b128 a[188:191], v238 offset:2048
v_cvt_pk_bf16_f32 v186, v162, v163
v_mfma_f32_16x16x32_bf16 v[76:79], a[172:175], v[64:67], v[76:79]
v_exp_f32_e64 v121, v121
v_mfma_f32_16x16x32_bf16 v[76:79], a[168:171], v[60:63], v[76:79]
v_exp_f32_e64 v126, v126
v_mfma_f32_16x16x32_bf16 v[76:79], a[164:167], v[56:59], v[76:79]
ds_read_b128 a[192:195], v238 offset:3072
v_cvt_pk_bf16_f32 v187, v160, v161
v_mfma_f32_16x16x32_bf16 v[76:79], a[160:163], v[52:55], v[76:79]
v_exp_f32_e64 v127, v127
v_mfma_f32_16x16x32_bf16 v[76:79], a[156:159], v[48:51], v[76:79]
v_exp_f32_e64 v124, v124
v_mfma_f32_16x16x32_bf16 v[80:83], a[156:159], v[96:99], v[216:219]
ds_read_b128 a[196:199], v238 offset:4096
v_cvt_pk_bf16_f32 v190, v166, v167
v_mfma_f32_16x16x32_bf16 v[80:83], a[160:163], v[100:103], v[80:83]
buffer_load_dwordx4 v248, s[12:15], s26 offen offset:1024 sc1 lds
v_mfma_f32_16x16x32_bf16 v[80:83], a[164:167], v[104:107], v[80:83]
v_exp_f32_e64 v125, v125
v_mfma_f32_16x16x32_bf16 v[80:83], a[168:171], v[108:111], v[80:83]
ds_read_b128 a[200:203], v238 offset:5120
v_cvt_pk_bf16_f32 v191, v164, v165
v_mfma_f32_16x16x32_bf16 v[80:83], a[172:175], v[112:115], v[80:83]
v_exp_f32_e64 v130, v130
v_mfma_f32_16x16x32_bf16 v[80:83], a[176:179], v[116:119], v[80:83]
s_waitcnt lgkmcnt(1)
v_exp_f32 v131, v131
v_mfma_f32_16x16x32_bf16 a[0:3], a[180:183], v[168:171], a[0:3]
ds_read_b128 a[156:159], v239 offset:43008
ds_read_b128 a[160:163], v239 offset:44032
v_mfma_f32_16x16x32_bf16 a[0:3], a[184:187], v[180:183], a[0:3]
v_exp_f32_e64 v128, v128
v_mfma_f32_16x16x32_bf16 a[4:7], a[184:187], v[184:187], a[4:7]
v_exp_f32_e64 v129, v129
v_mfma_f32_16x16x32_bf16 a[4:7], a[180:183], v[172:175], a[4:7]
ds_read_b128 a[164:167], v239 offset:45056
ds_read_b128 a[168:171], v239 offset:46080
v_mfma_f32_16x16x32_bf16 a[8:11], a[180:183], v[176:179], a[8:11]
v_rcp_f32_e64 v86, v86
v_mfma_f32_16x16x32_bf16 a[8:11], a[184:187], v[188:191], a[8:11]
buffer_load_dwordx4 v248, s[12:15], s26 offen offset:2048 sc1 lds
v_mfma_f32_16x16x32_bf16 a[20:23], a[192:195], v[188:191], a[20:23]
ds_read_b128 a[172:175], v239 offset:47104
ds_read_b128 a[176:179], v239 offset:48128
v_mfma_f32_16x16x32_bf16 a[20:23], a[188:191], v[176:179], a[20:23]
v_rcp_f32_e64 v87, v87
v_mfma_f32_16x16x32_bf16 a[16:19], a[188:191], v[172:175], a[16:19]
ds_read_b128 v[216:219], v241 offset:448
v_rcp_f32_e64 v234, v234
v_mfma_f32_16x16x32_bf16 a[16:19], a[192:195], v[184:187], a[16:19]
v_rcp_f32_e64 v84, v84
v_mfma_f32_16x16x32_bf16 a[12:15], a[192:195], v[180:183], a[12:15]
v_rcp_f32_e64 v85, v85
v_mfma_f32_16x16x32_bf16 a[12:15], a[188:191], v[168:171], a[12:15]
v_rcp_f32_e64 v90, v90
v_mfma_f32_16x16x32_bf16 a[24:27], a[196:199], v[168:171], a[24:27]
s_waitcnt lgkmcnt(1)
v_rcp_f32 v91, v91
v_mfma_f32_16x16x32_bf16 a[24:27], a[200:203], v[180:183], a[24:27]
v_rcp_f32_e64 v88, v88
v_mfma_f32_16x16x32_bf16 a[28:31], a[200:203], v[184:187], a[28:31]
v_rcp_f32_e64 v89, v89
v_mfma_f32_16x16x32_bf16 a[28:31], a[196:199], v[172:175], a[28:31]
v_rcp_f32_e64 v94, v94
v_mfma_f32_16x16x32_bf16 a[32:35], a[196:199], v[176:179], a[32:35]
v_rcp_f32 v95, v95
v_mfma_f32_16x16x32_bf16 a[32:35], a[200:203], v[188:191], a[32:35]
s_waitcnt vmcnt(6) lgkmcnt(0)
ds_write_b8 v251, v250 offset:0
v_rcp_f32 v92, v92
v_rcp_f32 v93, v93
v_pk_add_f32 v[122:123], v[122:123], 1.0 op_sel_hi:[1,0]
v_pk_add_f32 v[120:121], v[120:121], 1.0 op_sel_hi:[1,0]
v_pk_add_f32 v[126:127], v[126:127], 1.0 op_sel_hi:[1,0]
v_pk_add_f32 v[124:125], v[124:125], 1.0 op_sel_hi:[1,0]
v_pk_add_f32 v[130:131], v[130:131], 1.0 op_sel_hi:[1,0]
v_pk_add_f32 v[128:129], v[128:129], 1.0 op_sel_hi:[1,0]
v_pk_mul_f32 v[86:87], v[24:25], v[86:87]
v_pk_mul_f32 v[84:85], v[26:27], v[84:85]
v_pk_mul_f32 v[90:91], v[28:29], v[90:91]
v_pk_mul_f32 v[88:89], v[30:31], v[88:89]
v_pk_mul_f32 v[94:95], v[32:33], v[94:95]
v_pk_mul_f32 v[92:93], v[34:35], v[92:93]
v_pk_mul_f32 v[234:235], v[224:225], v[234:235]
v_pk_mul_f32 v[232:233], v[226:227], v[232:233]
v_pk_fma_f32 v[134:135], v[72:73], v[72:73], s[28:29]
v_pk_fma_f32 v[132:133], v[74:75], v[74:75], s[28:29]
v_pk_fma_f32 v[138:139], v[76:77], v[76:77], s[28:29]
v_pk_fma_f32 v[136:137], v[78:79], v[78:79], s[28:29]
v_pk_fma_f32 v[142:143], v[80:81], v[80:81], s[28:29]
v_pk_fma_f32 v[140:141], v[82:83], v[82:83], s[28:29]
v_pk_mul_f32 v[134:135], v[72:73], v[134:135] neg_lo:[1,0] neg_hi:[1,0]
v_pk_mul_f32 v[132:133], v[74:75], v[132:133] neg_lo:[1,0] neg_hi:[1,0]
v_pk_mul_f32 v[138:139], v[76:77], v[138:139] neg_lo:[1,0] neg_hi:[1,0]
v_pk_mul_f32 v[136:137], v[78:79], v[136:137] neg_lo:[1,0] neg_hi:[1,0]
v_pk_mul_f32 v[142:143], v[80:81], v[142:143] neg_lo:[1,0] neg_hi:[1,0]
v_pk_mul_f32 v[140:141], v[82:83], v[140:141] neg_lo:[1,0] neg_hi:[1,0]
.p2align 6
L_loop_x:
v_mfma_f32_16x16x32_bf16 v[144:147], a[156:159], v[0:3], v[216:219]
ds_read_b128 a[180:183], v238 offset:6144
v_cvt_pk_bf16_f32 v192, v86, v87
v_mfma_f32_16x16x32_bf16 v[144:147], a[160:163], v[4:7], v[144:147]
v_cvt_pk_bf16_f32 v236, v234, v235
v_cvt_pk_bf16_f32 v237, v232, v233
v_mfma_f32_16x16x32_bf16 v[144:147], a[164:167], v[8:11], v[144:147]
v_exp_f32_e64 v134, v134
v_mfma_f32_16x16x32_bf16 v[144:147], a[168:171], v[12:15], v[144:147]
ds_read_b128 a[184:187], v238 offset:7168
v_cvt_pk_bf16_f32 v193, v84, v85
v_mfma_f32_16x16x32_bf16 v[144:147], a[172:175], v[16:19], v[144:147]
v_exp_f32_e64 v135, v135
v_mfma_f32_16x16x32_bf16 v[144:147], a[176:179], v[20:23], v[144:147]
v_exp_f32_e64 v132, v132
v_mfma_f32_16x16x32_bf16 v[148:151], a[176:179], v[68:71], v[216:219]
ds_read_b128 a[188:191], v238 offset:8192
v_cvt_pk_bf16_f32 v196, v90, v91
v_mfma_f32_16x16x32_bf16 v[148:151], a[172:175], v[64:67], v[148:151]
v_exp_f32_e64 v133, v133
v_mfma_f32_16x16x32_bf16 v[148:151], a[168:171], v[60:63], v[148:151]
ds_read_b32 v255, v252 offset:0
v_cvt_pk_bf16_f32 v197, v88, v89
v_mfma_f32_16x16x32_bf16 v[148:151], a[164:167], v[56:59], v[148:151]
ds_read_b128 a[192:195], v238 offset:9216
v_cvt_pk_bf16_f32 v200, v94, v95
v_mfma_f32_16x16x32_bf16 v[148:151], a[160:163], v[52:55], v[148:151]
v_exp_f32_e64 v138, v138
v_mfma_f32_16x16x32_bf16 v[148:151], a[156:159], v[48:51], v[148:151]
v_exp_f32_e64 v139, v139
v_mfma_f32_16x16x32_bf16 v[152:155], a[156:159], v[96:99], v[216:219]
ds_read_b128 a[196:199], v238 offset:10240
v_cvt_pk_bf16_f32 v201, v92, v93
v_mfma_f32_16x16x32_bf16 v[152:155], a[160:163], v[100:103], v[152:155]
v_exp_f32_e64 v136, v136
v_mfma_f32_16x16x32_bf16 v[152:155], a[164:167], v[104:107], v[152:155]
v_exp_f32_e64 v137, v137
v_mfma_f32_16x16x32_bf16 v[152:155], a[168:171], v[108:111], v[152:155]
s_waitcnt lgkmcnt(1)
ds_read_b128 a[200:203], v238 offset:11264
v_cmp_ne_u32 vcc, s33, v255
v_mfma_f32_16x16x32_bf16 v[152:155], a[172:175], v[112:115], v[152:155]
v_exp_f32_e64 v142, v142
v_mfma_f32_16x16x32_bf16 v[152:155], a[176:179], v[116:119], v[152:155]
s_cbranch_vccnz L_sy_x7
L_syb_x7:
v_exp_f32 v143, v143
v_mfma_f32_16x16x32_bf16 a[36:39], a[180:183], v[168:171], a[36:39]
ds_read_b128 a[156:159], v239 offset:0
ds_read_b128 a[160:163], v239 offset:1024
v_mfma_f32_16x16x32_bf16 a[36:39], a[184:187], v[180:183], a[36:39]
ds_write_b64 v240, v[236:237] offset:2048
v_rcp_f32_e64 v131, v131
v_mfma_f32_16x16x32_bf16 a[40:43], a[184:187], v[184:187], a[40:43]
v_exp_f32_e64 v140, v140
v_mfma_f32_16x16x32_bf16 a[40:43], a[180:183], v[172:175], a[40:43]
ds_read_b128 a[164:167], v239 offset:2048
ds_read_b128 a[168:171], v239 offset:3072
v_mfma_f32_16x16x32_bf16 a[44:47], a[180:183], v[176:179], a[44:47]
v_exp_f32_e64 v141, v141
v_mfma_f32_16x16x32_bf16 a[44:47], a[184:187], v[188:191], a[44:47]
v_rcp_f32_e64 v122, v122
v_mfma_f32_16x16x32_bf16 a[56:59], a[192:195], v[188:191], a[56:59]
ds_read_b128 a[172:175], v239 offset:4096
ds_read_b128 a[176:179], v239 offset:5120
v_mfma_f32_16x16x32_bf16 a[56:59], a[188:191], v[176:179], a[56:59]
s_add_u32 m0, s23, 129024
v_rcp_f32_e64 v123, v123
v_mfma_f32_16x16x32_bf16 a[52:55], a[188:191], v[172:175], a[52:55]
s_add_u32 s26, s62, 147456
ds_read_b128 v[216:219], v241 offset:512
v_rcp_f32_e64 v128, v128
v_mfma_f32_16x16x32_bf16 a[52:55], a[192:195], v[184:187], a[52:55]
buffer_load_dwordx4 v248, s[12:15], s26 offen offset:0 sc1 lds
v_mfma_f32_16x16x32_bf16 a[48:51], a[192:195], v[180:183], a[48:51]
v_rcp_f32_e64 v120, v120
v_mfma_f32_16x16x32_bf16 a[48:51], a[188:191], v[168:171], a[48:51]
s_waitcnt lgkmcnt(1)
v_rcp_f32 v121, v121
v_mfma_f32_16x16x32_bf16 a[60:63], a[196:199], v[168:171], a[60:63]
buffer_load_dwordx4 v248, s[12:15], s26 offen offset:1024 sc1 lds
v_mfma_f32_16x16x32_bf16 a[60:63], a[200:203], v[180:183], a[60:63]
v_rcp_f32_e64 v126, v126
v_mfma_f32_16x16x32_bf16 a[64:67], a[200:203], v[184:187], a[64:67]
v_rcp_f32_e64 v127, v127
v_mfma_f32_16x16x32_bf16 a[64:67], a[196:199], v[172:175], a[64:67]
buffer_load_dwordx4 v248, s[12:15], s26 offen offset:2048 sc1 lds
v_mfma_f32_16x16x32_bf16 a[68:71], a[196:199], v[176:179], a[68:71]
v_rcp_f32_e64 v124, v124
v_mfma_f32_16x16x32_bf16 a[68:71], a[200:203], v[188:191], a[68:71]
s_waitcnt lgkmcnt(0)
v_rcp_f32 v125, v125
v_rcp_f32 v130, v130
v_rcp_f32 v129, v129
v_pk_add_f32 v[134:135], v[134:135], 1.0 op_sel_hi:[1,0]
v_pk_add_f32 v[132:133], v[132:133], 1.0 op_sel_hi:[1,0]
v_pk_add_f32 v[138:139], v[138:139], 1.0 op_sel_hi:[1,0]
v_pk_add_f32 v[136:137], v[136:137], 1.0 op_sel_hi:[1,0]
v_pk_add_f32 v[142:143], v[142:143], 1.0 op_sel_hi:[1,0]
v_pk_add_f32 v[140:141], v[140:141], 1.0 op_sel_hi:[1,0]
v_pk_mul_f32 v[122:123], v[36:37], v[122:123]
v_pk_mul_f32 v[120:121], v[38:39], v[120:121]
v_pk_mul_f32 v[126:127], v[40:41], v[126:127]
v_pk_mul_f32 v[124:125], v[42:43], v[124:125]
v_pk_mul_f32 v[130:131], v[44:45], v[130:131]
v_pk_mul_f32 v[128:129], v[46:47], v[128:129]
v_pk_fma_f32 v[158:159], v[144:145], v[144:145], s[28:29]
v_pk_fma_f32 v[156:157], v[146:147], v[146:147], s[28:29]
v_pk_fma_f32 v[162:163], v[148:149], v[148:149], s[28:29]
v_pk_fma_f32 v[160:161], v[150:151], v[150:151], s[28:29]
v_pk_fma_f32 v[166:167], v[152:153], v[152:153], s[28:29]
v_pk_fma_f32 v[164:165], v[154:155], v[154:155], s[28:29]
v_pk_mul_f32 v[158:159], v[144:145], v[158:159] neg_lo:[1,0] neg_hi:[1,0]
v_pk_mul_f32 v[156:157], v[146:147], v[156:157] neg_lo:[1,0] neg_hi:[1,0]
v_pk_mul_f32 v[162:163], v[148:149], v[162:163] neg_lo:[1,0] neg_hi:[1,0]
v_pk_mul_f32 v[160:161], v[150:151], v[160:161] neg_lo:[1,0] neg_hi:[1,0]
v_pk_mul_f32 v[166:167], v[152:153], v[166:167] neg_lo:[1,0] neg_hi:[1,0]
v_pk_mul_f32 v[164:165], v[154:155], v[164:165] neg_lo:[1,0] neg_hi:[1,0]
v_mfma_f32_16x16x32_bf16 v[24:27], a[156:159], v[0:3], v[216:219]
ds_read_b128 a[180:183], v238 offset:12288
ds_read_b128 v[228:231], v239 offset:53248
v_mfma_f32_16x16x32_bf16 v[24:27], a[160:163], v[4:7], v[24:27]
s_add_u32 s33, s33, 0x01010101
ds_read_b128 a[204:207], v243 offset:0
ds_read_b128 v[244:247], v239 offset:54272
v_mfma_f32_16x16x32_bf16 v[24:27], a[164:167], v[8:11], v[24:27]
v_add_u32_e64 v250, 1, v250
v_cvt_pk_bf16_f32 v194, v122, v123
v_mfma_f32_16x16x32_bf16 v[24:27], a[168:171], v[12:15], v[24:27]
ds_read_b128 a[184:187], v238 offset:13312
ds_read_b128 a[208:211], v243 offset:1024
v_mfma_f32_16x16x32_bf16 v[24:27], a[172:175], v[16:19], v[24:27]
v_exp_f32_e64 v158, v158
v_mfma_f32_16x16x32_bf16 v[24:27], a[176:179], v[20:23], v[24:27]
ds_read_b128 a[212:215], v243 offset:2048
v_cvt_pk_bf16_f32 v195, v120, v121
v_mfma_f32_16x16x32_bf16 v[28:31], a[176:179], v[68:71], v[216:219]
ds_read_b128 a[188:191], v238 offset:14336
v_cvt_pk_bf16_f32 v198, v126, v127
v_mfma_f32_16x16x32_bf16 v[28:31], a[172:175], v[64:67], v[28:31]
ds_read_b128 a[216:219], v243 offset:3072
v_cvt_pk_bf16_f32 v199, v124, v125
v_mfma_f32_16x16x32_bf16 v[28:31], a[168:171], v[60:63], v[28:31]
v_exp_f32_e64 v159, v159
v_mfma_f32_16x16x32_bf16 v[28:31], a[164:167], v[56:59], v[28:31]
ds_read_b128 a[192:195], v238 offset:15360
ds_read_b128 a[220:223], v243 offset:4096
v_mfma_f32_16x16x32_bf16 v[28:31], a[160:163], v[52:55], v[28:31]
v_exp_f32_e64 v156, v156
v_mfma_f32_16x16x32_bf16 v[28:31], a[156:159], v[48:51], v[28:31]
ds_read_b128 a[224:227], v243 offset:5120
v_cvt_pk_bf16_f32 v202, v130, v131
v_mfma_f32_16x16x32_bf16 v[32:35], a[156:159], v[96:99], v[216:219]
ds_read_b128 a[196:199], v238 offset:16384
v_cvt_pk_bf16_f32 v203, v128, v129
v_mfma_f32_16x16x32_bf16 v[32:35], a[160:163], v[100:103], v[32:35]
ds_read_b128 v[220:223], v242 offset:512
v_rcp_f32_e64 v143, v143
v_mfma_f32_16x16x32_bf16 v[32:35], a[164:167], v[104:107], v[32:35]
v_exp_f32_e64 v157, v157
v_mfma_f32_16x16x32_bf16 v[32:35], a[168:171], v[108:111], v[32:35]
ds_read_b128 a[200:203], v238 offset:17408
v_rcp_f32_e64 v140, v140
v_mfma_f32_16x16x32_bf16 v[32:35], a[172:175], v[112:115], v[32:35]
v_exp_f32_e64 v162, v162
v_mfma_f32_16x16x32_bf16 v[32:35], a[176:179], v[116:119], v[32:35]
s_waitcnt lgkmcnt(1)
v_exp_f32 v163, v163
v_mfma_f32_16x16x32_bf16 v[224:227], a[204:207], a[228:231], v[220:223]
v_exp_f32_e64 v160, v160
v_mfma_f32_16x16x32_bf16 v[224:227], a[208:211], a[232:235], v[224:227]
v_exp_f32_e64 v161, v161
v_mfma_f32_16x16x32_bf16 v[224:227], a[212:215], a[236:239], v[224:227]
v_exp_f32_e64 v166, v166
v_mfma_f32_16x16x32_bf16 v[224:227], a[216:219], a[240:243], v[224:227]
v_exp_f32_e64 v167, v167
v_mfma_f32_16x16x32_bf16 v[224:227], a[220:223], a[244:247], v[224:227]
v_exp_f32_e64 v164, v164
v_mfma_f32_16x16x32_bf16 v[224:227], a[224:227], a[248:251], v[224:227]
v_exp_f32_e64 v165, v165
v_mfma_f32_16x16x32_bf16 a[72:75], a[180:183], v[168:171], a[72:75]
ds_read_b128 a[156:159], v239 offset:6144
ds_read_b128 a[160:163], v239 offset:7168
v_mfma_f32_16x16x32_bf16 a[72:75], a[184:187], v[180:183], a[72:75]
v_rcp_f32_e64 v134, v134
v_mfma_f32_16x16x32_bf16 a[76:79], a[184:187], v[184:187], a[76:79]
v_rcp_f32_e64 v135, v135
v_mfma_f32_16x16x32_bf16 a[76:79], a[180:183], v[172:175], a[76:79]
ds_read_b128 a[164:167], v239 offset:8192
ds_read_b128 a[168:171], v239 offset:9216
v_mfma_f32_16x16x32_bf16 a[80:83], a[180:183], v[176:179], a[80:83]
ds_read_b128 a[208:211], v249 offset:1024
ds_read_b128 a[212:215], v249 offset:8192
v_mfma_f32_16x16x32_bf16 a[80:83], a[184:187], v[188:191], a[80:83]
v_rcp_f32_e64 v132, v132
v_mfma_f32_16x16x32_bf16 a[92:95], a[192:195], v[188:191], a[92:95]
ds_read_b128 a[172:175], v239 offset:10240
ds_read_b128 a[176:179], v239 offset:11264
v_mfma_f32_16x16x32_bf16 a[92:95], a[188:191], v[176:179], a[92:95]
s_add_u32 m0, s23, 132096
ds_read_b128 a[216:219], v249 offset:9216
ds_read_b128 a[220:223], v249 offset:16384
v_mfma_f32_16x16x32_bf16 a[88:91], a[188:191], v[172:175], a[88:91]
s_add_u32 s26, s62, 150528
ds_read_b128 v[216:219], v241 offset:576
ds_read_b128 a[204:207], v249 offset:0
v_mfma_f32_16x16x32_bf16 a[88:91], a[192:195], v[184:187], a[88:91]
buffer_load_dwordx4 v248, s[12:15], s26 offen offset:0 sc1 lds
v_mfma_f32_16x16x32_bf16 a[84:87], a[192:195], v[180:183], a[84:87]
v_rcp_f32_e64 v133, v133
v_mfma_f32_16x16x32_bf16 a[84:87], a[188:191], v[168:171], a[84:87]
buffer_load_dwordx4 v248, s[12:15], s26 offen offset:1024 sc1 lds
v_mfma_f32_16x16x32_bf16 a[96:99], a[196:199], v[168:171], a[96:99]
s_waitcnt lgkmcnt(2)
ds_read_b128 a[224:227], v249 offset:17408
v_rcp_f32 v141, v141
v_mfma_f32_16x16x32_bf16 a[96:99], a[200:203], v[180:183], a[96:99]
v_rcp_f32_e64 v138, v138
v_mfma_f32_16x16x32_bf16 a[100:103], a[200:203], v[184:187], a[100:103]
v_rcp_f32_e64 v139, v139
v_mfma_f32_16x16x32_bf16 a[100:103], a[196:199], v[172:175], a[100:103]
buffer_load_dwordx4 v248, s[12:15], s26 offen offset:2048 sc1 lds
v_mfma_f32_16x16x32_bf16 a[104:107], a[196:199], v[176:179], a[104:107]
v_rcp_f32_e64 v136, v136
v_mfma_f32_16x16x32_bf16 a[104:107], a[200:203], v[188:191], a[104:107]
s_waitcnt lgkmcnt(0)
v_rcp_f32 v137, v137
v_rcp_f32 v142, v142
v_add_f32 v158, 1.0, v158
v_add_f32_e64 v159, 1.0, v159
v_pk_add_f32 v[156:157], v[156:157], 1.0 op_sel_hi:[1,0]
v_pk_add_f32 v[162:163], v[162:163], 1.0 op_sel_hi:[1,0]
v_pk_add_f32 v[160:161], v[160:161], 1.0 op_sel_hi:[1,0]
v_pk_add_f32 v[166:167], v[166:167], 1.0 op_sel_hi:[1,0]
v_pk_add_f32 v[164:165], v[164:165], 1.0 op_sel_hi:[1,0]
v_pk_mul_f32 v[134:135], v[72:73], v[134:135]
v_pk_mul_f32 v[132:133], v[74:75], v[132:133]
v_pk_mul_f32 v[138:139], v[76:77], v[138:139]
v_pk_mul_f32 v[136:137], v[78:79], v[136:137]
v_pk_mul_f32 v[142:143], v[80:81], v[142:143]
v_pk_mul_f32 v[140:141], v[82:83], v[140:141]
v_pk_fma_f32 v[86:87], v[24:25], v[24:25], s[28:29]
v_pk_fma_f32 v[84:85], v[26:27], v[26:27], s[28:29]
v_pk_fma_f32 v[90:91], v[28:29], v[28:29], s[28:29]
v_pk_fma_f32 v[88:89], v[30:31], v[30:31], s[28:29]
v_pk_fma_f32 v[94:95], v[32:33], v[32:33], s[28:29]
v_pk_fma_f32 v[92:93], v[34:35], v[34:35], s[28:29]
v_pk_fma_f32 v[234:235], v[224:225], v[224:225], s[28:29]
v_pk_fma_f32 v[232:233], v[226:227], v[226:227], s[28:29]
v_pk_mul_f32 v[86:87], v[24:25], v[86:87] neg_lo:[1,0] neg_hi:[1,0]
v_pk_mul_f32 v[84:85], v[26:27], v[84:85] neg_lo:[1,0] neg_hi:[1,0]
v_pk_mul_f32 v[90:91], v[28:29], v[90:91] neg_lo:[1,0] neg_hi:[1,0]
v_pk_mul_f32 v[88:89], v[30:31], v[88:89] neg_lo:[1,0] neg_hi:[1,0]
v_pk_mul_f32 v[94:95], v[32:33], v[94:95] neg_lo:[1,0] neg_hi:[1,0]
v_pk_mul_f32 v[92:93], v[34:35], v[92:93] neg_lo:[1,0] neg_hi:[1,0]
v_pk_mul_f32 v[234:235], v[224:225], v[234:235] neg_lo:[1,0] neg_hi:[1,0]
v_pk_mul_f32 v[232:233], v[226:227], v[232:233] neg_lo:[1,0] neg_hi:[1,0]
v_mfma_f32_16x16x32_bf16 v[36:39], a[156:159], v[0:3], v[216:219]
ds_read_b128 a[180:183], v238 offset:18432
v_cvt_pk_bf16_f32 v204, v134, v135
v_mfma_f32_16x16x32_bf16 v[36:39], a[160:163], v[4:7], v[36:39]
v_exp_f32_e64 v86, v86
v_mfma_f32_16x16x32_bf16 v[36:39], a[164:167], v[8:11], v[36:39]
v_exp_f32_e64 v87, v87
v_mfma_f32_16x16x32_bf16 v[36:39], a[168:171], v[12:15], v[36:39]
ds_read_b128 a[184:187], v238 offset:19456
v_cvt_pk_bf16_f32 v205, v132, v133
v_mfma_f32_16x16x32_bf16 v[36:39], a[172:175], v[16:19], v[36:39]
v_exp_f32_e64 v84, v84
v_mfma_f32_16x16x32_bf16 v[36:39], a[176:179], v[20:23], v[36:39]
v_exp_f32_e64 v85, v85
v_mfma_f32_16x16x32_bf16 v[40:43], a[176:179], v[68:71], v[216:219]
ds_read_b128 a[188:191], v238 offset:20480
v_cvt_pk_bf16_f32 v208, v138, v139
v_mfma_f32_16x16x32_bf16 v[40:43], a[172:175], v[64:67], v[40:43]
v_exp_f32_e64 v90, v90
v_mfma_f32_16x16x32_bf16 v[40:43], a[168:171], v[60:63], v[40:43]
v_exp_f32_e64 v91, v91
v_mfma_f32_16x16x32_bf16 v[40:43], a[164:167], v[56:59], v[40:43]
ds_read_b128 a[192:195], v238 offset:21504
v_cvt_pk_bf16_f32 v209, v136, v137
v_mfma_f32_16x16x32_bf16 v[40:43], a[160:163], v[52:55], v[40:43]
v_exp_f32_e64 v88, v88
v_mfma_f32_16x16x32_bf16 v[40:43], a[156:159], v[48:51], v[40:43]
v_exp_f32_e64 v89, v89
v_mfma_f32_16x16x32_bf16 v[44:47], a[156:159], v[96:99], v[216:219]
ds_read_b128 a[196:199], v238 offset:22528
v_cvt_pk_bf16_f32 v212, v142, v143
v_mfma_f32_16x16x32_bf16 v[44:47], a[160:163], v[100:103], v[44:47]
v_exp_f32_e64 v94, v94
v_mfma_f32_16x16x32_bf16 v[44:47], a[164:167], v[104:107], v[44:47]
v_exp_f32_e64 v95, v95
v_mfma_f32_16x16x32_bf16 v[44:47], a[168:171], v[108:111], v[44:47]
ds_read_b128 a[200:203], v238 offset:23552
v_cvt_pk_bf16_f32 v213, v140, v141
v_mfma_f32_16x16x32_bf16 v[44:47], a[172:175], v[112:115], v[44:47]
v_exp_f32_e64 v92, v92
v_mfma_f32_16x16x32_bf16 v[44:47], a[176:179], v[116:119], v[44:47]
s_waitcnt lgkmcnt(1)
v_exp_f32 v93, v93
v_mfma_f32_16x16x32_bf16 a[108:111], a[180:183], v[168:171], a[108:111]
ds_read_b128 a[156:159], v239 offset:12288
ds_read_b128 a[160:163], v239 offset:13312
v_mfma_f32_16x16x32_bf16 a[108:111], a[184:187], v[180:183], a[108:111]
v_rcp_f32_e64 v158, v158
v_mfma_f32_16x16x32_bf16 a[112:115], a[184:187], v[184:187], a[112:115]
v_rcp_f32_e64 v159, v159
v_mfma_f32_16x16x32_bf16 a[112:115], a[180:183], v[172:175], a[112:115]
ds_read_b128 a[164:167], v239 offset:14336
ds_read_b128 a[168:171], v239 offset:15360
v_mfma_f32_16x16x32_bf16 a[116:119], a[180:183], v[176:179], a[116:119]
v_rcp_f32_e64 v156, v156
v_mfma_f32_16x16x32_bf16 a[116:119], a[184:187], v[188:191], a[116:119]
v_rcp_f32_e64 v157, v157
v_mfma_f32_16x16x32_bf16 a[128:131], a[192:195], v[188:191], a[128:131]
ds_read_b128 a[172:175], v239 offset:16384
ds_read_b128 a[176:179], v239 offset:17408
v_mfma_f32_16x16x32_bf16 a[128:131], a[188:191], v[176:179], a[128:131]
s_add_u32 m0, s63, 24576
v_rcp_f32_e64 v162, v162
v_mfma_f32_16x16x32_bf16 a[124:127], a[188:191], v[172:175], a[124:127]
s_add_u32 s26, s62, 172032
ds_read_b128 v[216:219], v241 offset:640
v_add_f32_e64 v86, 1.0, v86
v_mfma_f32_16x16x32_bf16 a[124:127], a[192:195], v[184:187], a[124:127]
buffer_load_dwordx4 v248, s[12:15], s26 offen offset:0 sc1 lds
v_mfma_f32_16x16x32_bf16 a[120:123], a[192:195], v[180:183], a[120:123]
v_rcp_f32_e64 v163, v163
v_mfma_f32_16x16x32_bf16 a[120:123], a[188:191], v[168:171], a[120:123]
v_rcp_f32 v160, v160
v_mfma_f32_16x16x32_bf16 a[132:135], a[196:199], v[168:171], a[132:135]
s_waitcnt lgkmcnt(1)
buffer_load_dwordx4 v248, s[12:15], s26 offen offset:1024 sc1 lds
v_mfma_f32_16x16x32_bf16 a[132:135], a[200:203], v[180:183], a[132:135]
v_rcp_f32_e64 v161, v161
v_mfma_f32_16x16x32_bf16 a[136:139], a[200:203], v[184:187], a[136:139]
v_rcp_f32_e64 v166, v166
v_mfma_f32_16x16x32_bf16 a[136:139], a[196:199], v[172:175], a[136:139]
buffer_load_dwordx4 v248, s[12:15], s26 offen offset:2048 sc1 lds
v_mfma_f32_16x16x32_bf16 a[140:143], a[196:199], v[176:179], a[140:143]
v_rcp_f32_e64 v167, v167
v_mfma_f32_16x16x32_bf16 a[140:143], a[200:203], v[188:191], a[140:143]
v_rcp_f32_e64 v164, v164
v_mfma_f32_16x16x32_bf16 a[144:147], a[204:207], v[228:231], a[144:147]
v_rcp_f32_e64 v165, v165
v_mfma_f32_16x16x32_bf16 a[144:147], a[208:211], v[244:247], a[144:147]
v_exp_f32_e64 v234, v234
v_mfma_f32_16x16x32_bf16 a[148:151], a[216:219], v[244:247], a[148:151]
v_exp_f32_e64 v235, v235
v_mfma_f32_16x16x32_bf16 a[148:151], a[212:215], v[228:231], a[148:151]
s_waitcnt lgkmcnt(0)
v_exp_f32 v232, v232
v_exp_f32 v233, v233
v_add_f32 v87, 1.0, v87
v_pk_add_f32 v[84:85], v[84:85], 1.0 op_sel_hi:[1,0]
v_pk_add_f32 v[90:91], v[90:91], 1.0 op_sel_hi:[1,0]
v_pk_add_f32 v[88:89], v[88:89], 1.0 op_sel_hi:[1,0]
v_pk_add_f32 v[94:95], v[94:95], 1.0 op_sel_hi:[1,0]
v_pk_add_f32 v[92:93], v[92:93], 1.0 op_sel_hi:[1,0]
v_pk_add_f32 v[234:235], v[234:235], 1.0 op_sel_hi:[1,0]
v_pk_add_f32 v[232:233], v[232:233], 1.0 op_sel_hi:[1,0]
v_pk_mul_f32 v[158:159], v[144:145], v[158:159]
v_pk_mul_f32 v[156:157], v[146:147], v[156:157]
v_pk_mul_f32 v[162:163], v[148:149], v[162:163]
v_pk_mul_f32 v[160:161], v[150:151], v[160:161]
v_pk_mul_f32 v[166:167], v[152:153], v[166:167]
v_pk_mul_f32 v[164:165], v[154:155], v[164:165]
v_pk_fma_f32 v[122:123], v[36:37], v[36:37], s[28:29]
v_pk_fma_f32 v[120:121], v[38:39], v[38:39], s[28:29]
v_pk_fma_f32 v[126:127], v[40:41], v[40:41], s[28:29]
v_pk_fma_f32 v[124:125], v[42:43], v[42:43], s[28:29]
v_pk_fma_f32 v[130:131], v[44:45], v[44:45], s[28:29]
v_pk_fma_f32 v[128:129], v[46:47], v[46:47], s[28:29]
v_pk_mul_f32 v[122:123], v[36:37], v[122:123] neg_lo:[1,0] neg_hi:[1,0]
v_pk_mul_f32 v[120:121], v[38:39], v[120:121] neg_lo:[1,0] neg_hi:[1,0]
v_pk_mul_f32 v[126:127], v[40:41], v[126:127] neg_lo:[1,0] neg_hi:[1,0]
v_pk_mul_f32 v[124:125], v[42:43], v[124:125] neg_lo:[1,0] neg_hi:[1,0]
v_pk_mul_f32 v[130:131], v[44:45], v[130:131] neg_lo:[1,0] neg_hi:[1,0]
v_pk_mul_f32 v[128:129], v[46:47], v[128:129] neg_lo:[1,0] neg_hi:[1,0]
v_mfma_f32_16x16x32_bf16 v[72:75], a[156:159], v[0:3], v[216:219]
ds_read_b128 a[180:183], v238 offset:24576
v_cvt_pk_bf16_f32 v206, v158, v159
v_rcp_f32_e64 v233, v233
v_mfma_f32_16x16x32_bf16 v[72:75], a[160:163], v[4:7], v[72:75]
v_exp_f32_e64 v122, v122
v_mfma_f32_16x16x32_bf16 v[72:75], a[164:167], v[8:11], v[72:75]
v_exp_f32_e64 v123, v123
v_mfma_f32_16x16x32_bf16 v[72:75], a[168:171], v[12:15], v[72:75]
ds_read_b128 a[184:187], v238 offset:25600
v_cvt_pk_bf16_f32 v207, v156, v157
v_mfma_f32_16x16x32_bf16 v[72:75], a[172:175], v[16:19], v[72:75]
v_exp_f32_e64 v120, v120
v_mfma_f32_16x16x32_bf16 v[72:75], a[176:179], v[20:23], v[72:75]
v_exp_f32_e64 v121, v121
v_mfma_f32_16x16x32_bf16 v[76:79], a[176:179], v[68:71], v[216:219]
ds_read_b128 a[188:191], v238 offset:26624
v_cvt_pk_bf16_f32 v210, v162, v163
v_mfma_f32_16x16x32_bf16 v[76:79], a[172:175], v[64:67], v[76:79]
v_exp_f32_e64 v126, v126
v_mfma_f32_16x16x32_bf16 v[76:79], a[168:171], v[60:63], v[76:79]
v_exp_f32_e64 v127, v127
v_mfma_f32_16x16x32_bf16 v[76:79], a[164:167], v[56:59], v[76:79]
ds_read_b128 a[192:195], v238 offset:27648
v_cvt_pk_bf16_f32 v211, v160, v161
v_mfma_f32_16x16x32_bf16 v[76:79], a[160:163], v[52:55], v[76:79]
v_exp_f32_e64 v124, v124
v_mfma_f32_16x16x32_bf16 v[76:79], a[156:159], v[48:51], v[76:79]
v_exp_f32_e64 v125, v125
v_mfma_f32_16x16x32_bf16 v[80:83], a[156:159], v[96:99], v[216:219]
ds_read_b128 a[196:199], v238 offset:28672
v_cvt_pk_bf16_f32 v214, v166, v167
v_mfma_f32_16x16x32_bf16 v[80:83], a[160:163], v[100:103], v[80:83]
v_exp_f32_e64 v130, v130
v_mfma_f32_16x16x32_bf16 v[80:83], a[164:167], v[104:107], v[80:83]
v_exp_f32_e64 v131, v131
v_mfma_f32_16x16x32_bf16 v[80:83], a[168:171], v[108:111], v[80:83]
ds_read_b128 a[200:203], v238 offset:29696
v_cvt_pk_bf16_f32 v215, v164, v165
v_mfma_f32_16x16x32_bf16 v[80:83], a[172:175], v[112:115], v[80:83]
v_exp_f32_e64 v128, v128
v_mfma_f32_16x16x32_bf16 v[80:83], a[176:179], v[116:119], v[80:83]
s_waitcnt lgkmcnt(1)
v_exp_f32 v129, v129
v_mfma_f32_16x16x32_bf16 a[0:3], a[180:183], v[192:195], a[0:3]
ds_read_b128 a[156:159], v239 offset:18432
ds_read_b128 a[160:163], v239 offset:19456
v_mfma_f32_16x16x32_bf16 a[0:3], a[184:187], v[204:207], a[0:3]
v_rcp_f32_e64 v86, v86
v_mfma_f32_16x16x32_bf16 a[4:7], a[184:187], v[208:211], a[4:7]
v_rcp_f32_e64 v87, v87
v_mfma_f32_16x16x32_bf16 a[4:7], a[180:183], v[196:199], a[4:7]
ds_read_b128 a[164:167], v239 offset:20480
ds_read_b128 a[168:171], v239 offset:21504
v_mfma_f32_16x16x32_bf16 a[8:11], a[180:183], v[200:203], a[8:11]
v_rcp_f32_e64 v84, v84
v_mfma_f32_16x16x32_bf16 a[8:11], a[184:187], v[212:215], a[8:11]
v_rcp_f32_e64 v85, v85
v_mfma_f32_16x16x32_bf16 a[20:23], a[192:195], v[212:215], a[20:23]
ds_read_b128 a[172:175], v239 offset:22528
ds_read_b128 a[176:179], v239 offset:23552
v_mfma_f32_16x16x32_bf16 a[20:23], a[188:191], v[200:203], a[20:23]
s_add_u32 m0, s63, 27648
v_rcp_f32_e64 v90, v90
v_mfma_f32_16x16x32_bf16 a[16:19], a[188:191], v[196:199], a[16:19]
s_add_u32 s26, s62, 175104
ds_read_b128 v[216:219], v241 offset:704
v_rcp_f32_e64 v232, v232
v_mfma_f32_16x16x32_bf16 a[16:19], a[192:195], v[208:211], a[16:19]
buffer_load_dwordx4 v248, s[12:15], s26 offen offset:0 sc1 lds
v_mfma_f32_16x16x32_bf16 a[12:15], a[192:195], v[204:207], a[12:15]
v_rcp_f32_e64 v91, v91
v_mfma_f32_16x16x32_bf16 a[12:15], a[188:191], v[192:195], a[12:15]
v_rcp_f32 v88, v88
v_mfma_f32_16x16x32_bf16 a[24:27], a[196:199], v[192:195], a[24:27]
s_waitcnt lgkmcnt(1)
buffer_load_dwordx4 v248, s[12:15], s26 offen offset:1024 sc1 lds
v_mfma_f32_16x16x32_bf16 a[24:27], a[200:203], v[204:207], a[24:27]
v_rcp_f32_e64 v89, v89
v_mfma_f32_16x16x32_bf16 a[28:31], a[200:203], v[208:211], a[28:31]
v_rcp_f32_e64 v94, v94
v_mfma_f32_16x16x32_bf16 a[28:31], a[196:199], v[196:199], a[28:31]
buffer_load_dwordx4 v248, s[12:15], s26 offen offset:2048 sc1 lds
v_mfma_f32_16x16x32_bf16 a[32:35], a[196:199], v[200:203], a[32:35]
v_rcp_f32_e64 v95, v95
v_mfma_f32_16x16x32_bf16 a[32:35], a[200:203], v[212:215], a[32:35]
v_rcp_f32_e64 v92, v92
v_mfma_f32_16x16x32_bf16 a[152:155], a[220:223], v[228:231], a[152:155]
v_rcp_f32 v93, v93
v_mfma_f32_16x16x32_bf16 a[152:155], a[224:227], v[244:247], a[152:155]
s_waitcnt vmcnt(6) lgkmcnt(0)
ds_write_b8 v251, v250 offset:4
v_rcp_f32 v234, v234
v_rcp_f32 v235, v235
v_pk_add_f32 v[122:123], v[122:123], 1.0 op_sel_hi:[1,0]
v_pk_add_f32 v[120:121], v[120:121], 1.0 op_sel_hi:[1,0]
v_pk_add_f32 v[126:127], v[126:127], 1.0 op_sel_hi:[1,0]
v_pk_add_f32 v[124:125], v[124:125], 1.0 op_sel_hi:[1,0]
v_pk_add_f32 v[130:131], v[130:131], 1.0 op_sel_hi:[1,0]
v_pk_add_f32 v[128:129], v[128:129], 1.0 op_sel_hi:[1,0]
v_pk_mul_f32 v[86:87], v[24:25], v[86:87]
v_pk_mul_f32 v[84:85], v[26:27], v[84:85]
v_pk_mul_f32 v[90:91], v[28:29], v[90:91]
v_pk_mul_f32 v[88:89], v[30:31], v[88:89]
v_pk_mul_f32 v[94:95], v[32:33], v[94:95]
v_pk_mul_f32 v[92:93], v[34:35], v[92:93]
v_pk_mul_f32 v[234:235], v[224:225], v[234:235]
v_pk_mul_f32 v[232:233], v[226:227], v[232:233]
v_pk_fma_f32 v[134:135], v[72:73], v[72:73], s[28:29]
v_pk_fma_f32 v[132:133], v[74:75], v[74:75], s[28:29]
v_pk_fma_f32 v[138:139], v[76:77], v[76:77], s[28:29]
v_pk_fma_f32 v[136:137], v[78:79], v[78:79], s[28:29]
v_pk_fma_f32 v[142:143], v[80:81], v[80:81], s[28:29]
v_pk_fma_f32 v[140:141], v[82:83], v[82:83], s[28:29]
v_pk_mul_f32 v[134:135], v[72:73], v[134:135] neg_lo:[1,0] neg_hi:[1,0]
v_pk_mul_f32 v[132:133], v[74:75], v[132:133] neg_lo:[1,0] neg_hi:[1,0]
v_pk_mul_f32 v[138:139], v[76:77], v[138:139] neg_lo:[1,0] neg_hi:[1,0]
v_pk_mul_f32 v[136:137], v[78:79], v[136:137] neg_lo:[1,0] neg_hi:[1,0]
v_pk_mul_f32 v[142:143], v[80:81], v[142:143] neg_lo:[1,0] neg_hi:[1,0]
v_pk_mul_f32 v[140:141], v[82:83], v[140:141] neg_lo:[1,0] neg_hi:[1,0]
v_mfma_f32_16x16x32_bf16 v[144:147], a[156:159], v[0:3], v[216:219]
ds_read_b128 a[180:183], v238 offset:30720
v_cvt_pk_bf16_f32 v168, v86, v87
v_mfma_f32_16x16x32_bf16 v[144:147], a[160:163], v[4:7], v[144:147]
v_cvt_pk_bf16_f32 v236, v234, v235
v_cvt_pk_bf16_f32 v237, v232, v233
v_mfma_f32_16x16x32_bf16 v[144:147], a[164:167], v[8:11], v[144:147]
v_exp_f32_e64 v134, v134
v_mfma_f32_16x16x32_bf16 v[144:147], a[168:171], v[12:15], v[144:147]
ds_read_b128 a[184:187], v238 offset:31744
v_cvt_pk_bf16_f32 v169, v84, v85
v_mfma_f32_16x16x32_bf16 v[144:147], a[172:175], v[16:19], v[144:147]
v_exp_f32_e64 v135, v135
v_mfma_f32_16x16x32_bf16 v[144:147], a[176:179], v[20:23], v[144:147]
v_exp_f32_e64 v132, v132
v_mfma_f32_16x16x32_bf16 v[148:151], a[176:179], v[68:71], v[216:219]
ds_read_b128 a[188:191], v238 offset:32768
v_cvt_pk_bf16_f32 v172, v90, v91
v_mfma_f32_16x16x32_bf16 v[148:151], a[172:175], v[64:67], v[148:151]
v_exp_f32_e64 v133, v133
v_mfma_f32_16x16x32_bf16 v[148:151], a[168:171], v[60:63], v[148:151]
ds_read_b32 v255, v252 offset:4
v_cvt_pk_bf16_f32 v173, v88, v89
v_mfma_f32_16x16x32_bf16 v[148:151], a[164:167], v[56:59], v[148:151]
ds_read_b128 a[192:195], v238 offset:33792
v_cvt_pk_bf16_f32 v176, v94, v95
v_mfma_f32_16x16x32_bf16 v[148:151], a[160:163], v[52:55], v[148:151]
v_exp_f32_e64 v138, v138
v_mfma_f32_16x16x32_bf16 v[148:151], a[156:159], v[48:51], v[148:151]
v_exp_f32_e64 v139, v139
v_mfma_f32_16x16x32_bf16 v[152:155], a[156:159], v[96:99], v[216:219]
ds_read_b128 a[196:199], v238 offset:34816
v_cvt_pk_bf16_f32 v177, v92, v93
v_mfma_f32_16x16x32_bf16 v[152:155], a[160:163], v[100:103], v[152:155]
v_exp_f32_e64 v136, v136
v_mfma_f32_16x16x32_bf16 v[152:155], a[164:167], v[104:107], v[152:155]
v_exp_f32_e64 v137, v137
v_mfma_f32_16x16x32_bf16 v[152:155], a[168:171], v[108:111], v[152:155]
s_waitcnt lgkmcnt(1)
ds_read_b128 a[200:203], v238 offset:35840
v_cmp_ne_u32 vcc, s33, v255
v_mfma_f32_16x16x32_bf16 v[152:155], a[172:175], v[112:115], v[152:155]
v_exp_f32_e64 v142, v142
v_mfma_f32_16x16x32_bf16 v[152:155], a[176:179], v[116:119], v[152:155]
s_cbranch_vccnz L_sy_x11
L_syb_x11:
v_exp_f32 v143, v143
v_mfma_f32_16x16x32_bf16 a[36:39], a[180:183], v[192:195], a[36:39]
ds_read_b128 a[156:159], v239 offset:24576
ds_read_b128 a[160:163], v239 offset:25600
v_mfma_f32_16x16x32_bf16 a[36:39], a[184:187], v[204:207], a[36:39]
ds_write_b64 v240, v[236:237] offset:0
v_rcp_f32_e64 v131, v131
v_mfma_f32_16x16x32_bf16 a[40:43], a[184:187], v[208:211], a[40:43]
v_exp_f32_e64 v140, v140
v_mfma_f32_16x16x32_bf16 a[40:43], a[180:183], v[196:199], a[40:43]
ds_read_b128 a[164:167], v239 offset:26624
ds_read_b128 a[168:171], v239 offset:27648
v_mfma_f32_16x16x32_bf16 a[44:47], a[180:183], v[200:203], a[44:47]
v_exp_f32_e64 v141, v141
v_mfma_f32_16x16x32_bf16 a[44:47], a[184:187], v[212:215], a[44:47]
v_rcp_f32_e64 v122, v122
v_mfma_f32_16x16x32_bf16 a[56:59], a[192:195], v[212:215], a[56:59]
ds_read_b128 a[172:175], v239 offset:28672
ds_read_b128 a[176:179], v239 offset:29696
v_mfma_f32_16x16x32_bf16 a[56:59], a[188:191], v[200:203], a[56:59]
s_add_u32 m0, s23, 104448
v_rcp_f32_e64 v123, v123
v_mfma_f32_16x16x32_bf16 a[52:55], a[188:191], v[196:199], a[52:55]
s_add_u32 s26, s62, 196608
ds_read_b128 v[216:219], v241 offset:768
v_rcp_f32_e64 v128, v128
v_mfma_f32_16x16x32_bf16 a[52:55], a[192:195], v[208:211], a[52:55]
buffer_load_dwordx4 v248, s[12:15], s26 offen offset:0 sc1 lds
v_mfma_f32_16x16x32_bf16 a[48:51], a[192:195], v[204:207], a[48:51]
v_rcp_f32_e64 v120, v120
v_mfma_f32_16x16x32_bf16 a[48:51], a[188:191], v[192:195], a[48:51]
s_waitcnt lgkmcnt(1)
v_rcp_f32 v121, v121
v_mfma_f32_16x16x32_bf16 a[60:63], a[196:199], v[192:195], a[60:63]
buffer_load_dwordx4 v248, s[12:15], s26 offen offset:1024 sc1 lds
v_mfma_f32_16x16x32_bf16 a[60:63], a[200:203], v[204:207], a[60:63]
v_rcp_f32_e64 v126, v126
v_mfma_f32_16x16x32_bf16 a[64:67], a[200:203], v[208:211], a[64:67]
v_rcp_f32_e64 v127, v127
v_mfma_f32_16x16x32_bf16 a[64:67], a[196:199], v[196:199], a[64:67]
buffer_load_dwordx4 v248, s[12:15], s26 offen offset:2048 sc1 lds
v_mfma_f32_16x16x32_bf16 a[68:71], a[196:199], v[200:203], a[68:71]
v_rcp_f32_e64 v124, v124
v_mfma_f32_16x16x32_bf16 a[68:71], a[200:203], v[212:215], a[68:71]
s_waitcnt lgkmcnt(0)
v_rcp_f32 v125, v125
v_rcp_f32 v130, v130
v_rcp_f32 v129, v129
v_pk_add_f32 v[134:135], v[134:135], 1.0 op_sel_hi:[1,0]
v_pk_add_f32 v[132:133], v[132:133], 1.0 op_sel_hi:[1,0]
v_pk_add_f32 v[138:139], v[138:139], 1.0 op_sel_hi:[1,0]
v_pk_add_f32 v[136:137], v[136:137], 1.0 op_sel_hi:[1,0]
v_pk_add_f32 v[142:143], v[142:143], 1.0 op_sel_hi:[1,0]
v_pk_add_f32 v[140:141], v[140:141], 1.0 op_sel_hi:[1,0]
v_pk_mul_f32 v[122:123], v[36:37], v[122:123]
v_pk_mul_f32 v[120:121], v[38:39], v[120:121]
v_pk_mul_f32 v[126:127], v[40:41], v[126:127]
v_pk_mul_f32 v[124:125], v[42:43], v[124:125]
v_pk_mul_f32 v[130:131], v[44:45], v[130:131]
v_pk_mul_f32 v[128:129], v[46:47], v[128:129]
v_pk_fma_f32 v[158:159], v[144:145], v[144:145], s[28:29]
v_pk_fma_f32 v[156:157], v[146:147], v[146:147], s[28:29]
v_pk_fma_f32 v[162:163], v[148:149], v[148:149], s[28:29]
v_pk_fma_f32 v[160:161], v[150:151], v[150:151], s[28:29]
v_pk_fma_f32 v[166:167], v[152:153], v[152:153], s[28:29]
v_pk_fma_f32 v[164:165], v[154:155], v[154:155], s[28:29]
v_pk_mul_f32 v[158:159], v[144:145], v[158:159] neg_lo:[1,0] neg_hi:[1,0]
v_pk_mul_f32 v[156:157], v[146:147], v[156:157] neg_lo:[1,0] neg_hi:[1,0]
v_pk_mul_f32 v[162:163], v[148:149], v[162:163] neg_lo:[1,0] neg_hi:[1,0]
v_pk_mul_f32 v[160:161], v[150:151], v[160:161] neg_lo:[1,0] neg_hi:[1,0]
v_pk_mul_f32 v[166:167], v[152:153], v[166:167] neg_lo:[1,0] neg_hi:[1,0]
v_pk_mul_f32 v[164:165], v[154:155], v[164:165] neg_lo:[1,0] neg_hi:[1,0]
v_mfma_f32_16x16x32_bf16 v[24:27], a[156:159], v[0:3], v[216:219]
ds_read_b128 a[180:183], v238 offset:36864
ds_read_b128 v[228:231], v239 offset:55296
v_mfma_f32_16x16x32_bf16 v[24:27], a[160:163], v[4:7], v[24:27]
ds_read_b128 a[204:207], v243 offset:24576
ds_read_b128 v[244:247], v239 offset:56320
v_mfma_f32_16x16x32_bf16 v[24:27], a[164:167], v[8:11], v[24:27]
v_exp_f32_e64 v158, v158
v_mfma_f32_16x16x32_bf16 v[24:27], a[168:171], v[12:15], v[24:27]
ds_read_b128 a[184:187], v238 offset:37888
ds_read_b128 a[208:211], v243 offset:25600
v_mfma_f32_16x16x32_bf16 v[24:27], a[172:175], v[16:19], v[24:27]
v_exp_f32_e64 v159, v159
v_mfma_f32_16x16x32_bf16 v[24:27], a[176:179], v[20:23], v[24:27]
ds_read_b128 a[212:215], v243 offset:26624
v_cvt_pk_bf16_f32 v170, v122, v123
v_mfma_f32_16x16x32_bf16 v[28:31], a[176:179], v[68:71], v[216:219]
ds_read_b128 a[188:191], v238 offset:38912
v_cvt_pk_bf16_f32 v171, v120, v121
v_mfma_f32_16x16x32_bf16 v[28:31], a[172:175], v[64:67], v[28:31]
ds_read_b128 a[216:219], v243 offset:27648
v_cvt_pk_bf16_f32 v174, v126, v127
v_mfma_f32_16x16x32_bf16 v[28:31], a[168:171], v[60:63], v[28:31]
v_exp_f32_e64 v156, v156
v_mfma_f32_16x16x32_bf16 v[28:31], a[164:167], v[56:59], v[28:31]
ds_read_b128 a[192:195], v238 offset:39936
ds_read_b128 a[220:223], v243 offset:28672
v_mfma_f32_16x16x32_bf16 v[28:31], a[160:163], v[52:55], v[28:31]
v_exp_f32_e64 v157, v157
v_mfma_f32_16x16x32_bf16 v[28:31], a[156:159], v[48:51], v[28:31]
ds_read_b128 a[224:227], v243 offset:29696
v_cvt_pk_bf16_f32 v175, v124, v125
v_mfma_f32_16x16x32_bf16 v[32:35], a[156:159], v[96:99], v[216:219]
ds_read_b128 a[196:199], v238 offset:40960
v_cvt_pk_bf16_f32 v178, v130, v131
v_mfma_f32_16x16x32_bf16 v[32:35], a[160:163], v[100:103], v[32:35]
ds_read_b128 v[220:223], v242 offset:768
v_cvt_pk_bf16_f32 v179, v128, v129
v_mfma_f32_16x16x32_bf16 v[32:35], a[164:167], v[104:107], v[32:35]
v_exp_f32_e64 v162, v162
v_mfma_f32_16x16x32_bf16 v[32:35], a[168:171], v[108:111], v[32:35]
ds_read_b128 a[200:203], v238 offset:41984
v_rcp_f32_e64 v140, v140
v_mfma_f32_16x16x32_bf16 v[32:35], a[172:175], v[112:115], v[32:35]
v_exp_f32_e64 v163, v163
v_mfma_f32_16x16x32_bf16 v[32:35], a[176:179], v[116:119], v[32:35]
s_waitcnt lgkmcnt(1)
v_exp_f32 v160, v160
v_mfma_f32_16x16x32_bf16 v[224:227], a[204:207], a[228:231], v[220:223]
v_exp_f32_e64 v161, v161
v_mfma_f32_16x16x32_bf16 v[224:227], a[208:211], a[232:235], v[224:227]
v_exp_f32_e64 v166, v166
v_mfma_f32_16x16x32_bf16 v[224:227], a[212:215], a[236:239], v[224:227]
v_exp_f32_e64 v167, v167
v_mfma_f32_16x16x32_bf16 v[224:227], a[216:219], a[240:243], v[224:227]
v_exp_f32_e64 v164, v164
v_mfma_f32_16x16x32_bf16 v[224:227], a[220:223], a[244:247], v[224:227]
v_exp_f32_e64 v165, v165
v_mfma_f32_16x16x32_bf16 v[224:227], a[224:227], a[248:251], v[224:227]
v_rcp_f32_e64 v134, v134
v_mfma_f32_16x16x32_bf16 a[72:75], a[180:183], v[192:195], a[72:75]
ds_read_b128 a[156:159], v239 offset:30720
ds_read_b128 a[160:163], v239 offset:31744
v_mfma_f32_16x16x32_bf16 a[72:75], a[184:187], v[204:207], a[72:75]
v_rcp_f32_e64 v135, v135
v_mfma_f32_16x16x32_bf16 a[76:79], a[184:187], v[208:211], a[76:79]
v_rcp_f32_e64 v132, v132
v_mfma_f32_16x16x32_bf16 a[76:79], a[180:183], v[196:199], a[76:79]
ds_read_b128 a[164:167], v239 offset:32768
ds_read_b128 a[168:171], v239 offset:33792
v_mfma_f32_16x16x32_bf16 a[80:83], a[180:183], v[200:203], a[80:83]
ds_read_b128 a[208:211], v249 offset:25600
ds_read_b128 a[212:215], v249 offset:32768
v_mfma_f32_16x16x32_bf16 a[80:83], a[184:187], v[212:215], a[80:83]
v_rcp_f32_e64 v133, v133
v_mfma_f32_16x16x32_bf16 a[92:95], a[192:195], v[212:215], a[92:95]
ds_read_b128 a[172:175], v239 offset:34816
ds_read_b128 a[176:179], v239 offset:35840
v_mfma_f32_16x16x32_bf16 a[92:95], a[188:191], v[200:203], a[92:95]
s_add_u32 m0, s23, 107520
ds_read_b128 a[216:219], v249 offset:33792
ds_read_b128 a[220:223], v249 offset:40960
v_mfma_f32_16x16x32_bf16 a[88:91], a[188:191], v[196:199], a[88:91]
s_add_u32 s26, s62, 199680
ds_read_b128 v[216:219], v241 offset:832
ds_read_b128 a[204:207], v249 offset:24576
v_mfma_f32_16x16x32_bf16 a[88:91], a[192:195], v[208:211], a[88:91]
buffer_load_dwordx4 v248, s[12:15], s26 offen offset:0 sc1 lds
v_mfma_f32_16x16x32_bf16 a[84:87], a[192:195], v[204:207], a[84:87]
v_rcp_f32_e64 v138, v138
v_mfma_f32_16x16x32_bf16 a[84:87], a[188:191], v[192:195], a[84:87]
buffer_load_dwordx4 v248, s[12:15], s26 offen offset:1024 sc1 lds
v_mfma_f32_16x16x32_bf16 a[96:99], a[196:199], v[192:195], a[96:99]
s_waitcnt lgkmcnt(2)
ds_read_b128 a[224:227], v249 offset:41984
v_rcp_f32 v141, v141
v_mfma_f32_16x16x32_bf16 a[96:99], a[200:203], v[204:207], a[96:99]
v_rcp_f32_e64 v139, v139
v_mfma_f32_16x16x32_bf16 a[100:103], a[200:203], v[208:211], a[100:103]
v_rcp_f32_e64 v136, v136
v_mfma_f32_16x16x32_bf16 a[100:103], a[196:199], v[196:199], a[100:103]
buffer_load_dwordx4 v248, s[12:15], s26 offen offset:2048 sc1 lds
v_mfma_f32_16x16x32_bf16 a[104:107], a[196:199], v[200:203], a[104:107]
v_rcp_f32_e64 v137, v137
v_mfma_f32_16x16x32_bf16 a[104:107], a[200:203], v[212:215], a[104:107]
s_waitcnt lgkmcnt(0)
v_rcp_f32 v142, v142
v_rcp_f32 v143, v143
v_add_f32 v158, 1.0, v158
v_add_f32_e64 v159, 1.0, v159
v_pk_add_f32 v[156:157], v[156:157], 1.0 op_sel_hi:[1,0]
v_pk_add_f32 v[162:163], v[162:163], 1.0 op_sel_hi:[1,0]
v_pk_add_f32 v[160:161], v[160:161], 1.0 op_sel_hi:[1,0]
v_pk_add_f32 v[166:167], v[166:167], 1.0 op_sel_hi:[1,0]
v_pk_add_f32 v[164:165], v[164:165], 1.0 op_sel_hi:[1,0]
v_pk_mul_f32 v[134:135], v[72:73], v[134:135]
v_pk_mul_f32 v[132:133], v[74:75], v[132:133]
v_pk_mul_f32 v[138:139], v[76:77], v[138:139]
v_pk_mul_f32 v[136:137], v[78:79], v[136:137]
v_pk_mul_f32 v[142:143], v[80:81], v[142:143]
v_pk_mul_f32 v[140:141], v[82:83], v[140:141]
v_pk_fma_f32 v[86:87], v[24:25], v[24:25], s[28:29]
v_pk_fma_f32 v[84:85], v[26:27], v[26:27], s[28:29]
v_pk_fma_f32 v[90:91], v[28:29], v[28:29], s[28:29]
v_pk_fma_f32 v[88:89], v[30:31], v[30:31], s[28:29]
v_pk_fma_f32 v[94:95], v[32:33], v[32:33], s[28:29]
v_pk_fma_f32 v[92:93], v[34:35], v[34:35], s[28:29]
v_pk_fma_f32 v[234:235], v[224:225], v[224:225], s[28:29]
v_pk_fma_f32 v[232:233], v[226:227], v[226:227], s[28:29]
v_pk_mul_f32 v[86:87], v[24:25], v[86:87] neg_lo:[1,0] neg_hi:[1,0]
v_pk_mul_f32 v[84:85], v[26:27], v[84:85] neg_lo:[1,0] neg_hi:[1,0]
v_pk_mul_f32 v[90:91], v[28:29], v[90:91] neg_lo:[1,0] neg_hi:[1,0]
v_pk_mul_f32 v[88:89], v[30:31], v[88:89] neg_lo:[1,0] neg_hi:[1,0]
v_pk_mul_f32 v[94:95], v[32:33], v[94:95] neg_lo:[1,0] neg_hi:[1,0]
v_pk_mul_f32 v[92:93], v[34:35], v[92:93] neg_lo:[1,0] neg_hi:[1,0]
v_pk_mul_f32 v[234:235], v[224:225], v[234:235] neg_lo:[1,0] neg_hi:[1,0]
v_pk_mul_f32 v[232:233], v[226:227], v[232:233] neg_lo:[1,0] neg_hi:[1,0]
v_mfma_f32_16x16x32_bf16 v[36:39], a[156:159], v[0:3], v[216:219]
ds_read_b128 a[180:183], v238 offset:43008
v_cvt_pk_bf16_f32 v180, v134, v135
v_mfma_f32_16x16x32_bf16 v[36:39], a[160:163], v[4:7], v[36:39]
v_exp_f32_e64 v86, v86
v_mfma_f32_16x16x32_bf16 v[36:39], a[164:167], v[8:11], v[36:39]
v_exp_f32_e64 v87, v87
v_mfma_f32_16x16x32_bf16 v[36:39], a[168:171], v[12:15], v[36:39]
ds_read_b128 a[184:187], v238 offset:44032
v_cvt_pk_bf16_f32 v181, v132, v133
v_mfma_f32_16x16x32_bf16 v[36:39], a[172:175], v[16:19], v[36:39]
v_exp_f32_e64 v84, v84
v_mfma_f32_16x16x32_bf16 v[36:39], a[176:179], v[20:23], v[36:39]
v_exp_f32_e64 v85, v85
v_mfma_f32_16x16x32_bf16 v[40:43], a[176:179], v[68:71], v[216:219]
ds_read_b128 a[188:191], v238 offset:45056
v_cvt_pk_bf16_f32 v184, v138, v139
v_mfma_f32_16x16x32_bf16 v[40:43], a[172:175], v[64:67], v[40:43]
v_exp_f32_e64 v90, v90
v_mfma_f32_16x16x32_bf16 v[40:43], a[168:171], v[60:63], v[40:43]
v_exp_f32_e64 v91, v91
v_mfma_f32_16x16x32_bf16 v[40:43], a[164:167], v[56:59], v[40:43]
ds_read_b128 a[192:195], v238 offset:46080
v_cvt_pk_bf16_f32 v185, v136, v137
v_mfma_f32_16x16x32_bf16 v[40:43], a[160:163], v[52:55], v[40:43]
v_exp_f32_e64 v88, v88
v_mfma_f32_16x16x32_bf16 v[40:43], a[156:159], v[48:51], v[40:43]
v_exp_f32_e64 v89, v89
v_mfma_f32_16x16x32_bf16 v[44:47], a[156:159], v[96:99], v[216:219]
ds_read_b128 a[196:199], v238 offset:47104
v_cvt_pk_bf16_f32 v188, v142, v143
v_mfma_f32_16x16x32_bf16 v[44:47], a[160:163], v[100:103], v[44:47]
v_exp_f32_e64 v94, v94
v_mfma_f32_16x16x32_bf16 v[44:47], a[164:167], v[104:107], v[44:47]
v_exp_f32_e64 v95, v95
v_mfma_f32_16x16x32_bf16 v[44:47], a[168:171], v[108:111], v[44:47]
ds_read_b128 a[200:203], v238 offset:48128
v_cvt_pk_bf16_f32 v189, v140, v141
v_mfma_f32_16x16x32_bf16 v[44:47], a[172:175], v[112:115], v[44:47]
v_xor_b32 v238, 49152, v238
v_xor_b32 v249, 49152, v249
v_mfma_f32_16x16x32_bf16 v[44:47], a[176:179], v[116:119], v[44:47]
s_waitcnt lgkmcnt(1)
v_exp_f32 v92, v92
v_mfma_f32_16x16x32_bf16 a[108:111], a[180:183], v[192:195], a[108:111]
ds_read_b128 a[156:159], v239 offset:36864
ds_read_b128 a[160:163], v239 offset:37888
v_mfma_f32_16x16x32_bf16 a[108:111], a[184:187], v[204:207], a[108:111]
v_exp_f32_e64 v93, v93
v_mfma_f32_16x16x32_bf16 a[112:115], a[184:187], v[208:211], a[112:115]
v_rcp_f32_e64 v158, v158
v_mfma_f32_16x16x32_bf16 a[112:115], a[180:183], v[196:199], a[112:115]
ds_read_b128 a[164:167], v239 offset:38912
ds_read_b128 a[168:171], v239 offset:39936
v_mfma_f32_16x16x32_bf16 a[116:119], a[180:183], v[200:203], a[116:119]
v_rcp_f32_e64 v159, v159
v_mfma_f32_16x16x32_bf16 a[116:119], a[184:187], v[212:215], a[116:119]
v_rcp_f32 v156, v156
v_mfma_f32_16x16x32_bf16 a[128:131], a[192:195], v[212:215], a[128:131]
s_xor_b32 s63, s63, s36
ds_read_b128 a[172:175], v239 offset:40960
ds_read_b128 a[176:179], v239 offset:41984
v_mfma_f32_16x16x32_bf16 a[128:131], a[188:191], v[200:203], a[128:131]
s_add_u32 m0, s63, 0
v_rcp_f32 v157, v157
v_mfma_f32_16x16x32_bf16 a[124:127], a[188:191], v[196:199], a[124:127]
s_add_u32 s26, s62, 221184
ds_read_b128 v[216:219], v241 offset:896
v_exp_f32_e64 v233, v233
v_mfma_f32_16x16x32_bf16 a[124:127], a[192:195], v[208:211], a[124:127]
buffer_load_dwordx4 v248, s[12:15], s26 offen offset:0 sc1 lds
v_mfma_f32_16x16x32_bf16 a[120:123], a[192:195], v[204:207], a[120:123]
v_rcp_f32_e64 v162, v162
v_mfma_f32_16x16x32_bf16 a[120:123], a[188:191], v[192:195], a[120:123]
v_rcp_f32 v163, v163
v_mfma_f32_16x16x32_bf16 a[132:135], a[196:199], v[192:195], a[132:135]
s_waitcnt lgkmcnt(1)
buffer_load_dwordx4 v248, s[12:15], s26 offen offset:1024 sc1 lds
v_mfma_f32_16x16x32_bf16 a[132:135], a[200:203], v[204:207], a[132:135]
v_rcp_f32_e64 v160, v160
v_mfma_f32_16x16x32_bf16 a[136:139], a[200:203], v[208:211], a[136:139]
v_rcp_f32_e64 v161, v161
v_mfma_f32_16x16x32_bf16 a[136:139], a[196:199], v[196:199], a[136:139]
buffer_load_dwordx4 v248, s[12:15], s26 offen offset:2048 sc1 lds
v_mfma_f32_16x16x32_bf16 a[140:143], a[196:199], v[200:203], a[140:143]
v_rcp_f32_e64 v166, v166
v_mfma_f32_16x16x32_bf16 a[140:143], a[200:203], v[212:215], a[140:143]
v_rcp_f32_e64 v167, v167
v_mfma_f32_16x16x32_bf16 a[144:147], a[204:207], v[228:231], a[144:147]
v_rcp_f32_e64 v164, v164
v_mfma_f32_16x16x32_bf16 a[144:147], a[208:211], v[244:247], a[144:147]
v_rcp_f32_e64 v165, v165
v_mfma_f32_16x16x32_bf16 a[148:151], a[216:219], v[244:247], a[148:151]
v_exp_f32_e64 v234, v234
v_mfma_f32_16x16x32_bf16 a[148:151], a[212:215], v[228:231], a[148:151]
s_waitcnt lgkmcnt(0)
v_exp_f32 v235, v235
v_exp_f32 v232, v232
v_add_f32 v86, 1.0, v86
v_add_f32_e64 v87, 1.0, v87
v_pk_add_f32 v[84:85], v[84:85], 1.0 op_sel_hi:[1,0]
v_pk_add_f32 v[90:91], v[90:91], 1.0 op_sel_hi:[1,0]
v_pk_add_f32 v[88:89], v[88:89], 1.0 op_sel_hi:[1,0]
v_pk_add_f32 v[94:95], v[94:95], 1.0 op_sel_hi:[1,0]
v_pk_add_f32 v[92:93], v[92:93], 1.0 op_sel_hi:[1,0]
v_pk_add_f32 v[234:235], v[234:235], 1.0 op_sel_hi:[1,0]
v_pk_add_f32 v[232:233], v[232:233], 1.0 op_sel_hi:[1,0]
v_pk_mul_f32 v[158:159], v[144:145], v[158:159]
v_pk_mul_f32 v[156:157], v[146:147], v[156:157]
v_pk_mul_f32 v[162:163], v[148:149], v[162:163]
v_pk_mul_f32 v[160:161], v[150:151], v[160:161]
v_pk_mul_f32 v[166:167], v[152:153], v[166:167]
v_pk_mul_f32 v[164:165], v[154:155], v[164:165]
v_pk_fma_f32 v[122:123], v[36:37], v[36:37], s[28:29]
v_pk_fma_f32 v[120:121], v[38:39], v[38:39], s[28:29]
v_pk_fma_f32 v[126:127], v[40:41], v[40:41], s[28:29]
v_pk_fma_f32 v[124:125], v[42:43], v[42:43], s[28:29]
v_pk_fma_f32 v[130:131], v[44:45], v[44:45], s[28:29]
v_pk_fma_f32 v[128:129], v[46:47], v[46:47], s[28:29]
v_pk_mul_f32 v[122:123], v[36:37], v[122:123] neg_lo:[1,0] neg_hi:[1,0]
v_pk_mul_f32 v[120:121], v[38:39], v[120:121] neg_lo:[1,0] neg_hi:[1,0]
v_pk_mul_f32 v[126:127], v[40:41], v[126:127] neg_lo:[1,0] neg_hi:[1,0]
v_pk_mul_f32 v[124:125], v[42:43], v[124:125] neg_lo:[1,0] neg_hi:[1,0]
v_pk_mul_f32 v[130:131], v[44:45], v[130:131] neg_lo:[1,0] neg_hi:[1,0]
v_pk_mul_f32 v[128:129], v[46:47], v[128:129] neg_lo:[1,0] neg_hi:[1,0]
v_mfma_f32_16x16x32_bf16 v[72:75], a[156:159], v[0:3], v[216:219]
ds_read_b128 a[180:183], v238 offset:0
v_cvt_pk_bf16_f32 v182, v158, v159
v_rcp_f32_e64 v232, v232
v_mfma_f32_16x16x32_bf16 v[72:75], a[160:163], v[4:7], v[72:75]
v_exp_f32 v122, v122
v_rcp_f32 v233, v233
v_mfma_f32_16x16x32_bf16 v[72:75], a[164:167], v[8:11], v[72:75]
v_exp_f32_e64 v123, v123
v_mfma_f32_16x16x32_bf16 v[72:75], a[168:171], v[12:15], v[72:75]
ds_read_b128 a[184:187], v238 offset:1024
v_cvt_pk_bf16_f32 v183, v156, v157
v_mfma_f32_16x16x32_bf16 v[72:75], a[172:175], v[16:19], v[72:75]
v_exp_f32_e64 v120, v120
v_mfma_f32_16x16x32_bf16 v[72:75], a[176:179], v[20:23], v[72:75]
v_exp_f32_e64 v121, v121
v_mfma_f32_16x16x32_bf16 v[76:79], a[176:179], v[68:71], v[216:219]
ds_read_b128 a[188:191], v238 offset:2048
v_cvt_pk_bf16_f32 v186, v162, v163
v_mfma_f32_16x16x32_bf16 v[76:79], a[172:175], v[64:67], v[76:79]
v_exp_f32_e64 v126, v126
v_mfma_f32_16x16x32_bf16 v[76:79], a[168:171], v[60:63], v[76:79]
v_exp_f32_e64 v127, v127
v_mfma_f32_16x16x32_bf16 v[76:79], a[164:167], v[56:59], v[76:79]
ds_read_b128 a[192:195], v238 offset:3072
v_cvt_pk_bf16_f32 v187, v160, v161
v_mfma_f32_16x16x32_bf16 v[76:79], a[160:163], v[52:55], v[76:79]
v_exp_f32_e64 v124, v124
v_mfma_f32_16x16x32_bf16 v[76:79], a[156:159], v[48:51], v[76:79]
v_exp_f32_e64 v125, v125
v_mfma_f32_16x16x32_bf16 v[80:83], a[156:159], v[96:99], v[216:219]
ds_read_b128 a[196:199], v238 offset:4096
v_cvt_pk_bf16_f32 v190, v166, v167
v_mfma_f32_16x16x32_bf16 v[80:83], a[160:163], v[100:103], v[80:83]
v_exp_f32_e64 v130, v130
v_mfma_f32_16x16x32_bf16 v[80:83], a[164:167], v[104:107], v[80:83]
v_exp_f32_e64 v131, v131
v_mfma_f32_16x16x32_bf16 v[80:83], a[168:171], v[108:111], v[80:83]
ds_read_b128 a[200:203], v238 offset:5120
v_cvt_pk_bf16_f32 v191, v164, v165
v_mfma_f32_16x16x32_bf16 v[80:83], a[172:175], v[112:115], v[80:83]
v_exp_f32_e64 v128, v128
v_mfma_f32_16x16x32_bf16 v[80:83], a[176:179], v[116:119], v[80:83]
s_waitcnt lgkmcnt(1)
v_exp_f32 v129, v129
v_mfma_f32_16x16x32_bf16 a[0:3], a[180:183], v[168:171], a[0:3]
ds_read_b128 a[156:159], v239 offset:43008
ds_read_b128 a[160:163], v239 offset:44032
v_mfma_f32_16x16x32_bf16 a[0:3], a[184:187], v[180:183], a[0:3]
v_rcp_f32_e64 v86, v86
v_mfma_f32_16x16x32_bf16 a[4:7], a[184:187], v[184:187], a[4:7]
v_rcp_f32_e64 v87, v87
v_mfma_f32_16x16x32_bf16 a[4:7], a[180:183], v[172:175], a[4:7]
ds_read_b128 a[164:167], v239 offset:45056
ds_read_b128 a[168:171], v239 offset:46080
v_mfma_f32_16x16x32_bf16 a[8:11], a[180:183], v[176:179], a[8:11]
v_rcp_f32_e64 v84, v84
v_mfma_f32_16x16x32_bf16 a[8:11], a[184:187], v[188:191], a[8:11]
v_rcp_f32_e64 v85, v85
v_mfma_f32_16x16x32_bf16 a[20:23], a[192:195], v[188:191], a[20:23]
ds_read_b128 a[172:175], v239 offset:47104
ds_read_b128 a[176:179], v239 offset:48128
v_mfma_f32_16x16x32_bf16 a[20:23], a[188:191], v[176:179], a[20:23]
s_add_u32 m0, s63, 3072
v_rcp_f32_e64 v90, v90
v_mfma_f32_16x16x32_bf16 a[16:19], a[188:191], v[172:175], a[16:19]
s_add_u32 s26, s62, 224256
ds_read_b128 v[216:219], v241 offset:960
v_rcp_f32_e64 v235, v235
v_mfma_f32_16x16x32_bf16 a[16:19], a[192:195], v[184:187], a[16:19]
s_add_u32 s62, s62, 98304
buffer_load_dwordx4 v248, s[12:15], s26 offen offset:0 sc1 lds
v_mfma_f32_16x16x32_bf16 a[12:15], a[192:195], v[180:183], a[12:15]
s_sub_u32 s37, s37, 1
v_add_u32 v241, 512, v241
v_add_u32 v242, 512, v242
v_mfma_f32_16x16x32_bf16 a[12:15], a[188:191], v[168:171], a[12:15]
s_cmp_lg_u32 s37, 0
v_rcp_f32 v91, v91
v_mfma_f32_16x16x32_bf16 a[24:27], a[196:199], v[168:171], a[24:27]
s_waitcnt lgkmcnt(1)
buffer_load_dwordx4 v248, s[12:15], s26 offen offset:1024 sc1 lds
v_mfma_f32_16x16x32_bf16 a[24:27], a[200:203], v[180:183], a[24:27]
v_rcp_f32_e64 v88, v88
v_mfma_f32_16x16x32_bf16 a[28:31], a[200:203], v[184:187], a[28:31]
v_rcp_f32_e64 v89, v89
v_mfma_f32_16x16x32_bf16 a[28:31], a[196:199], v[172:175], a[28:31]
buffer_load_dwordx4 v248, s[12:15], s26 offen offset:2048 sc1 lds
v_mfma_f32_16x16x32_bf16 a[32:35], a[196:199], v[176:179], a[32:35]
v_rcp_f32_e64 v94, v94
v_mfma_f32_16x16x32_bf16 a[32:35], a[200:203], v[188:191], a[32:35]
v_rcp_f32_e64 v95, v95
v_mfma_f32_16x16x32_bf16 a[152:155], a[220:223], v[228:231], a[152:155]
v_rcp_f32 v92, v92
v_mfma_f32_16x16x32_bf16 a[152:155], a[224:227], v[244:247], a[152:155]
s_waitcnt vmcnt(6) lgkmcnt(0)
ds_write_b8 v251, v250 offset:0
v_rcp_f32 v93, v93
v_rcp_f32 v234, v234
v_pk_add_f32 v[122:123], v[122:123], 1.0 op_sel_hi:[1,0]
v_pk_add_f32 v[120:121], v[120:121], 1.0 op_sel_hi:[1,0]
v_pk_add_f32 v[126:127], v[126:127], 1.0 op_sel_hi:[1,0]
v_pk_add_f32 v[124:125], v[124:125], 1.0 op_sel_hi:[1,0]
v_pk_add_f32 v[130:131], v[130:131], 1.0 op_sel_hi:[1,0]
v_pk_add_f32 v[128:129], v[128:129], 1.0 op_sel_hi:[1,0]
v_pk_mul_f32 v[86:87], v[24:25], v[86:87]
v_pk_mul_f32 v[84:85], v[26:27], v[84:85]
v_pk_mul_f32 v[90:91], v[28:29], v[90:91]
v_pk_mul_f32 v[88:89], v[30:31], v[88:89]
v_pk_mul_f32 v[94:95], v[32:33], v[94:95]
v_pk_mul_f32 v[92:93], v[34:35], v[92:93]
v_pk_mul_f32 v[234:235], v[224:225], v[234:235]
v_pk_mul_f32 v[232:233], v[226:227], v[232:233]
v_pk_fma_f32 v[134:135], v[72:73], v[72:73], s[28:29]
v_pk_fma_f32 v[132:133], v[74:75], v[74:75], s[28:29]
v_pk_fma_f32 v[138:139], v[76:77], v[76:77], s[28:29]
v_pk_fma_f32 v[136:137], v[78:79], v[78:79], s[28:29]
v_pk_fma_f32 v[142:143], v[80:81], v[80:81], s[28:29]
v_pk_fma_f32 v[140:141], v[82:83], v[82:83], s[28:29]
v_pk_mul_f32 v[134:135], v[72:73], v[134:135] neg_lo:[1,0] neg_hi:[1,0]
v_pk_mul_f32 v[132:133], v[74:75], v[132:133] neg_lo:[1,0] neg_hi:[1,0]
v_pk_mul_f32 v[138:139], v[76:77], v[138:139] neg_lo:[1,0] neg_hi:[1,0]
v_pk_mul_f32 v[136:137], v[78:79], v[136:137] neg_lo:[1,0] neg_hi:[1,0]
v_pk_mul_f32 v[142:143], v[80:81], v[142:143] neg_lo:[1,0] neg_hi:[1,0]
v_pk_mul_f32 v[140:141], v[82:83], v[140:141] neg_lo:[1,0] neg_hi:[1,0]
s_cbranch_scc1 L_loop_x
v_mfma_f32_16x16x32_bf16 v[144:147], a[156:159], v[0:3], v[216:219]
s_nop 0
ds_read_b128 a[180:183], v238 offset:6144
v_cvt_pk_bf16_f32 v192, v86, v87
v_mfma_f32_16x16x32_bf16 v[144:147], a[160:163], v[4:7], v[144:147]
v_cvt_pk_bf16_f32 v236, v234, v235
v_cvt_pk_bf16_f32 v237, v232, v233
v_mfma_f32_16x16x32_bf16 v[144:147], a[164:167], v[8:11], v[144:147]
v_exp_f32_e64 v134, v134
v_mfma_f32_16x16x32_bf16 v[144:147], a[168:171], v[12:15], v[144:147]
ds_read_b128 a[184:187], v238 offset:7168
v_cvt_pk_bf16_f32 v193, v84, v85
v_mfma_f32_16x16x32_bf16 v[144:147], a[172:175], v[16:19], v[144:147]
v_exp_f32_e64 v135, v135
v_mfma_f32_16x16x32_bf16 v[144:147], a[176:179], v[20:23], v[144:147]
v_exp_f32_e64 v132, v132
v_mfma_f32_16x16x32_bf16 v[148:151], a[176:179], v[68:71], v[216:219]
ds_read_b128 a[188:191], v238 offset:8192
v_cvt_pk_bf16_f32 v196, v90, v91
v_mfma_f32_16x16x32_bf16 v[148:151], a[172:175], v[64:67], v[148:151]
v_exp_f32_e64 v133, v133
v_mfma_f32_16x16x32_bf16 v[148:151], a[168:171], v[60:63], v[148:151]
ds_read_b32 v255, v252 offset:0
v_cvt_pk_bf16_f32 v197, v88, v89
v_mfma_f32_16x16x32_bf16 v[148:151], a[164:167], v[56:59], v[148:151]
ds_read_b128 a[192:195], v238 offset:9216
v_cvt_pk_bf16_f32 v200, v94, v95
v_mfma_f32_16x16x32_bf16 v[148:151], a[160:163], v[52:55], v[148:151]
v_exp_f32_e64 v138, v138
v_mfma_f32_16x16x32_bf16 v[148:151], a[156:159], v[48:51], v[148:151]
v_exp_f32_e64 v139, v139
v_mfma_f32_16x16x32_bf16 v[152:155], a[156:159], v[96:99], v[216:219]
ds_read_b128 a[196:199], v238 offset:10240
v_cvt_pk_bf16_f32 v201, v92, v93
v_mfma_f32_16x16x32_bf16 v[152:155], a[160:163], v[100:103], v[152:155]
v_exp_f32_e64 v136, v136
v_mfma_f32_16x16x32_bf16 v[152:155], a[164:167], v[104:107], v[152:155]
v_exp_f32_e64 v137, v137
v_mfma_f32_16x16x32_bf16 v[152:155], a[168:171], v[108:111], v[152:155]
s_waitcnt lgkmcnt(1)
ds_read_b128 a[200:203], v238 offset:11264
v_cmp_ne_u32 vcc, s33, v255
v_mfma_f32_16x16x32_bf16 v[152:155], a[172:175], v[112:115], v[152:155]
v_exp_f32_e64 v142, v142
v_mfma_f32_16x16x32_bf16 v[152:155], a[176:179], v[116:119], v[152:155]
s_cbranch_vccnz L_sy_x47
L_syb_x47:
v_exp_f32 v143, v143
v_mfma_f32_16x16x32_bf16 a[36:39], a[180:183], v[168:171], a[36:39]
ds_read_b128 v[228:231], v239 offset:53248
ds_read_b128 a[156:159], v238 offset:12288
v_mfma_f32_16x16x32_bf16 a[36:39], a[184:187], v[180:183], a[36:39]
ds_write_b64 v240, v[236:237] offset:2048
v_mbcnt_lo_u32_b32 v208, -1, 0
v_mfma_f32_16x16x32_bf16 a[40:43], a[184:187], v[184:187], a[40:43]
ds_read_b128 v[244:247], v239 offset:54272
ds_read_b128 a[160:163], v238 offset:13312
v_mfma_f32_16x16x32_bf16 a[40:43], a[180:183], v[172:175], a[40:43]
v_exp_f32_e64 v140, v140
v_mfma_f32_16x16x32_bf16 a[44:47], a[180:183], v[176:179], a[44:47]
ds_read_b128 a[204:207], v249 offset:0
ds_read_b128 a[164:167], v238 offset:14336
v_mfma_f32_16x16x32_bf16 a[44:47], a[184:187], v[188:191], a[44:47]
v_exp_f32_e64 v141, v141
v_mfma_f32_16x16x32_bf16 a[56:59], a[192:195], v[188:191], a[56:59]
ds_read_b128 a[208:211], v249 offset:1024
ds_read_b128 a[168:171], v238 offset:15360
v_mfma_f32_16x16x32_bf16 a[56:59], a[188:191], v[176:179], a[56:59]
v_rcp_f32_e64 v122, v122
v_mfma_f32_16x16x32_bf16 a[52:55], a[188:191], v[172:175], a[52:55]
ds_read_b128 a[212:215], v249 offset:8192
ds_read_b128 a[172:175], v238 offset:16384
v_mfma_f32_16x16x32_bf16 a[52:55], a[192:195], v[184:187], a[52:55]
v_rcp_f32_e64 v123, v123
v_mfma_f32_16x16x32_bf16 a[48:51], a[192:195], v[180:183], a[48:51]
ds_read_b128 a[216:219], v249 offset:9216
ds_read_b128 a[176:179], v238 offset:17408
v_mfma_f32_16x16x32_bf16 a[48:51], a[188:191], v[168:171], a[48:51]
s_waitcnt lgkmcnt(4)
v_rcp_f32 v120, v120
v_mfma_f32_16x16x32_bf16 a[60:63], a[196:199], v[168:171], a[60:63]
ds_read_b128 a[220:223], v249 offset:16384
v_mbcnt_hi_u32_b32 v208, -1, v208
s_mul_i32 s31, s20, 12288
s_add_u32 s31, s31, 104448
v_mfma_f32_16x16x32_bf16 a[60:63], a[200:203], v[180:183], a[60:63]
v_rcp_f32_e64 v121, v121
v_mfma_f32_16x16x32_bf16 a[64:67], a[200:203], v[184:187], a[64:67]
ds_read_b128 a[224:227], v249 offset:17408
v_lshrrev_b32_e64 v209, 3, v208
v_mfma_f32_16x16x32_bf16 a[64:67], a[196:199], v[172:175], a[64:67]
v_rcp_f32_e64 v126, v126
v_mfma_f32_16x16x32_bf16 a[68:71], a[196:199], v[176:179], a[68:71]
v_rcp_f32_e64 v127, v127
v_mfma_f32_16x16x32_bf16 a[68:71], a[200:203], v[188:191], a[68:71]
v_rcp_f32_e64 v124, v124
v_mfma_f32_16x16x32_bf16 a[144:147], a[204:207], v[228:231], a[144:147]
v_rcp_f32_e64 v125, v125
v_mfma_f32_16x16x32_bf16 a[144:147], a[208:211], v[244:247], a[144:147]
s_waitcnt lgkmcnt(0)
v_rcp_f32 v130, v130
v_mfma_f32_16x16x32_bf16 a[148:151], a[216:219], v[244:247], a[148:151]
v_rcp_f32_e64 v131, v131
v_mfma_f32_16x16x32_bf16 a[148:151], a[212:215], v[228:231], a[148:151]
v_rcp_f32_e64 v128, v128
v_mfma_f32_16x16x32_bf16 a[152:155], a[220:223], v[228:231], a[152:155]
v_rcp_f32_e64 v129, v129
v_mfma_f32_16x16x32_bf16 a[152:155], a[224:227], v[244:247], a[152:155]
v_and_b32 v210, 7, v208
v_xor_b32 v210, v210, v209
v_mul_u32_u24 v209, 384, v209
v_lshl_add_u32 v250, v210, 4, v209
v_lshl_add_u32 v253, v208, 4, s31
s_mov_b32 s27, 485376
buffer_load_dwordx4 a[252:255], v239, s[12:15], s27 offen offset:0 sc1
v_pk_add_f32 v[134:135], v[134:135], 1.0 op_sel_hi:[1,0]
v_pk_add_f32 v[132:133], v[132:133], 1.0 op_sel_hi:[1,0]
v_pk_add_f32 v[138:139], v[138:139], 1.0 op_sel_hi:[1,0]
v_pk_add_f32 v[136:137], v[136:137], 1.0 op_sel_hi:[1,0]
v_pk_add_f32 v[142:143], v[142:143], 1.0 op_sel_hi:[1,0]
v_pk_add_f32 v[140:141], v[140:141], 1.0 op_sel_hi:[1,0]
v_pk_mul_f32 v[122:123], v[36:37], v[122:123]
v_pk_mul_f32 v[120:121], v[38:39], v[120:121]
v_pk_mul_f32 v[126:127], v[40:41], v[126:127]
v_pk_mul_f32 v[124:125], v[42:43], v[124:125]
v_pk_mul_f32 v[130:131], v[44:45], v[130:131]
v_pk_mul_f32 v[128:129], v[46:47], v[128:129]
v_pk_fma_f32 v[158:159], v[144:145], v[144:145], s[28:29]
v_pk_fma_f32 v[156:157], v[146:147], v[146:147], s[28:29]
v_pk_fma_f32 v[162:163], v[148:149], v[148:149], s[28:29]
v_pk_fma_f32 v[160:161], v[150:151], v[150:151], s[28:29]
v_pk_fma_f32 v[166:167], v[152:153], v[152:153], s[28:29]
v_pk_fma_f32 v[164:165], v[154:155], v[154:155], s[28:29]
v_pk_mul_f32 v[158:159], v[144:145], v[158:159] neg_lo:[1,0] neg_hi:[1,0]
v_pk_mul_f32 v[156:157], v[146:147], v[156:157] neg_lo:[1,0] neg_hi:[1,0]
v_pk_mul_f32 v[162:163], v[148:149], v[162:163] neg_lo:[1,0] neg_hi:[1,0]
v_pk_mul_f32 v[160:161], v[150:151], v[160:161] neg_lo:[1,0] neg_hi:[1,0]
v_pk_mul_f32 v[166:167], v[152:153], v[166:167] neg_lo:[1,0] neg_hi:[1,0]
v_pk_mul_f32 v[164:165], v[154:155], v[164:165] neg_lo:[1,0] neg_hi:[1,0]
v_mfma_f32_16x16x32_bf16 a[72:75], a[156:159], v[168:171], a[72:75]
ds_read_b128 a[180:183], v238 offset:18432
v_cvt_pk_bf16_f32 v194, v122, v123
v_rcp_f32_e64 v132, v132
v_mfma_f32_16x16x32_bf16 a[72:75], a[160:163], v[180:183], a[72:75]
v_exp_f32 v158, v158
v_rcp_f32 v133, v133
v_mfma_f32_16x16x32_bf16 a[76:79], a[160:163], v[184:187], a[76:79]
ds_read_b128 a[184:187], v238 offset:19456
v_cvt_pk_bf16_f32 v195, v120, v121
v_rcp_f32_e64 v138, v138
v_mfma_f32_16x16x32_bf16 a[76:79], a[156:159], v[172:175], a[76:79]
v_exp_f32 v159, v159
v_rcp_f32 v139, v139
v_mfma_f32_16x16x32_bf16 a[80:83], a[156:159], v[176:179], a[80:83]
ds_read_b128 a[188:191], v238 offset:20480
v_cvt_pk_bf16_f32 v198, v126, v127
v_rcp_f32_e64 v136, v136
v_mfma_f32_16x16x32_bf16 a[80:83], a[160:163], v[188:191], a[80:83]
v_exp_f32 v156, v156
v_rcp_f32 v137, v137
v_mfma_f32_16x16x32_bf16 a[92:95], a[168:171], v[188:191], a[92:95]
ds_read_b128 a[192:195], v238 offset:21504
v_cvt_pk_bf16_f32 v199, v124, v125
v_rcp_f32_e64 v142, v142
v_mfma_f32_16x16x32_bf16 a[92:95], a[164:167], v[176:179], a[92:95]
v_exp_f32 v157, v157
v_rcp_f32 v143, v143
v_mfma_f32_16x16x32_bf16 a[88:91], a[164:167], v[172:175], a[88:91]
ds_read_b128 a[196:199], v238 offset:22528
v_cvt_pk_bf16_f32 v202, v130, v131
v_rcp_f32_e64 v140, v140
v_mfma_f32_16x16x32_bf16 a[88:91], a[168:171], v[184:187], a[88:91]
v_exp_f32 v162, v162
v_rcp_f32 v141, v141
v_mfma_f32_16x16x32_bf16 a[84:87], a[168:171], v[180:183], a[84:87]
ds_read_b128 a[200:203], v238 offset:23552
v_cvt_pk_bf16_f32 v203, v128, v129
v_lshlrev_b32_e64 v255, 4, v208
v_mfma_f32_16x16x32_bf16 a[84:87], a[164:167], v[168:171], a[84:87]
v_exp_f32 v163, v163
v_and_b32 v209, 15, v208
v_mfma_f32_16x16x32_bf16 a[96:99], a[172:175], v[168:171], a[96:99]
v_exp_f32 v160, v160
v_lshrrev_b32 v210, 4, v208
v_mfma_f32_16x16x32_bf16 a[96:99], a[176:179], v[180:183], a[96:99]
v_exp_f32 v161, v161
v_and_b32 v208, 7, v208
v_mfma_f32_16x16x32_bf16 a[100:103], a[176:179], v[184:187], a[100:103]
v_exp_f32 v166, v166
v_xor_b32 v210, v210, v208
v_mfma_f32_16x16x32_bf16 a[100:103], a[172:175], v[172:175], a[100:103]
v_exp_f32 v167, v167
v_lshlrev_b32 v210, 4, v210
v_mfma_f32_16x16x32_bf16 a[104:107], a[172:175], v[176:179], a[104:107]
v_exp_f32_e64 v164, v164
v_lshl_add_u32 v210, v209, 7, v210
v_add_u32_e64 v251, s31, v210
v_mfma_f32_16x16x32_bf16 a[104:107], a[176:179], v[188:191], a[104:107]
s_waitcnt lgkmcnt(0)
v_exp_f32 v165, v165
v_rcp_f32 v134, v134
v_rcp_f32 v135, v135
v_xor_b32 v252, 64, v251
s_lshr_b32 s30, s20, 1
s_lshl_b32 s30, s30, 6
v_xor_b32 v254, s30, v210
s_and_b32 s30, s20, 1
s_lshl_b32 s30, s30, 3
v_add_u32 v254, s30, v254
s_lshl_b32 s30, s20, 11
v_add_u32_e64 v255, s30, v255
s_waitcnt vmcnt(0)
s_barrier
v_pk_add_f32 v[158:159], v[158:159], 1.0 op_sel_hi:[1,0]
v_pk_add_f32 v[156:157], v[156:157], 1.0 op_sel_hi:[1,0]
v_pk_add_f32 v[162:163], v[162:163], 1.0 op_sel_hi:[1,0]
v_pk_add_f32 v[160:161], v[160:161], 1.0 op_sel_hi:[1,0]
v_pk_add_f32 v[166:167], v[166:167], 1.0 op_sel_hi:[1,0]
v_pk_add_f32 v[164:165], v[164:165], 1.0 op_sel_hi:[1,0]
v_pk_mul_f32 v[134:135], v[72:73], v[134:135]
v_pk_mul_f32 v[132:133], v[74:75], v[132:133]
v_pk_mul_f32 v[138:139], v[76:77], v[138:139]
v_pk_mul_f32 v[136:137], v[78:79], v[136:137]
v_pk_mul_f32 v[142:143], v[80:81], v[142:143]
v_pk_mul_f32 v[140:141], v[82:83], v[140:141]
v_mfma_f32_16x16x32_bf16 a[108:111], a[180:183], v[168:171], a[108:111]
ds_read_b128 a[156:159], v238 offset:24576
v_cvt_pk_bf16_f32 v204, v134, v135
v_mfma_f32_16x16x32_bf16 a[108:111], a[184:187], v[180:183], a[108:111]
ds_read_b128 v[228:231], v239 offset:55296
v_cvt_pk_bf16_f32 v205, v132, v133
v_mfma_f32_16x16x32_bf16 a[112:115], a[184:187], v[184:187], a[112:115]
ds_read_b128 a[160:163], v238 offset:25600
v_cvt_pk_bf16_f32 v208, v138, v139
v_mfma_f32_16x16x32_bf16 a[112:115], a[180:183], v[172:175], a[112:115]
v_rcp_f32_e64 v158, v158
v_mfma_f32_16x16x32_bf16 a[116:119], a[180:183], v[176:179], a[116:119]
ds_read_b128 v[244:247], v239 offset:56320
ds_read_b128 a[164:167], v238 offset:26624
v_mfma_f32_16x16x32_bf16 a[116:119], a[184:187], v[188:191], a[116:119]
v_rcp_f32_e64 v159, v159
v_mfma_f32_16x16x32_bf16 a[128:131], a[192:195], v[188:191], a[128:131]
ds_read_b128 a[168:171], v238 offset:27648
v_cvt_pk_bf16_f32 v209, v136, v137
v_mfma_f32_16x16x32_bf16 a[128:131], a[188:191], v[176:179], a[128:131]
ds_read_b128 a[204:207], v249 offset:24576
v_cvt_pk_bf16_f32 v212, v142, v143
v_mfma_f32_16x16x32_bf16 a[124:127], a[188:191], v[172:175], a[124:127]
ds_read_b128 a[172:175], v238 offset:28672
v_cvt_pk_bf16_f32 v213, v140, v141
v_mfma_f32_16x16x32_bf16 a[124:127], a[192:195], v[184:187], a[124:127]
v_rcp_f32_e64 v156, v156
v_mfma_f32_16x16x32_bf16 a[120:123], a[192:195], v[180:183], a[120:123]
ds_read_b128 a[208:211], v249 offset:25600
ds_read_b128 a[176:179], v238 offset:29696
v_mfma_f32_16x16x32_bf16 a[120:123], a[188:191], v[168:171], a[120:123]
v_rcp_f32_e64 v157, v157
v_mfma_f32_16x16x32_bf16 a[132:135], a[196:199], v[168:171], a[132:135]
ds_read_b128 a[228:231], v238 offset:30720
v_mfma_f32_16x16x32_bf16 a[132:135], a[200:203], v[180:183], a[132:135]
ds_read_b128 a[212:215], v249 offset:32768
ds_read_b128 a[224:227], v249 offset:41984
v_mfma_f32_16x16x32_bf16 a[136:139], a[200:203], v[184:187], a[136:139]
ds_read_b128 a[216:219], v249 offset:33792
ds_read_b128 a[220:223], v249 offset:40960
v_mfma_f32_16x16x32_bf16 a[136:139], a[196:199], v[172:175], a[136:139]
s_waitcnt lgkmcnt(11)
ds_read_b128 a[232:235], v238 offset:31744
v_mfma_f32_16x16x32_bf16 a[140:143], a[196:199], v[176:179], a[140:143]
v_rcp_f32 v162, v162
v_mfma_f32_16x16x32_bf16 a[140:143], a[200:203], v[188:191], a[140:143]
s_waitcnt lgkmcnt(5)
v_rcp_f32 v163, v163
v_mfma_f32_16x16x32_bf16 a[144:147], a[204:207], v[228:231], a[144:147]
v_rcp_f32_e64 v160, v160
v_mfma_f32_16x16x32_bf16 a[144:147], a[208:211], v[244:247], a[144:147]
s_waitcnt lgkmcnt(1)
v_rcp_f32 v161, v161
v_mfma_f32_16x16x32_bf16 a[148:151], a[216:219], v[244:247], a[148:151]
v_rcp_f32_e64 v166, v166
v_mfma_f32_16x16x32_bf16 a[148:151], a[212:215], v[228:231], a[148:151]
v_rcp_f32_e64 v167, v167
v_mfma_f32_16x16x32_bf16 a[152:155], a[220:223], v[228:231], a[152:155]
v_rcp_f32_e64 v164, v164
v_mfma_f32_16x16x32_bf16 a[152:155], a[224:227], v[244:247], a[152:155]
s_waitcnt lgkmcnt(0)
v_rcp_f32 v165, v165
v_pk_mul_f32 v[158:159], v[144:145], v[158:159]
v_pk_mul_f32 v[156:157], v[146:147], v[156:157]
v_pk_mul_f32 v[162:163], v[148:149], v[162:163]
v_pk_mul_f32 v[160:161], v[150:151], v[160:161]
v_pk_mul_f32 v[166:167], v[152:153], v[166:167]
v_pk_mul_f32 v[164:165], v[154:155], v[164:165]
v_cvt_pk_bf16_f32 v206, v158, v159
v_cvt_pk_bf16_f32 v207, v156, v157
v_cvt_pk_bf16_f32 v210, v162, v163
v_cvt_pk_bf16_f32 v211, v160, v161
v_cvt_pk_bf16_f32 v214, v166, v167
v_cvt_pk_bf16_f32 v215, v164, v165
v_mfma_f32_16x16x32_bf16 a[0:3], a[156:159], v[192:195], a[0:3]
ds_read_b128 a[180:183], v238 offset:32768
ds_read_b128 a[184:187], v238 offset:33792
v_accvgpr_read_b32 v0, a144
v_accvgpr_read_b32 v1, a145
v_accvgpr_read_b32 v2, a146
v_mfma_f32_16x16x32_bf16 a[0:3], a[160:163], v[204:207], a[0:3]
ds_read_b128 a[188:191], v238 offset:34816
ds_read_b128 a[192:195], v238 offset:35840
v_accvgpr_read_b32 v3, a147
v_cvt_pk_bf16_f32 v136, v0, v1
v_cvt_pk_bf16_f32 v137, v2, v3
v_mfma_f32_16x16x32_bf16 a[12:15], a[168:171], v[204:207], a[12:15]
ds_write_b64 v254, v[136:137] offset:0
v_accvgpr_read_b32 v4, a148
v_accvgpr_read_b32 v5, a149
v_mfma_f32_16x16x32_bf16 a[12:15], a[164:167], v[192:195], a[12:15]
ds_read_b128 a[196:199], v238 offset:36864
ds_read_b128 a[200:203], v238 offset:37888
v_accvgpr_read_b32 v6, a150
v_accvgpr_read_b32 v7, a151
v_mfma_f32_16x16x32_bf16 a[24:27], a[172:175], v[192:195], a[24:27]
ds_read_b128 a[236:239], v238 offset:38912
ds_read_b128 a[240:243], v238 offset:39936
v_cvt_pk_bf16_f32 v138, v4, v5
v_cvt_pk_bf16_f32 v139, v6, v7
v_mfma_f32_16x16x32_bf16 a[24:27], a[176:179], v[204:207], a[24:27]
ds_write_b64 v254, v[138:139] offset:2048
v_accvgpr_read_b32 v8, a152
v_accvgpr_read_b32 v9, a153
v_mfma_f32_16x16x32_bf16 a[36:39], a[232:235], v[204:207], a[36:39]
v_accvgpr_read_b32 v10, a154
v_accvgpr_read_b32 v11, a155
v_mfma_f32_16x16x32_bf16 a[36:39], a[228:231], v[192:195], a[36:39]
v_cvt_pk_bf16_f32 v140, v8, v9
v_cvt_pk_bf16_f32 v141, v10, v11
v_mfma_f32_16x16x32_bf16 a[4:7], a[156:159], v[196:199], a[4:7]
ds_write_b64 v254, v[140:141] offset:4096
v_accvgpr_read_b32 v156, a140
v_mfma_f32_16x16x32_bf16 a[4:7], a[160:163], v[208:211], a[4:7]
v_accvgpr_read_b32 v40, a0
v_accvgpr_read_b32 v41, a1
v_accvgpr_read_b32 v42, a2
v_accvgpr_read_b32 v43, a3
v_mfma_f32_16x16x32_bf16 a[16:19], a[168:171], v[208:211], a[16:19]
v_accvgpr_read_b32 v44, a12
v_accvgpr_read_b32 v45, a13
v_accvgpr_read_b32 v46, a14
v_accvgpr_read_b32 v47, a15
v_mfma_f32_16x16x32_bf16 a[16:19], a[164:167], v[196:199], a[16:19]
v_cvt_pk_bf16_f32 v16, v40, v41
v_cvt_pk_bf16_f32 v17, v42, v43
v_cvt_pk_bf16_f32 v18, v44, v45
v_cvt_pk_bf16_f32 v19, v46, v47
v_mfma_f32_16x16x32_bf16 a[28:31], a[172:175], v[196:199], a[28:31]
s_waitcnt lgkmcnt(0)
s_barrier
v_accvgpr_read_b32 v48, a24
v_accvgpr_read_b32 v49, a25
v_accvgpr_read_b32 v50, a26
v_mfma_f32_16x16x32_bf16 a[28:31], a[176:179], v[208:211], a[28:31]
ds_read_b128 v[0:3], v255 offset:0
ds_read_b128 v[4:7], v255 offset:1024
v_accvgpr_read_b32 v51, a27
v_accvgpr_read_b32 v52, a36
v_accvgpr_read_b32 v53, a37
v_mfma_f32_16x16x32_bf16 a[40:43], a[232:235], v[208:211], a[40:43]
ds_write_b128 v251, v[16:19] offset:0
v_accvgpr_read_b32 v54, a38
v_accvgpr_read_b32 v55, a39
v_cvt_pk_bf16_f32 v20, v48, v49
v_mfma_f32_16x16x32_bf16 a[40:43], a[228:231], v[196:199], a[40:43]
v_cvt_pk_bf16_f32 v21, v50, v51
v_cvt_pk_bf16_f32 v22, v52, v53
v_cvt_pk_bf16_f32 v23, v54, v55
v_mfma_f32_16x16x32_bf16 a[8:11], a[156:159], v[200:203], a[8:11]
ds_write_b128 v252, v[20:23] offset:0
v_accvgpr_read_b32 v157, a141
v_mfma_f32_16x16x32_bf16 a[8:11], a[160:163], v[212:215], a[8:11]
s_waitcnt lgkmcnt(3)
buffer_store_dwordx4 v[0:3], v250, s[16:19], s47 offen offset:0 sc1
v_accvgpr_read_b32 v56, a4
v_accvgpr_read_b32 v57, a5
v_accvgpr_read_b32 v58, a6
v_accvgpr_read_b32 v59, a7
v_mfma_f32_16x16x32_bf16 a[20:23], a[168:171], v[212:215], a[20:23]
s_waitcnt lgkmcnt(2)
buffer_store_dwordx4 v[4:7], v250, s[16:19], s47 offen offset:3072 sc1
v_accvgpr_read_b32 v60, a16
v_accvgpr_read_b32 v61, a17
v_accvgpr_read_b32 v62, a18
v_accvgpr_read_b32 v63, a19
v_mfma_f32_16x16x32_bf16 a[20:23], a[164:167], v[200:203], a[20:23]
ds_read_b128 v[24:27], v253 offset:0
ds_read_b128 v[28:31], v253 offset:1024
v_cvt_pk_bf16_f32 v12, v56, v57
v_cvt_pk_bf16_f32 v13, v58, v59
v_cvt_pk_bf16_f32 v14, v60, v61
v_cvt_pk_bf16_f32 v15, v62, v63
v_mfma_f32_16x16x32_bf16 a[32:35], a[172:175], v[200:203], a[32:35]
ds_write_b128 v251, v[12:15] offset:2048
v_accvgpr_read_b32 v64, a28
v_accvgpr_read_b32 v65, a29
v_accvgpr_read_b32 v66, a30
v_mfma_f32_16x16x32_bf16 a[32:35], a[176:179], v[212:215], a[32:35]
v_accvgpr_read_b32 v67, a31
v_accvgpr_read_b32 v68, a40
v_accvgpr_read_b32 v69, a41
v_mfma_f32_16x16x32_bf16 a[44:47], a[232:235], v[212:215], a[44:47]
v_accvgpr_read_b32 v70, a42
v_accvgpr_read_b32 v71, a43
v_cvt_pk_bf16_f32 v16, v64, v65
v_mfma_f32_16x16x32_bf16 a[44:47], a[228:231], v[200:203], a[44:47]
s_waitcnt lgkmcnt(2)
buffer_store_dwordx4 v[24:27], v250, s[16:19], s44 offen offset:0 sc1
v_cvt_pk_bf16_f32 v17, v66, v67
v_cvt_pk_bf16_f32 v18, v68, v69
v_cvt_pk_bf16_f32 v19, v70, v71
v_mfma_f32_16x16x32_bf16 a[48:51], a[180:183], v[192:195], a[48:51]
s_waitcnt lgkmcnt(1)
buffer_store_dwordx4 v[28:31], v250, s[16:19], s44 offen offset:3072 sc1
v_mfma_f32_16x16x32_bf16 a[48:51], a[184:187], v[204:207], a[48:51]
ds_write_b128 v252, v[16:19] offset:2048
v_accvgpr_read_b32 v72, a8
v_accvgpr_read_b32 v73, a9
v_accvgpr_read_b32 v74, a10
v_accvgpr_read_b32 v75, a11
v_mfma_f32_16x16x32_bf16 a[60:63], a[192:195], v[204:207], a[60:63]
ds_read_b128 v[32:35], v253 offset:2048
ds_read_b128 v[36:39], v253 offset:3072
v_accvgpr_read_b32 v76, a20
v_accvgpr_read_b32 v77, a21
v_accvgpr_read_b32 v78, a22
v_accvgpr_read_b32 v79, a23
v_mfma_f32_16x16x32_bf16 a[60:63], a[188:191], v[192:195], a[60:63]
ds_read_b128 a[156:159], v238 offset:40960
ds_read_b128 a[160:163], v238 offset:41984
v_cvt_pk_bf16_f32 v20, v72, v73
v_cvt_pk_bf16_f32 v21, v74, v75
v_cvt_pk_bf16_f32 v22, v76, v77
v_cvt_pk_bf16_f32 v23, v78, v79
v_mfma_f32_16x16x32_bf16 a[72:75], a[196:199], v[192:195], a[72:75]
ds_write_b128 v251, v[20:23] offset:4096
v_accvgpr_read_b32 v80, a32
v_accvgpr_read_b32 v81, a33
v_accvgpr_read_b32 v82, a34
v_mfma_f32_16x16x32_bf16 a[72:75], a[200:203], v[204:207], a[72:75]
ds_read_b128 a[164:167], v238 offset:43008
ds_read_b128 a[168:171], v238 offset:44032
v_accvgpr_read_b32 v83, a35
v_accvgpr_read_b32 v84, a44
v_accvgpr_read_b32 v85, a45
v_mfma_f32_16x16x32_bf16 a[84:87], a[240:243], v[204:207], a[84:87]
s_waitcnt lgkmcnt(6)
buffer_store_dwordx4 v[32:35], v250, s[16:19], s45 offen offset:0 sc1
v_accvgpr_read_b32 v86, a46
v_accvgpr_read_b32 v87, a47
v_cvt_pk_bf16_f32 v12, v80, v81
v_mfma_f32_16x16x32_bf16 a[84:87], a[236:239], v[192:195], a[84:87]
s_waitcnt lgkmcnt(5)
buffer_store_dwordx4 v[36:39], v250, s[16:19], s45 offen offset:3072 sc1
v_cvt_pk_bf16_f32 v13, v82, v83
v_cvt_pk_bf16_f32 v14, v84, v85
v_cvt_pk_bf16_f32 v15, v86, v87
v_mfma_f32_16x16x32_bf16 a[52:55], a[180:183], v[196:199], a[52:55]
ds_write_b128 v252, v[12:15] offset:4096
v_accvgpr_read_b32 v158, a142
v_mfma_f32_16x16x32_bf16 a[52:55], a[184:187], v[208:211], a[52:55]
ds_read_b128 v[24:27], v253 offset:4096
ds_read_b128 v[28:31], v253 offset:5120
v_accvgpr_read_b32 v88, a48
v_accvgpr_read_b32 v89, a49
v_accvgpr_read_b32 v90, a50
v_accvgpr_read_b32 v91, a51
v_mfma_f32_16x16x32_bf16 a[64:67], a[192:195], v[208:211], a[64:67]
ds_read_b128 a[172:175], v238 offset:45056
ds_read_b128 a[176:179], v238 offset:46080
v_accvgpr_read_b32 v92, a60
v_accvgpr_read_b32 v93, a61
v_accvgpr_read_b32 v94, a62
v_accvgpr_read_b32 v95, a63
v_mfma_f32_16x16x32_bf16 a[64:67], a[188:191], v[196:199], a[64:67]
ds_read_b128 a[228:231], v238 offset:47104
ds_read_b128 a[232:235], v238 offset:48128
v_cvt_pk_bf16_f32 v16, v88, v89
v_cvt_pk_bf16_f32 v17, v90, v91
v_cvt_pk_bf16_f32 v18, v92, v93
v_cvt_pk_bf16_f32 v19, v94, v95
v_mfma_f32_16x16x32_bf16 a[76:79], a[196:199], v[196:199], a[76:79]
ds_write_b128 v251, v[16:19] offset:6144
v_accvgpr_read_b32 v96, a72
v_accvgpr_read_b32 v97, a73
v_accvgpr_read_b32 v98, a74
v_mfma_f32_16x16x32_bf16 a[76:79], a[200:203], v[208:211], a[76:79]
s_waitcnt lgkmcnt(6)
buffer_store_dwordx4 v[24:27], v250, s[16:19], s46 offen offset:0 sc1
v_accvgpr_read_b32 v99, a75
v_accvgpr_read_b32 v100, a84
v_accvgpr_read_b32 v101, a85
v_mfma_f32_16x16x32_bf16 a[88:91], a[240:243], v[208:211], a[88:91]
s_waitcnt lgkmcnt(5)
buffer_store_dwordx4 v[28:31], v250, s[16:19], s46 offen offset:3072 sc1
v_accvgpr_read_b32 v102, a86
v_accvgpr_read_b32 v103, a87
v_cvt_pk_bf16_f32 v20, v96, v97
v_mfma_f32_16x16x32_bf16 a[88:91], a[236:239], v[196:199], a[88:91]
v_cvt_pk_bf16_f32 v21, v98, v99
v_cvt_pk_bf16_f32 v22, v100, v101
v_cvt_pk_bf16_f32 v23, v102, v103
v_mfma_f32_16x16x32_bf16 a[56:59], a[180:183], v[200:203], a[56:59]
ds_write_b128 v252, v[20:23] offset:6144
v_accvgpr_read_b32 v159, a143
v_mfma_f32_16x16x32_bf16 a[56:59], a[184:187], v[212:215], a[56:59]
ds_read_b128 v[32:35], v253 offset:6144
ds_read_b128 v[36:39], v253 offset:7168
v_accvgpr_read_b32 v104, a52
v_accvgpr_read_b32 v105, a53
v_accvgpr_read_b32 v106, a54
v_accvgpr_read_b32 v107, a55
v_mfma_f32_16x16x32_bf16 a[68:71], a[192:195], v[212:215], a[68:71]
v_accvgpr_read_b32 v108, a64
v_accvgpr_read_b32 v109, a65
v_accvgpr_read_b32 v110, a66
v_accvgpr_read_b32 v111, a67
v_mfma_f32_16x16x32_bf16 a[68:71], a[188:191], v[200:203], a[68:71]
v_cvt_pk_bf16_f32 v12, v104, v105
v_cvt_pk_bf16_f32 v13, v106, v107
v_cvt_pk_bf16_f32 v14, v108, v109
v_cvt_pk_bf16_f32 v15, v110, v111
v_mfma_f32_16x16x32_bf16 a[80:83], a[196:199], v[200:203], a[80:83]
ds_write_b128 v251, v[12:15] offset:8192
v_accvgpr_read_b32 v112, a76
v_accvgpr_read_b32 v113, a77
v_accvgpr_read_b32 v114, a78
v_mfma_f32_16x16x32_bf16 a[80:83], a[200:203], v[212:215], a[80:83]
s_waitcnt lgkmcnt(2)
buffer_store_dwordx4 v[32:35], v250, s[16:19], s44 offen offset:128 sc1
v_accvgpr_read_b32 v115, a79
v_accvgpr_read_b32 v116, a88
v_accvgpr_read_b32 v117, a89
v_mfma_f32_16x16x32_bf16 a[92:95], a[240:243], v[212:215], a[92:95]
s_waitcnt lgkmcnt(1)
buffer_store_dwordx4 v[36:39], v250, s[16:19], s44 offen offset:3200 sc1
v_accvgpr_read_b32 v118, a90
v_accvgpr_read_b32 v119, a91
v_cvt_pk_bf16_f32 v16, v112, v113
v_mfma_f32_16x16x32_bf16 a[92:95], a[236:239], v[200:203], a[92:95]
v_cvt_pk_bf16_f32 v17, v114, v115
v_cvt_pk_bf16_f32 v18, v116, v117
v_cvt_pk_bf16_f32 v19, v118, v119
v_mfma_f32_16x16x32_bf16 a[96:99], a[156:159], v[192:195], a[96:99]
ds_write_b128 v252, v[16:19] offset:8192
v_accvgpr_read_b32 v152, a128
v_mfma_f32_16x16x32_bf16 a[96:99], a[160:163], v[204:207], a[96:99]
ds_read_b128 v[24:27], v253 offset:8192
ds_read_b128 v[28:31], v253 offset:9216
v_accvgpr_read_b32 v120, a56
v_accvgpr_read_b32 v121, a57
v_accvgpr_read_b32 v122, a58
v_accvgpr_read_b32 v123, a59
v_mfma_f32_16x16x32_bf16 a[108:111], a[168:171], v[204:207], a[108:111]
v_accvgpr_read_b32 v124, a68
v_accvgpr_read_b32 v125, a69
v_accvgpr_read_b32 v126, a70
v_accvgpr_read_b32 v127, a71
v_mfma_f32_16x16x32_bf16 a[108:111], a[164:167], v[192:195], a[108:111]
v_cvt_pk_bf16_f32 v20, v120, v121
v_cvt_pk_bf16_f32 v21, v122, v123
v_cvt_pk_bf16_f32 v22, v124, v125
v_cvt_pk_bf16_f32 v23, v126, v127
v_mfma_f32_16x16x32_bf16 a[120:123], a[172:175], v[192:195], a[120:123]
ds_write_b128 v251, v[20:23] offset:10240
v_accvgpr_read_b32 v128, a80
v_accvgpr_read_b32 v129, a81
v_accvgpr_read_b32 v130, a82
v_mfma_f32_16x16x32_bf16 a[120:123], a[176:179], v[204:207], a[120:123]
s_waitcnt lgkmcnt(2)
buffer_store_dwordx4 v[24:27], v250, s[16:19], s45 offen offset:128 sc1
v_accvgpr_read_b32 v131, a83
v_accvgpr_read_b32 v132, a92
v_accvgpr_read_b32 v133, a93
v_mfma_f32_16x16x32_bf16 a[132:135], a[232:235], v[204:207], a[132:135]
s_waitcnt lgkmcnt(1)
buffer_store_dwordx4 v[28:31], v250, s[16:19], s45 offen offset:3200 sc1
v_accvgpr_read_b32 v134, a94
v_accvgpr_read_b32 v135, a95
v_cvt_pk_bf16_f32 v12, v128, v129
v_mfma_f32_16x16x32_bf16 a[132:135], a[228:231], v[192:195], a[132:135]
v_cvt_pk_bf16_f32 v13, v130, v131
v_cvt_pk_bf16_f32 v14, v132, v133
v_cvt_pk_bf16_f32 v15, v134, v135
v_mfma_f32_16x16x32_bf16 a[100:103], a[156:159], v[196:199], a[100:103]
ds_write_b128 v252, v[12:15] offset:10240
v_accvgpr_read_b32 v153, a129
v_mfma_f32_16x16x32_bf16 a[100:103], a[160:163], v[208:211], a[100:103]
ds_read_b128 v[32:35], v253 offset:10240
ds_read_b128 v[36:39], v253 offset:11264
v_accvgpr_read_b32 v40, a96
v_accvgpr_read_b32 v41, a97
v_accvgpr_read_b32 v42, a98
v_accvgpr_read_b32 v43, a99
v_mfma_f32_16x16x32_bf16 a[112:115], a[168:171], v[208:211], a[112:115]
v_accvgpr_read_b32 v44, a108
v_accvgpr_read_b32 v45, a109
v_accvgpr_read_b32 v46, a110
v_accvgpr_read_b32 v47, a111
v_mfma_f32_16x16x32_bf16 a[112:115], a[164:167], v[196:199], a[112:115]
v_cvt_pk_bf16_f32 v16, v40, v41
v_cvt_pk_bf16_f32 v17, v42, v43
v_cvt_pk_bf16_f32 v18, v44, v45
v_cvt_pk_bf16_f32 v19, v46, v47
v_mfma_f32_16x16x32_bf16 a[124:127], a[172:175], v[196:199], a[124:127]
ds_write_b128 v251, v[16:19] offset:0
v_accvgpr_read_b32 v48, a120
v_accvgpr_read_b32 v49, a121
v_accvgpr_read_b32 v50, a122
v_mfma_f32_16x16x32_bf16 a[124:127], a[176:179], v[208:211], a[124:127]
s_waitcnt lgkmcnt(2)
buffer_store_dwordx4 v[32:35], v250, s[16:19], s46 offen offset:128 sc1
v_accvgpr_read_b32 v51, a123
v_accvgpr_read_b32 v52, a132
v_accvgpr_read_b32 v53, a133
v_mfma_f32_16x16x32_bf16 a[136:139], a[232:235], v[208:211], a[136:139]
s_waitcnt lgkmcnt(1)
buffer_store_dwordx4 v[36:39], v250, s[16:19], s46 offen offset:3200 sc1
v_accvgpr_read_b32 v54, a134
v_accvgpr_read_b32 v55, a135
v_cvt_pk_bf16_f32 v20, v48, v49
v_mfma_f32_16x16x32_bf16 a[136:139], a[228:231], v[196:199], a[136:139]
v_cvt_pk_bf16_f32 v21, v50, v51
v_cvt_pk_bf16_f32 v22, v52, v53
v_cvt_pk_bf16_f32 v23, v54, v55
v_mfma_f32_16x16x32_bf16 a[104:107], a[156:159], v[200:203], a[104:107]
ds_write_b128 v252, v[20:23] offset:0
v_accvgpr_read_b32 v154, a130
v_mfma_f32_16x16x32_bf16 a[104:107], a[160:163], v[212:215], a[104:107]
ds_read_b128 v[24:27], v253 offset:0
ds_read_b128 v[28:31], v253 offset:1024
v_accvgpr_read_b32 v168, a100
v_accvgpr_read_b32 v169, a101
v_accvgpr_read_b32 v170, a102
v_accvgpr_read_b32 v171, a103
v_mfma_f32_16x16x32_bf16 a[116:119], a[168:171], v[212:215], a[116:119]
v_accvgpr_read_b32 v172, a112
v_accvgpr_read_b32 v173, a113
v_accvgpr_read_b32 v174, a114
v_accvgpr_read_b32 v175, a115
v_mfma_f32_16x16x32_bf16 a[116:119], a[164:167], v[200:203], a[116:119]
v_cvt_pk_bf16_f32 v12, v168, v169
v_cvt_pk_bf16_f32 v13, v170, v171
v_cvt_pk_bf16_f32 v14, v172, v173
v_cvt_pk_bf16_f32 v15, v174, v175
v_accvgpr_read_b32 v155, a131
s_nop 1
v_mfma_f32_16x16x32_bf16 v[152:155], a[172:175], v[200:203], v[152:155]
s_nop 0
ds_write_b128 v251, v[12:15] offset:2048
v_accvgpr_read_b32 v176, a124
v_accvgpr_read_b32 v177, a125
v_accvgpr_read_b32 v178, a126
v_mfma_f32_16x16x32_bf16 v[152:155], a[176:179], v[212:215], v[152:155]
s_waitcnt lgkmcnt(2)
buffer_store_dwordx4 v[24:27], v250, s[16:19], s44 offen offset:256 sc1
v_accvgpr_read_b32 v179, a127
v_accvgpr_read_b32 v180, a136
v_accvgpr_read_b32 v181, a137
v_mfma_f32_16x16x32_bf16 v[156:159], a[232:235], v[212:215], v[156:159]
s_waitcnt lgkmcnt(1)
buffer_store_dwordx4 v[28:31], v250, s[16:19], s44 offen offset:3328 sc1
v_accvgpr_read_b32 v182, a138
v_accvgpr_read_b32 v183, a139
v_cvt_pk_bf16_f32 v16, v176, v177
v_mfma_f32_16x16x32_bf16 v[156:159], a[228:231], v[200:203], v[156:159]
v_cvt_pk_bf16_f32 v17, v178, v179
v_cvt_pk_bf16_f32 v18, v180, v181
v_cvt_pk_bf16_f32 v19, v182, v183
ds_write_b128 v252, v[16:19] offset:2048
ds_read_b128 v[32:35], v253 offset:2048
ds_read_b128 v[36:39], v253 offset:3072
v_accvgpr_read_b32 v144, a104
v_accvgpr_read_b32 v145, a105
v_accvgpr_read_b32 v146, a106
v_accvgpr_read_b32 v147, a107
v_accvgpr_read_b32 v148, a116
v_accvgpr_read_b32 v149, a117
v_accvgpr_read_b32 v150, a118
v_accvgpr_read_b32 v151, a119
v_cvt_pk_bf16_f32 v20, v144, v145
v_cvt_pk_bf16_f32 v21, v146, v147
v_cvt_pk_bf16_f32 v22, v148, v149
v_cvt_pk_bf16_f32 v23, v150, v151
ds_write_b128 v251, v[20:23] offset:4096
v_cvt_pk_bf16_f32 v12, v152, v153
v_cvt_pk_bf16_f32 v13, v154, v155
v_cvt_pk_bf16_f32 v14, v156, v157
v_cvt_pk_bf16_f32 v15, v158, v159
ds_write_b128 v252, v[12:15] offset:4096
ds_read_b128 v[24:27], v253 offset:4096
ds_read_b128 v[28:31], v253 offset:5120
s_waitcnt lgkmcnt(5)
buffer_store_dwordx4 v[32:35], v250, s[16:19], s45 offen offset:256 sc1
s_waitcnt lgkmcnt(4)
buffer_store_dwordx4 v[36:39], v250, s[16:19], s45 offen offset:3328 sc1
s_waitcnt lgkmcnt(1)
buffer_store_dwordx4 v[24:27], v250, s[16:19], s46 offen offset:256 sc1
s_waitcnt lgkmcnt(0)
buffer_store_dwordx4 v[28:31], v250, s[16:19], s46 offen offset:3328 sc1
s_or_b32 s30, s42, s20
s_cbranch_scc1 L_nobump
v_mbcnt_lo_u32_b32 v1, -1, 0
v_mbcnt_hi_u32_b32 v1, -1, v1
v_lshlrev_b32_e64 v8, 4, v1
s_mov_b32 s30, 589824
v_accvgpr_read_b32 v4, a252
v_accvgpr_read_b32 v5, a253
v_accvgpr_read_b32 v6, a254
v_accvgpr_read_b32 v7, a255
s_mov_b32 s26, 0
L_poll_xf:
v_cmp_eq_u32 vcc, s43, v4
v_cmp_eq_u32 s[34:35], s43, v5
v_cmp_eq_u32 s[64:65], s43, v6
v_cmp_eq_u32 s[66:67], s43, v7
s_nop 3
s_and_b64 vcc, vcc, s[34:35]
s_and_b64 s[64:65], s[64:65], s[66:67]
s_and_b64 vcc, vcc, s[64:65]
s_bitset1_b64 vcc, 63
s_cmp_eq_u64 vcc, -1
s_cbranch_scc1 L_go_xf
s_add_u32 s26, s26, 1
s_cmp_gt_u32 s26, 200000
s_cbranch_scc1 L_go_xf
s_sleep 4
s_mov_b32 s27, 589824
buffer_load_dwordx4 v[4:7], v8, s[12:15], s27 offen sc1
s_waitcnt vmcnt(0)
s_branch L_poll_xf
L_go_xf:
v_cmp_eq_u32 vcc, 0, v1
s_sub_u32 s31, s43, 0x9e3779b8
v_mov_b32 v2, 0x70000000
v_mov_b32 v3, s31
s_mov_b32 s30, 590848
v_cndmask_b32 v2, v2, 0, vcc
buffer_store_dword v3, v2, s[12:15], s30 offen sc1
L_nobump:
s_waitcnt vmcnt(0)
s_endpgm
L_slow_x:
s_mov_b32 s26, 0
L_poll_x:
v_cmp_eq_u32 vcc, s43, v204
v_cmp_eq_u32 s[34:35], s43, v205
v_cmp_eq_u32 s[64:65], s43, v206
v_cmp_eq_u32 s[66:67], s43, v207
s_nop 3
s_and_b64 vcc, vcc, s[34:35]
s_and_b64 s[64:65], s[64:65], s[66:67]
s_and_b64 vcc, vcc, s[64:65]
s_bitset1_b64 vcc, 63
s_or_b32 vcc_lo, vcc_lo, 0x0fffffff
s_cmp_eq_u64 vcc, -1
s_cbranch_scc1 L_go_x
s_add_u32 s26, s26, 1
s_cmp_gt_u32 s26, 200000
s_cbranch_scc1 L_go_x
s_sleep 4
s_mov_b32 s27, 589824
buffer_load_dwordx4 v[204:207], v238, s[12:15], s27 offen sc1
s_waitcnt vmcnt(0)
s_branch L_poll_x
L_go_x:
s_branch L_fast_x
L_sy_x7:
ds_read_b32 v255, v252 offset:0
s_waitcnt lgkmcnt(0)
v_cmp_ne_u32 vcc, s33, v255
s_nop 4
s_cbranch_vccnz L_sy_x7
s_branch L_syb_x7
L_sy_x11:
ds_read_b32 v255, v252 offset:4
s_waitcnt lgkmcnt(0)
v_cmp_ne_u32 vcc, s33, v255
s_nop 4
s_cbranch_vccnz L_sy_x11
s_branch L_syb_x11
L_sy_x47:
ds_read_b32 v255, v252 offset:0
s_waitcnt lgkmcnt(0)
v_cmp_ne_u32 vcc, s33, v255
s_nop 4
s_cbranch_vccnz L_sy_x47
s_branch L_syb_x47
s_endpgm
L_end_x:
.p2align 6
L_beg_p:
v_lshrrev_b32 v1, 6, v0
s_nop 0
v_readfirstlane_b32 s20, v1
s_lshl_b32 s21, s20, 10
s_mul_i32 s27, s24, 224
s_add_u32 s27, s27, s25
s_sub_u32 s27, s27, 225
s_cmp_eq_u32 s25, 0
v_and_b32 v2, 63, v0
s_cselect_b32 s33, 0, 63
s_cmp_eq_u32 s25, 208
v_lshrrev_b32 v5, 3, v2
s_cselect_b32 s33, 17, s33
s_lshl_b32 s31, s20, 3
v_add_u32 v8, s31, v5
s_mov_b32 s62, 206
v_mul_u32_u24 v64, 3641, v8
v_add_u32 v73, 32, v8
v_lshrrev_b32 v65, 16, v64
s_mov_b32 s63, -18
v_mad_i32_i24 v66, v65, s63, v8
v_mad_u32_u24 v67, v65, s62, v8
v_mul_u32_u24 v74, 3641, v73
v_and_b32 v6, 7, v2
v_add_u32 v68, s27, v67
v_and_b32 v69, 6, v66
v_lshrrev_b32 v75, 16, v74
v_add_u32 v83, 64, v8
v_xor_b32 v70, v69, v6
v_mul_i32_i24 v71, 384, v68
v_mad_i32_i24 v76, v75, s63, v73
v_mad_u32_u24 v77, v75, s62, v73
v_mul_u32_u24 v84, 3641, v83
v_cmp_ne_u32 vcc, s33, v66
v_mov_b32 v7, 0x70000000
v_lshl_add_u32 v72, v70, 4, v71
v_add_u32 v78, s27, v77
v_and_b32 v79, 6, v76
v_lshrrev_b32 v85, 16, v84
v_cndmask_b32 v156, v7, v72, vcc
v_xor_b32 v80, v79, v6
v_mul_i32_i24 v81, 384, v78
v_mad_i32_i24 v86, v85, s63, v83
v_mad_u32_u24 v87, v85, s62, v83
v_cmp_ne_u32 vcc, s33, v76
v_lshl_add_u32 v82, v80, 4, v81
v_add_u32 v88, s27, v87
v_and_b32 v89, 6, v86
v_cndmask_b32 v157, v7, v82, vcc
v_xor_b32 v90, v89, v6
v_mul_i32_i24 v91, 384, v88
v_cmp_ne_u32 vcc, s33, v86
v_lshl_add_u32 v92, v90, 4, v91
v_lshlrev_b32 v31, 4, v2
v_cndmask_b32 v158, v7, v92, vcc
s_mov_b32 s10, 19267584
s_mov_b32 s11, 0x20000
s_mov_b32 s70, 3456
s_mov_b32 s71, 0x20000
s_add_u32 m0, s21, 137216
s_waitcnt lgkmcnt(0)
s_mov_b32 s68, s46
s_and_b32 s69, s47, 0xffff
s_nop 0
s_mov_b32 s8, s44
s_and_b32 s9, s45, 0xffff
buffer_load_dwordx4 v31, s[68:71], s21 offen lds
s_cmp_lt_u32 s20, 2
s_cselect_b32 s64, s50, s54
s_cselect_b32 s65, s51, s55
s_cselect_b32 s66, 2, 0
s_cselect_b32 s30, s21, 0
s_cmp_eq_u32 s20, 3
s_cselect_b32 s64, s56, s64
s_cselect_b32 s65, s57, s65
s_lshl_b32 s66, 384, s66
s_and_b32 s65, s65, 0xffff
s_mov_b32 s67, 0x20000
s_add_u32 m0, s21, 141312
s_nop 0
buffer_load_dwordx4 v31, s[64:67], s30 offen lds
s_add_u32 m0, s21, 0
v_add_u32 v64, 96, v8
buffer_load_dwordx4 v156, s[8:11], 0 offen lds
v_add_u32 v74, 128, v8
v_mul_u32_u24 v65, 3641, v64
v_mul_u32_u24 v75, 3641, v74
v_lshrrev_b32 v66, 16, v65
v_lshrrev_b32 v76, 16, v75
v_mad_i32_i24 v67, v66, s63, v64
v_mad_u32_u24 v68, v66, s62, v64
v_and_b32 v70, 6, v67
v_add_u32 v69, s27, v68
v_add_u32 v84, 160, v8
v_xor_b32_e64 v71, v70, v6
s_add_u32 m0, s21, 4096
v_mul_i32_i24 v72, 384, v69
buffer_load_dwordx4 v157, s[8:11], 0 offen lds
v_mad_i32_i24 v77, v76, s63, v74
v_mad_u32_u24 v78, v76, s62, v74
v_mul_u32_u24 v85, 3641, v84
v_cmp_ne_u32 vcc, s33, v67
v_lshl_add_u32 v73, v71, 4, v72
v_add_u32 v79, s27, v78
v_and_b32 v80, 6, v77
v_lshrrev_b32 v86, 16, v85
v_cndmask_b32 v159, v7, v73, vcc
v_xor_b32 v81, v80, v6
v_mul_i32_i24 v82, 384, v79
s_add_u32 m0, s21, 8192
v_mad_i32_i24 v87, v86, s63, v84
buffer_load_dwordx4 v158, s[8:11], 0 offen lds
v_mad_u32_u24 v88, v86, s62, v84
v_cmp_ne_u32 vcc, s33, v77
v_lshl_add_u32 v83, v81, 4, v82
v_add_u32 v89, s27, v88
v_and_b32 v90, 6, v87
v_cndmask_b32 v160, v7, v83, vcc
v_xor_b32 v91, v90, v6
v_mul_i32_i24 v92, 384, v89
v_cmp_ne_u32 vcc, s33, v87
v_lshl_add_u32 v93, v91, 4, v92
s_nop 0
v_cndmask_b32 v161, v7, v93, vcc
s_add_u32 m0, s21, 12288
v_add_u32 v64, 192, v8
buffer_load_dwordx4 v159, s[8:11], 0 offen lds
v_add_u32 v74, 224, v8
v_mul_u32_u24 v65, 3641, v64
v_mul_u32_u24 v75, 3641, v74
v_lshrrev_b32 v66, 16, v65
v_lshrrev_b32 v76, 16, v75
v_mad_i32_i24 v67, v66, s63, v64
v_mad_u32_u24 v68, v66, s62, v64
s_add_u32 m0, s21, 16384
v_and_b32_e64 v70, 6, v67
buffer_load_dwordx4 v160, s[8:11], 0 offen lds
v_add_u32 v69, s27, v68
v_xor_b32 v71, v70, v6
v_mul_i32_i24 v72, 384, v69
v_mad_i32_i24 v77, v76, s63, v74
v_mad_u32_u24 v78, v76, s62, v74
v_cmp_ne_u32 vcc, s33, v67
v_lshl_add_u32 v73, v71, 4, v72
s_add_u32 m0, s21, 20480
v_add_u32 v79, s27, v78
buffer_load_dwordx4 v161, s[8:11], 0 offen lds
v_and_b32 v80, 6, v77
v_cndmask_b32 v162, v7, v73, vcc
v_xor_b32 v81, v80, v6
v_mul_i32_i24 v82, 384, v79
v_cmp_ne_u32 vcc, s33, v77
v_lshl_add_u32 v83, v81, 4, v82
s_nop 0
v_cndmask_b32 v163, v7, v83, vcc
s_add_u32 m0, s21, 24576
v_and_b32_e64 v64, 15, v0
buffer_load_dwordx4 v162, s[8:11], 0 offen lds
v_lshrrev_b32 v70, 4, v0
v_lshrrev_b32 v77, 4, v2
v_mul_u32_u24 v71, 240, v70
v_mul_u32_u24 v72, 20, v64
v_and_b32 v76, 15, v2
v_and_b32 v79, 1, v77
v_add_u32 v73, v71, v72
v_cmp_gt_u32 vcc, 12, v64
v_lshrrev_b32 v84, 1, v76
v_lshlrev_b32_e64 v85, 2, v79
v_add_u32 v74, 157696, v73
v_mov_b32 v75, 163584
v_and_b32 v80, 1, v76
v_sub_u32 v86, v84, v85
v_cndmask_b32 v19, v75, v74, vcc
v_lshlrev_b32 v81, 4, v80
v_mov_b32 v82, 0xffff
v_cmp_eq_u32 vcc, 0, v86
v_min_u32 v66, 11, v64
v_lshrrev_b32 v78, 5, v2
v_lshlrev_b32 v83, v81, v82
v_add_u32_e64 v92, 2, v76
v_bfe_u32 v65, v0, 4, 3
v_lshlrev_b32 v67, 5, v66
v_cndmask_b32 v216, 0, v83, vcc
v_add_u32_e64 v87, v76, v78
v_and_b32 v93, 6, v92
v_cmp_eq_u32 vcc, 1, v86
v_lshl_add_u32 v68, v65, 2, v67
v_and_b32_e64 v88, 6, v87
v_xor_b32 v94, v93, v79
s_lshr_b32 s30, s20, 1
s_mul_i32 s39, s30, 1152
s_mul_i32 s40, s30, 384
s_cmp_eq_u32 s30, 0
v_add_u32 v69, 137216, v68
v_cndmask_b32 v217, 0, v83, vcc
v_xor_b32 v89, v88, v79
v_lshlrev_b32 v95, 4, v94
s_cselect_b32 s30, 0, 384
s_add_u32 m0, s21, 28672
v_cmp_eq_u32 vcc, 2, v86
buffer_load_dwordx4 v163, s[8:11], 0 offen lds
v_add_u32 v29, s30, v69
v_lshlrev_b32 v90, 4, v89
v_lshl_add_u32 v96, v92, 7, v95
s_mul_i32 s30, s20, 6912
v_cndmask_b32 v218, 0, v83, vcc
v_lshl_add_u32 v91, v87, 7, v90
v_mul_u32_u24 v97, 2304, v78
v_add_u32_e64 v228, s30, v96
v_lshl_add_u32 v98, v78, 3, v84
v_cmp_eq_u32 vcc, 3, v86
v_add_u32 v220, s30, v91
v_add_u32_e64 v224, v97, v228
v_mul_u32_u24 v99, 240, v98
s_mov_b32 s36, 128
s_mov_b32 s37, 256
v_add_u32 v27, s40, v69
v_add_u32 v28, s39, v69
v_cndmask_b32 v219, 0, v83, vcc
v_xor_b32 v221, 32, v220
v_xor_b32 v225, 32, v224
v_xor_b32 v229, 32, v228
v_xor_b32 v222, 64, v220
v_xor_b32 v226, 64, v224
v_xor_b32_e64 v230, 64, v228
v_xor_b32 v223, 96, v220
v_xor_b32 v227, 96, v224
v_xor_b32 v231, 96, v228
v_add_u32 v235, 157696, v99
v_mov_b32_e64 v248, v31
v_mov_b32 v249, v0
s_mul_i32 s30, s20, 140
s_add_u32 s30, s30, s42
s_sub_u32 s30, s30, 112
s_cmp_lt_u32 s30, 132
s_cselect_b32 s31, 12, -132
s_cselect_b32 s62, 0, 1
s_add_u32 s30, s30, s31
s_mul_i32 s63, s30, 5462
s_lshr_b32 s63, s63, 16
s_mul_i32 s31, s63, 12
s_sub_u32 s38, s30, s31
s_cmp_ge_u32 s38, 6
s_cselect_b32 s30, 6, 0
s_cselect_b32 s31, 64, 0
s_add_u32 s41, s38, s30
s_sub_u32 s30, s38, s30
s_mul_i32 s30, s30, 49152
s_add_u32 s30, s30, s31
s_lshl_b32 s31, s63, 7
s_add_u32 s40, s30, s31
s_and_b32 s31, s38, 1
s_lshr_b32 s33, s38, 1
s_lshl_b32 s27, s33, 2
s_add_u32 s27, s27, s31
s_add_u32 s27, s27, 24
s_lshl_b32 s33, s33, 6
s_lshl_b32 s30, s63, 1
s_add_u32 s30, s30, s31
s_mul_i32 s30, s30, 12288
s_add_u32 s30, s30, s33
s_cmp_eq_u32 s62, 0
s_cselect_b32 s40, s40, s30
s_cselect_b32 s41, s41, s27
s_cselect_b32 s39, 2, 0
s_cselect_b32 s27, 6, 2
s_cselect_b32 s68, s48, s52
s_cselect_b32 s69, s49, s53
s_cselect_b32 s30, 3, 4
s_cselect_b32 s38, 32, 8
s_cselect_b64 s[34:35], 0, -1
s_lshl_b32 s39, 384, s39
s_and_b32 s69, s69, 0xffff
s_add_u32 s27, s27, s41
s_lshl_b32 s27, s27, 10
s_lshl_b32 s41, s41, 10
s_mul_i32 s31, s63, 49152
v_and_b32 v83, 3, v249
s_add_u32 s41, s41, s31
s_add_u32 s27, s27, s31
v_bfe_u32 v82, v249, 2, 4
v_lshlrev_b32 v86, s30, v83
s_lshl_b32 s30, s20, 11
v_mul_u32_u24_e64 v84, s39, v82
v_lshl_add_u32 v87, v82, 6, v86
s_add_u32 s30, s30, 129024
s_add_u32 s33, s33, 144384
v_lshl_add_u32 v85, v83, 4, v84
v_add_u32 v42, s30, v87
s_cmp_lt_u32 s63, 12
v_bfe_u32 v90, v249, 3, 3
s_cselect_b32 s30, 0, 0x70000000
v_and_b32 v80, 15, v249
v_or_b32 v45, s30, v85
v_or_b32 v46, s30, v248
v_and_b32 v91, 6, v90
v_and_b32_e64 v92, 7, v249
s_mov_b32 s30, 141312
v_and_b32_e64 v88, 12, v80
v_lshl_add_u32 v134, v249, 1, s30
v_xor_b32_e64 v93, v91, v92
s_mov_b32 s30, 153600
s_lshl_b32 s39, s39, 4
v_add_u32 v89, v88, v80
v_lshl_add_u32 v47, v249, 2, s30
s_add_u32 s39, s39, s40
v_lshlrev_b32 v94, 4, v93
v_mul_u32_u24 v95, 1536, v90
s_and_b32 s13, s59, 0xffff
s_mul_i32 s30, s20, 73728
s_mul_i32 s23, s20, 6144
s_mov_b32 s70, 294912
v_bfe_u32 v81, v249, 4, 2
v_add_u32_e64 v43, s38, v42
v_lshl_add_u32 v44, v89, 1, s33
s_mov_b32 s12, s58
s_mov_b32 s14, 590912
s_mov_b32 s15, 0x20000
s_mov_b32 s4, s48
s_and_b32 s5, s49, 0xffff
s_mov_b32 s6, 294912
s_mov_b32 s7, 0x20000
v_add3_u32 v255, v94, v95, s30
v_and_b32_e64 v80, 15, v249
v_bfe_u32 v81, v249, 4, 2
v_add_u32_e64 v82, 1, v80
s_and_b32 s17, s61, 0xffff
v_and_b32_e64 v83, 6, v82
s_mul_i32 s30, s20, 6912
v_xor_b32_e64 v84, v83, v81
s_add_u32 s30, s30, 2304
v_lshlrev_b32 v85, 4, v84
v_cmp_eq_u32 vcc, 0, v249
v_lshl_add_u32 v86, v82, 7, v85
s_lshl_b32 s31, s42, 2
v_add_u32 v236, s30, v86
v_mov_b32 v87, 0x70000000
v_mov_b32 v88, s31
s_mov_b32 s30, 156672
s_mov_b32 s16, s60
s_mov_b32 s18, 19267584
s_mov_b32 s19, 0x20000
s_mov_b32 s28, 0x409d30fc
s_mov_b32 s29, 0x409d30fc
v_xor_b32 v237, 64, v236
v_lshl_add_u32 v253, v81, 5, s30
v_cndmask_b32 v250, v87, v88, vcc
v_add_u32 v248, s23, v248
s_lshl_b32 s30, s20, 11
s_add_u32 m0, s30, 129024
s_nop 0
buffer_load_dwordx4 v45, s[68:71], s40 offen lds
s_add_u32 m0, s30, 130048
s_nop 0
buffer_load_dwordx4 v45, s[68:71], s39 offen lds
s_mov_b32 s30, 590848
buffer_load_dword v133, off, s[12:15], s30 sc1
s_waitcnt vmcnt(11)
s_barrier
ds_read_b32 v20, v27 offset:0
ds_read_b32 v21, v27 offset:1152
ds_read_b32 v22, v27 offset:2304
ds_read_b32 v23, v28 offset:768
ds_read_b32 v24, v29 offset:3072
ds_read_u16 v128, v134 offset:0
ds_read_u16 v129, v134 offset:512
ds_read_u16 v130, v134 offset:1024
ds_read_u16 v131, v134 offset:2048
ds_read_u16 v132, v134 offset:3072
s_add_u32 m0, s21, 34816
s_nop 0
buffer_load_dwordx4 v156, s[8:11], s36 offen lds
s_waitcnt lgkmcnt(8)
ds_write2_b32 v19, v20, v21 offset0:0 offset1:1
s_waitcnt lgkmcnt(7)
ds_write2_b32 v19, v22, v23 offset0:2 offset1:3
s_waitcnt lgkmcnt(7)
ds_write_b32 v19, v24 offset:16
s_waitcnt lgkmcnt(3)
s_add_u32 m0, s21, 38912
v_lshlrev_b32 v128, 16, v128
v_lshlrev_b32 v129, 16, v129
v_lshlrev_b32 v130, 16, v130
v_lshlrev_b32 v131, 16, v131
v_lshlrev_b32 v132, 16, v132
buffer_load_dwordx4 v157, s[8:11], s36 offen lds
v_mul_f32 v128, 0x3eeff558, v128
v_mul_f32 v129, 0x3eeff558, v129
v_mul_f32 v130, 0x3eeff558, v130
v_mul_f32_e64 v131, v131, v132
s_waitcnt lgkmcnt(0)
s_barrier
ds_write_b32 v47, v128 offset:0
ds_write_b32 v47, v129 offset:1024
ds_write_b32 v47, v130 offset:2048
ds_write_b32 v47, v131 offset:3072
ds_read_b128 v[208:211], v235 offset:0
s_add_u32 m0, s21, 43008
s_nop 0
buffer_load_dwordx4 v158, s[8:11], s36 offen lds
ds_read_b128 v[212:215], v235 offset:16
s_waitcnt lgkmcnt(1)
v_and_b32 v16, v216, v208
v_and_b32 v17, v217, v208
v_and_b32 v18, v218, v208
v_and_b32 v19, v219, v208
v_and_b32 v64, v216, v209
v_and_b32 v65, v217, v209
v_and_b32_e64 v66, v218, v209
s_add_u32 m0, s21, 47104
v_and_b32_e64 v67, v219, v209
buffer_load_dwordx4 v159, s[8:11], s36 offen lds
v_and_b32 v112, v216, v210
v_and_b32 v113, v217, v210
v_and_b32 v114, v218, v210
v_and_b32 v115, v219, v210
v_and_b32 v20, v216, v211
v_and_b32 v21, v217, v211
v_and_b32_e64 v22, v218, v211
s_add_u32 m0, s21, 51200
v_and_b32_e64 v23, v219, v211
buffer_load_dwordx4 v160, s[8:11], s36 offen lds
ds_read_b128 v[208:211], v235 offset:32
s_waitcnt lgkmcnt(1)
v_and_b32 v68, v216, v212
v_and_b32 v69, v217, v212
v_and_b32 v70, v218, v212
v_and_b32 v71, v219, v212
v_and_b32 v116, v216, v213
v_and_b32 v117, v217, v213
v_and_b32 v118, v218, v213
s_add_u32 m0, s21, 55296
v_and_b32_e64 v119, v219, v213
buffer_load_dwordx4 v161, s[8:11], s36 offen lds
v_and_b32 v24, v216, v214
v_and_b32 v25, v217, v214
v_and_b32 v26, v218, v214
v_and_b32 v27, v219, v214
v_and_b32 v72, v216, v215
v_and_b32 v73, v217, v215
v_and_b32_e64 v74, v218, v215
s_add_u32 m0, s21, 59392
v_and_b32_e64 v75, v219, v215
buffer_load_dwordx4 v162, s[8:11], s36 offen lds
ds_read_b128 v[212:215], v235 offset:48
s_waitcnt lgkmcnt(1)
v_and_b32 v120, v216, v208
v_and_b32 v121, v217, v208
v_and_b32 v122, v218, v208
v_and_b32 v123, v219, v208
v_and_b32 v28, v216, v209
v_and_b32 v29, v217, v209
v_and_b32 v30, v218, v209
s_add_u32 m0, s21, 63488
v_and_b32 v31, v219, v209
buffer_load_dwordx4 v163, s[8:11], s36 offen lds
s_waitcnt vmcnt(8)
ds_read_b64_tr_b16 v[32:33], v42 offset:0
ds_read_b64_tr_b16 v[34:35], v42 offset:1024
ds_read_u16 v40, v44 offset:0
ds_read_b64_tr_b16 v[36:37], v43 offset:0
ds_read_b64_tr_b16 v[38:39], v43 offset:1024
ds_read_u16 v41, v44 offset:8
s_waitcnt lgkmcnt(0)
v_lshlrev_b32 v40, 16, v40
v_lshlrev_b32_e64 v41, 16, v41
v_mul_f32 v40, 0x40088e99, v40
v_mul_f32 v41, 0x40088e99, v41
v_cndmask_b32 v40, 1.0, v40, s[34:35]
v_lshlrev_b32_e64 v80, 16, v32
v_and_b32 v81, 0xffff0000, v32
v_lshlrev_b32_e64 v82, 16, v33
v_and_b32 v83, 0xffff0000, v33
v_lshlrev_b32_e64 v84, 16, v34
v_and_b32 v85, 0xffff0000, v34
v_lshlrev_b32_e64 v86, 16, v35
v_and_b32 v87, 0xffff0000, v35
v_cndmask_b32 v41, 1.0, v41, s[34:35]
v_lshlrev_b32_e64 v88, 16, v36
v_and_b32 v89, 0xffff0000, v36
v_lshlrev_b32_e64 v90, 16, v37
v_and_b32 v91, 0xffff0000, v37
v_lshlrev_b32_e64 v92, 16, v38
v_and_b32 v93, 0xffff0000, v38
v_lshlrev_b32_e64 v94, 16, v39
v_and_b32 v95, 0xffff0000, v39
v_mul_f32 v80, v40, v80
v_mul_f32 v81, v40, v81
v_mul_f32 v82, v40, v82
v_mul_f32 v83, v40, v83
v_mul_f32 v84, v40, v84
v_mul_f32 v85, v40, v85
v_mul_f32 v86, v40, v86
v_mul_f32 v87, v40, v87
v_mul_f32 v88, v41, v88
v_mul_f32 v89, v41, v89
v_mul_f32 v90, v41, v90
v_mul_f32 v91, v41, v91
v_mul_f32 v92, v41, v92
v_mul_f32 v93, v41, v93
v_mul_f32 v94, v41, v94
v_mul_f32 v95, v41, v95
v_cvt_pk_bf16_f32 v32, v80, v81
v_cvt_pk_bf16_f32 v33, v82, v83
v_cvt_pk_bf16_f32 v34, v84, v85
v_cvt_pk_bf16_f32 v35, v86, v87
v_cvt_pk_bf16_f32 v36, v88, v89
v_cvt_pk_bf16_f32 v37, v90, v91
v_cvt_pk_bf16_f32 v38, v92, v93
v_cvt_pk_bf16_f32 v39, v94, v95
buffer_store_dwordx4 v[32:35], v46, s[12:15], s41 offen sc1
buffer_store_dwordx4 v[36:39], v46, s[12:15], s27 offen sc1
v_readfirstlane_b32 s43, v133
s_nop 1
s_add_u32 s43, s43, 0x9e3779b9
s_nop 0
s_barrier
ds_read_b128 a[156:159], v220 offset:0
ds_read_b128 a[160:163], v220 offset:2304
ds_read_b128 a[164:167], v220 offset:4608
ds_read_b128 a[168:171], v224 offset:0
ds_read_b128 a[172:175], v224 offset:4608
ds_read_b128 a[176:179], v220 offset:6912
ds_read_b128 a[180:183], v224 offset:2304
ds_read_b128 a[184:187], v224 offset:6912
ds_read_b128 a[188:191], v220 offset:9216
ds_read_b128 a[192:195], v228 offset:9216
s_waitcnt lgkmcnt(9)
v_mfma_f32_16x16x32_bf16 v[0:3], v[16:19], a[156:159], 0
s_waitcnt lgkmcnt(8)
ds_read_b128 a[196:199], v221 offset:0
v_and_b32 v76, v216, v210
v_mfma_f32_16x16x32_bf16 v[0:3], v[64:67], a[160:163], v[0:3]
s_waitcnt lgkmcnt(8)
ds_read_b128 a[200:203], v221 offset:2304
v_and_b32_e64 v77, v217, v210
v_mfma_f32_16x16x32_bf16 v[0:3], v[112:115], a[164:167], v[0:3]
s_waitcnt lgkmcnt(8)
v_and_b32 v78, v218, v210
v_and_b32 v79, v219, v210
v_mfma_f32_16x16x32_bf16 v[0:3], v[20:23], a[168:171], v[0:3]
s_waitcnt lgkmcnt(7)
ds_read_b128 a[204:207], v221 offset:4608
v_and_b32_e64 v124, v216, v211
v_mfma_f32_16x16x32_bf16 v[0:3], v[68:71], a[172:175], v[0:3]
ds_read_b128 a[208:211], v225 offset:0
v_and_b32_e64 v125, v217, v211
v_mfma_f32_16x16x32_bf16 v[48:51], v[16:19], a[160:163], 0
v_and_b32_e64 v126, v218, v211
v_and_b32 v127, v219, v211
v_mfma_f32_16x16x32_bf16 v[48:51], v[64:67], a[164:167], v[48:51]
s_waitcnt lgkmcnt(2)
s_add_u32 m0, s21, 69632
ds_read_b128 a[212:215], v225 offset:4608
ds_read_b128 v[208:211], v235 offset:64
v_and_b32_e64 v168, v216, v212
v_mfma_f32_16x16x32_bf16 v[48:51], v[112:115], a[176:179], v[48:51]
buffer_load_dwordx4 v156, s[8:11], s37 offen lds
ds_read_b128 a[216:219], v221 offset:6912
v_and_b32_e64 v169, v217, v212
v_mfma_f32_16x16x32_bf16 v[48:51], v[20:23], a[180:183], v[48:51]
v_and_b32 v170, v218, v212
v_and_b32 v171, v219, v212
v_mfma_f32_16x16x32_bf16 v[48:51], v[68:71], a[184:187], v[48:51]
ds_read_b128 a[220:223], v225 offset:2304
v_and_b32_e64 v172, v216, v213
v_mfma_f32_16x16x32_bf16 v[96:99], v[16:19], a[164:167], 0
ds_read_b128 a[224:227], v225 offset:6912
v_and_b32_e64 v173, v217, v213
v_mfma_f32_16x16x32_bf16 v[96:99], v[64:67], a[176:179], v[96:99]
v_and_b32 v174, v218, v213
v_and_b32 v175, v219, v213
v_mfma_f32_16x16x32_bf16 v[96:99], v[112:115], a[188:191], v[96:99]
ds_read_b128 a[228:231], v221 offset:9216
v_and_b32_e64 v16, v216, v214
v_mfma_f32_16x16x32_bf16 v[96:99], v[20:23], a[172:175], v[96:99]
ds_read_b128 a[232:235], v229 offset:9216
v_and_b32_e64 v17, v217, v214
v_mfma_f32_16x16x32_bf16 v[96:99], v[68:71], a[192:195], v[96:99]
s_add_u32 m0, s21, 73728
v_and_b32 v18, v218, v214
v_and_b32 v19, v219, v214
v_mfma_f32_16x16x32_bf16 v[4:7], v[116:119], a[196:199], 0
buffer_load_dwordx4 v157, s[8:11], s37 offen lds
ds_read_b128 a[156:159], v222 offset:0
v_and_b32 v64, v216, v215
v_mfma_f32_16x16x32_bf16 v[4:7], v[24:27], a[200:203], v[4:7]
s_waitcnt lgkmcnt(3)
ds_read_b128 a[160:163], v222 offset:2304
v_and_b32_e64 v65, v217, v215
v_mfma_f32_16x16x32_bf16 v[4:7], v[72:75], a[204:207], v[4:7]
v_and_b32 v66, v218, v215
v_and_b32 v67, v219, v215
v_mfma_f32_16x16x32_bf16 v[4:7], v[120:123], a[208:211], v[4:7]
ds_read_b128 a[164:167], v222 offset:4608
ds_read_b128 v[212:215], v235 offset:80
v_and_b32_e64 v112, v216, v208
v_mfma_f32_16x16x32_bf16 v[4:7], v[28:31], a[212:215], v[4:7]
ds_read_b128 a[168:171], v226 offset:0
v_and_b32_e64 v113, v217, v208
v_mfma_f32_16x16x32_bf16 v[52:55], v[116:119], a[200:203], 0
v_and_b32 v114, v218, v208
v_and_b32 v115, v219, v208
v_mfma_f32_16x16x32_bf16 v[52:55], v[24:27], a[204:207], v[52:55]
ds_read_b128 a[172:175], v226 offset:4608
v_and_b32_e64 v20, v216, v209
v_mfma_f32_16x16x32_bf16 v[52:55], v[72:75], a[216:219], v[52:55]
s_add_u32 m0, s21, 77824
ds_read_b128 a[176:179], v222 offset:6912
v_and_b32_e64 v21, v217, v209
v_mfma_f32_16x16x32_bf16 v[52:55], v[120:123], a[220:223], v[52:55]
buffer_load_dwordx4 v158, s[8:11], s37 offen lds
v_and_b32 v22, v218, v209
v_and_b32 v23, v219, v209
v_mfma_f32_16x16x32_bf16 v[52:55], v[28:31], a[224:227], v[52:55]
ds_read_b128 a[180:183], v226 offset:2304
v_and_b32_e64 v68, v216, v210
v_mfma_f32_16x16x32_bf16 v[100:103], v[116:119], a[204:207], 0
ds_read_b128 a[184:187], v226 offset:6912
v_and_b32_e64 v69, v217, v210
v_mfma_f32_16x16x32_bf16 v[100:103], v[24:27], a[216:219], v[100:103]
s_waitcnt lgkmcnt(3)
v_and_b32 v70, v218, v210
v_and_b32_e64 v71, v219, v210
v_mfma_f32_16x16x32_bf16 v[100:103], v[72:75], a[228:231], v[100:103]
ds_read_b128 a[188:191], v222 offset:9216
v_and_b32_e64 v116, v216, v211
v_mfma_f32_16x16x32_bf16 v[100:103], v[120:123], a[212:215], v[100:103]
ds_read_b128 a[192:195], v230 offset:9216
v_and_b32_e64 v117, v217, v211
v_mfma_f32_16x16x32_bf16 v[100:103], v[28:31], a[232:235], v[100:103]
v_and_b32 v118, v218, v211
v_and_b32 v119, v219, v211
v_mfma_f32_16x16x32_bf16 v[8:11], v[76:79], a[156:159], 0
s_add_u32 m0, s21, 81920
ds_read_b128 a[196:199], v223 offset:0
ds_read_b128 v[208:211], v235 offset:96
v_and_b32_e64 v24, v216, v212
v_mfma_f32_16x16x32_bf16 v[8:11], v[124:127], a[160:163], v[8:11]
buffer_load_dwordx4 v159, s[8:11], s37 offen lds
ds_read_b128 a[200:203], v223 offset:2304
v_and_b32_e64 v25, v217, v212
v_mfma_f32_16x16x32_bf16 v[8:11], v[168:171], a[164:167], v[8:11]
v_and_b32 v26, v218, v212
v_and_b32 v27, v219, v212
v_mfma_f32_16x16x32_bf16 v[8:11], v[172:175], a[168:171], v[8:11]
ds_read_b128 a[204:207], v223 offset:4608
v_and_b32_e64 v72, v216, v213
v_mfma_f32_16x16x32_bf16 v[8:11], v[16:19], a[172:175], v[8:11]
ds_read_b128 a[208:211], v227 offset:0
v_and_b32_e64 v73, v217, v213
v_mfma_f32_16x16x32_bf16 v[56:59], v[76:79], a[160:163], 0
v_and_b32 v74, v218, v213
v_and_b32 v75, v219, v213
v_mfma_f32_16x16x32_bf16 v[56:59], v[124:127], a[164:167], v[56:59]
s_waitcnt lgkmcnt(2)
ds_read_b128 a[212:215], v227 offset:4608
v_and_b32 v120, v216, v214
v_mfma_f32_16x16x32_bf16 v[56:59], v[168:171], a[176:179], v[56:59]
ds_read_b128 a[216:219], v223 offset:6912
v_and_b32_e64 v121, v217, v214
v_mfma_f32_16x16x32_bf16 v[56:59], v[172:175], a[180:183], v[56:59]
s_add_u32 m0, s21, 86016
v_and_b32 v122, v218, v214
v_and_b32 v123, v219, v214
v_mfma_f32_16x16x32_bf16 v[56:59], v[16:19], a[184:187], v[56:59]
buffer_load_dwordx4 v160, s[8:11], s37 offen lds
ds_read_b128 a[220:223], v227 offset:2304
v_and_b32_e64 v28, v216, v215
v_mfma_f32_16x16x32_bf16 v[104:107], v[76:79], a[164:167], 0
ds_read_b128 a[224:227], v227 offset:6912
v_and_b32_e64 v29, v217, v215
v_mfma_f32_16x16x32_bf16 v[104:107], v[124:127], a[176:179], v[104:107]
v_and_b32 v30, v218, v215
v_and_b32 v31, v219, v215
v_mfma_f32_16x16x32_bf16 v[104:107], v[168:171], a[188:191], v[104:107]
ds_read_b128 a[228:231], v223 offset:9216
ds_read_b128 v[212:215], v235 offset:112
v_and_b32_e64 v76, v216, v208
v_mfma_f32_16x16x32_bf16 v[104:107], v[172:175], a[172:175], v[104:107]
ds_read_b128 a[232:235], v231 offset:9216
v_and_b32_e64 v77, v217, v208
v_mfma_f32_16x16x32_bf16 v[104:107], v[16:19], a[192:195], v[104:107]
v_and_b32 v78, v218, v208
v_and_b32 v79, v219, v208
v_mfma_f32_16x16x32_bf16 v[12:15], v[64:67], a[196:199], 0
v_and_b32 v124, v216, v209
v_and_b32 v125, v217, v209
v_mfma_f32_16x16x32_bf16 v[12:15], v[112:115], a[200:203], v[12:15]
s_waitcnt lgkmcnt(3)
s_add_u32 m0, s21, 90112
v_and_b32 v126, v218, v209
v_and_b32_e64 v127, v219, v209
v_mfma_f32_16x16x32_bf16 v[12:15], v[20:23], a[204:207], v[12:15]
buffer_load_dwordx4 v161, s[8:11], s37 offen lds
v_and_b32 v176, v216, v210
v_and_b32 v177, v217, v210
v_mfma_f32_16x16x32_bf16 v[12:15], v[68:71], a[208:211], v[12:15]
v_and_b32 v178, v218, v210
v_and_b32 v179, v219, v210
v_mfma_f32_16x16x32_bf16 v[12:15], v[116:119], a[212:215], v[12:15]
v_and_b32 v180, v216, v211
v_and_b32 v181, v217, v211
v_mfma_f32_16x16x32_bf16 v[60:63], v[64:67], a[200:203], 0
v_and_b32 v182, v218, v211
v_and_b32 v183, v219, v211
v_mfma_f32_16x16x32_bf16 v[60:63], v[112:115], a[204:207], v[60:63]
ds_read_b128 v[208:211], v235 offset:128
s_waitcnt lgkmcnt(2)
v_and_b32 v184, v216, v212
v_and_b32_e64 v185, v217, v212
v_mfma_f32_16x16x32_bf16 v[60:63], v[20:23], a[216:219], v[60:63]
v_and_b32 v186, v218, v212
v_and_b32 v187, v219, v212
v_mfma_f32_16x16x32_bf16 v[60:63], v[68:71], a[220:223], v[60:63]
v_and_b32 v188, v216, v213
v_and_b32 v189, v217, v213
v_mfma_f32_16x16x32_bf16 v[60:63], v[116:119], a[224:227], v[60:63]
s_add_u32 m0, s21, 94208
v_and_b32 v190, v218, v213
v_and_b32 v191, v219, v213
v_mfma_f32_16x16x32_bf16 v[108:111], v[64:67], a[204:207], 0
buffer_load_dwordx4 v162, s[8:11], s37 offen lds
v_and_b32 v192, v216, v214
v_and_b32 v193, v217, v214
v_mfma_f32_16x16x32_bf16 v[108:111], v[112:115], a[216:219], v[108:111]
v_and_b32 v194, v218, v214
v_and_b32 v195, v219, v214
v_mfma_f32_16x16x32_bf16 v[108:111], v[20:23], a[228:231], v[108:111]
v_and_b32 v196, v216, v215
v_and_b32 v197, v217, v215
v_mfma_f32_16x16x32_bf16 v[108:111], v[68:71], a[212:215], v[108:111]
s_waitcnt lgkmcnt(0)
v_and_b32 v198, v218, v215
v_and_b32_e64 v199, v219, v215
v_mfma_f32_16x16x32_bf16 v[108:111], v[116:119], a[232:235], v[108:111]
ds_read_b128 v[212:215], v235 offset:144
v_and_b32_e64 v200, v216, v208
v_and_b32 v201, v217, v208
s_add_u32 m0, s21, 98304
s_nop 0
buffer_load_dwordx4 v163, s[8:11], s37 offen lds
ds_read_b128 a[12:15], v236 offset:0
ds_read_b128 a[16:19], v236 offset:2304
ds_read_b128 a[20:23], v236 offset:4608
ds_read_b128 a[36:39], v237 offset:0
ds_read_b128 a[40:43], v237 offset:2304
ds_read_b128 a[44:47], v237 offset:4608
v_pk_mul_f32 v[158:159], v[0:1], v[0:1]
v_pk_add_f32 v[156:157], v[0:1], 0 op_sel_hi:[1,0]
v_pk_mul_f32 v[162:163], v[48:49], v[48:49]
v_pk_add_f32 v[160:161], v[48:49], 0 op_sel_hi:[1,0]
v_pk_mul_f32 v[166:167], v[96:97], v[96:97]
v_pk_add_f32 v[164:165], v[96:97], 0 op_sel_hi:[1,0]
v_pk_fma_f32 v[158:159], v[2:3], v[2:3], v[158:159]
v_pk_add_f32 v[156:157], v[2:3], v[156:157]
v_pk_fma_f32 v[162:163], v[50:51], v[50:51], v[162:163]
v_pk_add_f32 v[160:161], v[50:51], v[160:161]
v_pk_fma_f32 v[166:167], v[98:99], v[98:99], v[166:167]
v_pk_add_f32 v[164:165], v[98:99], v[164:165]
v_pk_fma_f32 v[158:159], v[4:5], v[4:5], v[158:159]
v_pk_add_f32 v[156:157], v[4:5], v[156:157]
v_pk_fma_f32 v[162:163], v[52:53], v[52:53], v[162:163]
v_pk_add_f32 v[160:161], v[52:53], v[160:161]
v_pk_fma_f32 v[166:167], v[100:101], v[100:101], v[166:167]
v_pk_add_f32 v[164:165], v[100:101], v[164:165]
v_pk_fma_f32 v[158:159], v[6:7], v[6:7], v[158:159]
v_pk_add_f32 v[156:157], v[6:7], v[156:157]
v_pk_fma_f32 v[162:163], v[54:55], v[54:55], v[162:163]
v_pk_add_f32 v[160:161], v[54:55], v[160:161]
v_pk_fma_f32 v[166:167], v[102:103], v[102:103], v[166:167]
v_pk_add_f32 v[164:165], v[102:103], v[164:165]
v_pk_fma_f32 v[158:159], v[8:9], v[8:9], v[158:159]
v_pk_add_f32 v[156:157], v[8:9], v[156:157]
v_pk_fma_f32 v[162:163], v[56:57], v[56:57], v[162:163]
v_pk_add_f32 v[160:161], v[56:57], v[160:161]
v_pk_fma_f32 v[166:167], v[104:105], v[104:105], v[166:167]
v_pk_add_f32 v[164:165], v[104:105], v[164:165]
v_pk_fma_f32 v[158:159], v[10:11], v[10:11], v[158:159]
v_pk_add_f32 v[156:157], v[10:11], v[156:157]
v_pk_fma_f32 v[162:163], v[58:59], v[58:59], v[162:163]
v_pk_add_f32 v[160:161], v[58:59], v[160:161]
v_pk_fma_f32 v[166:167], v[106:107], v[106:107], v[166:167]
v_pk_add_f32 v[164:165], v[106:107], v[164:165]
v_pk_fma_f32 v[158:159], v[12:13], v[12:13], v[158:159]
v_pk_add_f32 v[156:157], v[12:13], v[156:157]
v_pk_fma_f32 v[162:163], v[60:61], v[60:61], v[162:163]
v_pk_add_f32 v[160:161], v[60:61], v[160:161]
v_pk_fma_f32 v[166:167], v[108:109], v[108:109], v[166:167]
v_pk_add_f32 v[164:165], v[108:109], v[164:165]
v_pk_fma_f32 v[158:159], v[14:15], v[14:15], v[158:159]
v_pk_add_f32 v[156:157], v[14:15], v[156:157]
v_pk_fma_f32 v[162:163], v[62:63], v[62:63], v[162:163]
v_pk_add_f32 v[160:161], v[62:63], v[160:161]
v_pk_fma_f32 v[166:167], v[110:111], v[110:111], v[166:167]
v_pk_add_f32 v[164:165], v[110:111], v[164:165]
v_and_b32 v202, v218, v208
v_and_b32 v203, v219, v208
s_waitcnt vmcnt(8) lgkmcnt(0)
s_barrier
ds_read_b128 a[156:159], v220 offset:34816
ds_read_b128 a[160:163], v220 offset:37120
ds_read_b128 a[164:167], v220 offset:39424
ds_read_b128 a[168:171], v224 offset:34816
ds_read_b128 a[172:175], v224 offset:39424
ds_read_b128 a[176:179], v220 offset:41728
ds_read_b128 a[180:183], v224 offset:37120
ds_read_b128 a[184:187], v224 offset:41728
ds_read_b128 a[188:191], v220 offset:44032
ds_read_b128 a[192:195], v228 offset:44032
s_mov_b32 s30, 589824
v_mov_b32_e64 v251, s43
buffer_store_dword v251, v250, s[12:15], s30 offen sc1
s_waitcnt lgkmcnt(9)
v_mfma_f32_16x16x32_bf16 v[16:19], v[24:27], a[156:159], 0
s_waitcnt lgkmcnt(8)
ds_read_b128 a[196:199], v221 offset:34816
v_and_b32 v36, v216, v209
v_mfma_f32_16x16x32_bf16 v[16:19], v[72:75], a[160:163], v[16:19]
s_waitcnt lgkmcnt(8)
ds_read_b128 a[200:203], v221 offset:37120
v_and_b32 v37, v217, v209
v_mfma_f32_16x16x32_bf16 v[16:19], v[120:123], a[164:167], v[16:19]
s_waitcnt lgkmcnt(8)
s_add_u32 m0, s23, 104448
s_mov_b32 s26, 0
v_and_b32 v38, v218, v209
v_and_b32 v39, v219, v209
v_mfma_f32_16x16x32_bf16 v[16:19], v[28:31], a[168:171], v[16:19]
s_waitcnt lgkmcnt(7)
buffer_load_dwordx4 v255, s[4:7], s26 offen lds
ds_read_b128 a[204:207], v221 offset:39424
v_and_b32 v84, v216, v210
v_mfma_f32_16x16x32_bf16 v[16:19], v[76:79], a[172:175], v[16:19]
s_mul_i32 s38, s20, 3
ds_read_b128 a[208:211], v225 offset:34816
v_and_b32_e64 v85, v217, v210
v_mfma_f32_16x16x32_bf16 v[64:67], v[24:27], a[160:163], 0
s_add_u32 s38, s38, s24
v_and_b32 v86, v218, v210
v_and_b32 v87, v219, v210
v_mfma_f32_16x16x32_bf16 v[64:67], v[72:75], a[164:167], v[64:67]
s_waitcnt lgkmcnt(2)
s_add_u32 m0, s23, 105472
s_mov_b32 s26, 12288
ds_read_b128 a[212:215], v225 offset:39424
v_and_b32_e64 v132, v216, v211
v_mfma_f32_16x16x32_bf16 v[64:67], v[120:123], a[176:179], v[64:67]
buffer_load_dwordx4 v255, s[4:7], s26 offen lds
ds_read_b128 a[216:219], v221 offset:41728
v_and_b32_e64 v133, v217, v211
v_mfma_f32_16x16x32_bf16 v[64:67], v[28:31], a[180:183], v[64:67]
s_mul_i32 s38, s38, 224
v_and_b32_e64 v134, v218, v211
v_and_b32 v135, v219, v211
v_mfma_f32_16x16x32_bf16 v[64:67], v[76:79], a[184:187], v[64:67]
s_add_u32 s38, s38, s25
ds_read_b128 a[220:223], v225 offset:37120
ds_read_b128 v[208:211], v235 offset:160
v_and_b32_e64 v40, v216, v212
v_mfma_f32_16x16x32_bf16 v[112:115], v[24:27], a[164:167], 0
s_add_u32 m0, s23, 106496
s_mov_b32 s26, 24576
ds_read_b128 a[224:227], v225 offset:41728
v_and_b32_e64 v41, v217, v212
v_mfma_f32_16x16x32_bf16 v[112:115], v[72:75], a[176:179], v[112:115]
buffer_load_dwordx4 v255, s[4:7], s26 offen lds
v_and_b32 v42, v218, v212
v_and_b32 v43, v219, v212
v_mfma_f32_16x16x32_bf16 v[112:115], v[120:123], a[188:191], v[112:115]
s_mul_i32 s44, s38, 384
ds_read_b128 a[228:231], v221 offset:44032
v_and_b32_e64 v88, v216, v213
v_mfma_f32_16x16x32_bf16 v[112:115], v[28:31], a[172:175], v[112:115]
s_add_u32 s45, s44, 86016
ds_read_b128 a[232:235], v229 offset:44032
v_and_b32_e64 v89, v217, v213
v_mfma_f32_16x16x32_bf16 v[112:115], v[76:79], a[192:195], v[112:115]
s_add_u32 m0, s23, 107520
s_mov_b32 s26, 36864
v_and_b32 v90, v218, v213
v_and_b32 v91, v219, v213
v_mfma_f32_16x16x32_bf16 v[20:23], v[124:127], a[196:199], 0
buffer_load_dwordx4 v255, s[4:7], s26 offen lds
ds_read_b128 a[156:159], v222 offset:34816
v_and_b32 v136, v216, v214
v_mfma_f32_16x16x32_bf16 v[20:23], v[176:179], a[200:203], v[20:23]
s_waitcnt lgkmcnt(3)
ds_read_b128 a[160:163], v222 offset:37120
v_and_b32_e64 v137, v217, v214
v_mfma_f32_16x16x32_bf16 v[20:23], v[180:183], a[204:207], v[20:23]
s_add_u32 s46, s44, 172032
v_and_b32 v138, v218, v214
v_and_b32 v139, v219, v214
v_mfma_f32_16x16x32_bf16 v[20:23], v[184:187], a[208:211], v[20:23]
s_add_u32 m0, s23, 108544
s_mov_b32 s26, 49152
ds_read_b128 a[164:167], v222 offset:39424
v_and_b32_e64 v44, v216, v215
v_mfma_f32_16x16x32_bf16 v[20:23], v[188:191], a[212:215], v[20:23]
buffer_load_dwordx4 v255, s[4:7], s26 offen lds
ds_read_b128 a[168:171], v226 offset:34816
v_and_b32_e64 v45, v217, v215
v_mfma_f32_16x16x32_bf16 v[68:71], v[124:127], a[200:203], 0
v_and_b32 v46, v218, v215
v_and_b32 v47, v219, v215
v_mfma_f32_16x16x32_bf16 v[68:71], v[176:179], a[204:207], v[68:71]
ds_read_b128 a[172:175], v226 offset:39424
ds_read_b128 v[212:215], v235 offset:176
v_and_b32_e64 v92, v216, v208
v_mfma_f32_16x16x32_bf16 v[68:71], v[180:183], a[216:219], v[68:71]
s_add_u32 m0, s23, 109568
s_mov_b32 s26, 61440
ds_read_b128 a[176:179], v222 offset:41728
v_and_b32_e64 v93, v217, v208
v_mfma_f32_16x16x32_bf16 v[68:71], v[184:187], a[220:223], v[68:71]
buffer_load_dwordx4 v255, s[4:7], s26 offen lds
v_and_b32 v94, v218, v208
v_and_b32 v95, v219, v208
v_mfma_f32_16x16x32_bf16 v[68:71], v[188:191], a[224:227], v[68:71]
ds_read_b128 a[180:183], v226 offset:37120
v_and_b32_e64 v140, v216, v209
v_mfma_f32_16x16x32_bf16 v[116:119], v[124:127], a[204:207], 0
ds_read_b128 a[184:187], v226 offset:41728
v_and_b32_e64 v141, v217, v209
v_mfma_f32_16x16x32_bf16 v[116:119], v[176:179], a[216:219], v[116:119]
s_waitcnt lgkmcnt(3)
v_and_b32 v142, v218, v209
v_and_b32_e64 v143, v219, v209
v_mfma_f32_16x16x32_bf16 v[116:119], v[180:183], a[228:231], v[116:119]
ds_read_b128 a[188:191], v222 offset:44032
v_and_b32_e64 v204, v216, v210
v_mfma_f32_16x16x32_bf16 v[116:119], v[184:187], a[212:215], v[116:119]
ds_read_b128 a[192:195], v230 offset:44032
v_and_b32_e64 v205, v217, v210
v_mfma_f32_16x16x32_bf16 v[116:119], v[188:191], a[232:235], v[116:119]
v_and_b32 v206, v218, v210
v_and_b32 v207, v219, v210
v_mfma_f32_16x16x32_bf16 v[24:27], v[192:195], a[156:159], 0
ds_read_b128 a[196:199], v223 offset:34816
v_and_b32_e64 v168, v216, v211
v_mfma_f32_16x16x32_bf16 v[24:27], v[196:199], a[160:163], v[24:27]
ds_read_b128 a[200:203], v223 offset:37120
v_and_b32_e64 v169, v217, v211
v_mfma_f32_16x16x32_bf16 v[24:27], v[200:203], a[164:167], v[24:27]
v_and_b32 v170, v218, v211
v_and_b32 v171, v219, v211
v_mfma_f32_16x16x32_bf16 v[24:27], v[36:39], a[168:171], v[24:27]
ds_read_b128 a[204:207], v223 offset:39424
ds_read_b128 v[208:211], v235 offset:192
v_and_b32_e64 v172, v216, v212
v_mfma_f32_16x16x32_bf16 v[24:27], v[84:87], a[172:175], v[24:27]
ds_read_b128 a[208:211], v227 offset:34816
v_and_b32_e64 v173, v217, v212
v_mfma_f32_16x16x32_bf16 v[72:75], v[192:195], a[160:163], 0
v_and_b32 v174, v218, v212
v_and_b32 v175, v219, v212
v_mfma_f32_16x16x32_bf16 v[72:75], v[196:199], a[164:167], v[72:75]
s_waitcnt lgkmcnt(3)
ds_read_b128 a[212:215], v227 offset:39424
v_and_b32 v176, v216, v213
v_mfma_f32_16x16x32_bf16 v[72:75], v[200:203], a[176:179], v[72:75]
ds_read_b128 a[216:219], v223 offset:41728
v_and_b32_e64 v177, v217, v213
v_mfma_f32_16x16x32_bf16 v[72:75], v[36:39], a[180:183], v[72:75]
v_and_b32 v178, v218, v213
v_and_b32 v179, v219, v213
v_mfma_f32_16x16x32_bf16 v[72:75], v[84:87], a[184:187], v[72:75]
ds_read_b128 a[220:223], v227 offset:37120
v_and_b32_e64 v180, v216, v214
v_mfma_f32_16x16x32_bf16 v[120:123], v[192:195], a[164:167], 0
ds_read_b128 a[224:227], v227 offset:41728
v_and_b32_e64 v181, v217, v214
v_mfma_f32_16x16x32_bf16 v[120:123], v[196:199], a[176:179], v[120:123]
v_and_b32 v182, v218, v214
v_and_b32 v183, v219, v214
v_mfma_f32_16x16x32_bf16 v[120:123], v[200:203], a[188:191], v[120:123]
ds_read_b128 a[228:231], v223 offset:44032
v_and_b32_e64 v184, v216, v215
v_mfma_f32_16x16x32_bf16 v[120:123], v[36:39], a[172:175], v[120:123]
ds_read_b128 a[232:235], v231 offset:44032
v_and_b32_e64 v185, v217, v215
v_mfma_f32_16x16x32_bf16 v[120:123], v[84:87], a[192:195], v[120:123]
v_and_b32 v186, v218, v215
v_and_b32 v187, v219, v215
v_mfma_f32_16x16x32_bf16 v[28:31], v[132:135], a[196:199], 0
ds_read_b128 v[212:215], v235 offset:208
s_waitcnt lgkmcnt(8)
v_and_b32 v188, v216, v208
v_and_b32_e64 v189, v217, v208
v_mfma_f32_16x16x32_bf16 v[28:31], v[40:43], a[200:203], v[28:31]
v_and_b32 v190, v218, v208
v_and_b32 v191, v219, v208
v_mfma_f32_16x16x32_bf16 v[28:31], v[88:91], a[204:207], v[28:31]
s_waitcnt lgkmcnt(2)
v_and_b32 v192, v216, v209
v_and_b32_e64 v193, v217, v209
v_mfma_f32_16x16x32_bf16 v[28:31], v[136:139], a[208:211], v[28:31]
v_and_b32 v194, v218, v209
v_and_b32 v195, v219, v209
v_mfma_f32_16x16x32_bf16 v[28:31], v[44:47], a[212:215], v[28:31]
v_and_b32 v196, v216, v210
v_and_b32 v197, v217, v210
v_mfma_f32_16x16x32_bf16 v[76:79], v[132:135], a[200:203], 0
v_and_b32 v198, v218, v210
v_and_b32 v199, v219, v210
v_mfma_f32_16x16x32_bf16 v[76:79], v[40:43], a[204:207], v[76:79]
v_and_b32 v200, v216, v211
v_and_b32 v201, v217, v211
v_mfma_f32_16x16x32_bf16 v[76:79], v[88:91], a[216:219], v[76:79]
v_and_b32 v202, v218, v211
v_and_b32 v203, v219, v211
v_mfma_f32_16x16x32_bf16 v[76:79], v[136:139], a[220:223], v[76:79]
v_mfma_f32_16x16x32_bf16 v[76:79], v[44:47], a[224:227], v[76:79]
v_mfma_f32_16x16x32_bf16 v[124:127], v[132:135], a[204:207], 0
v_mfma_f32_16x16x32_bf16 v[124:127], v[40:43], a[216:219], v[124:127]
v_mfma_f32_16x16x32_bf16 v[124:127], v[88:91], a[228:231], v[124:127]
s_nop 0
v_mfma_f32_16x16x32_bf16 v[124:127], v[136:139], a[212:215], v[124:127]
s_waitcnt lgkmcnt(0)
v_mfma_f32_16x16x32_bf16 v[124:127], v[44:47], a[232:235], v[124:127]
ds_read_b128 v[208:211], v235 offset:224
v_and_b32 v44, v216, v212
v_and_b32 v45, v217, v212
ds_read_b128 a[60:63], v236 offset:34816
ds_read_b128 a[64:67], v236 offset:37120
ds_read_b128 a[68:71], v236 offset:39424
ds_read_b128 a[84:87], v237 offset:34816
ds_read_b128 a[88:91], v237 offset:37120
ds_read_b128 a[92:95], v237 offset:39424
v_pk_fma_f32 v[158:159], v[16:17], v[16:17], v[158:159]
v_pk_add_f32 v[156:157], v[16:17], v[156:157]
v_pk_fma_f32 v[162:163], v[64:65], v[64:65], v[162:163]
v_pk_add_f32 v[160:161], v[64:65], v[160:161]
v_pk_fma_f32 v[166:167], v[112:113], v[112:113], v[166:167]
v_pk_add_f32 v[164:165], v[112:113], v[164:165]
v_pk_fma_f32 v[158:159], v[18:19], v[18:19], v[158:159]
v_pk_add_f32 v[156:157], v[18:19], v[156:157]
v_pk_fma_f32 v[162:163], v[66:67], v[66:67], v[162:163]
v_pk_add_f32 v[160:161], v[66:67], v[160:161]
v_pk_fma_f32 v[166:167], v[114:115], v[114:115], v[166:167]
v_pk_add_f32 v[164:165], v[114:115], v[164:165]
v_pk_fma_f32 v[158:159], v[20:21], v[20:21], v[158:159]
v_pk_add_f32 v[156:157], v[20:21], v[156:157]
v_pk_fma_f32 v[162:163], v[68:69], v[68:69], v[162:163]
v_pk_add_f32 v[160:161], v[68:69], v[160:161]
v_pk_fma_f32 v[166:167], v[116:117], v[116:117], v[166:167]
v_pk_add_f32 v[164:165], v[116:117], v[164:165]
v_pk_fma_f32 v[158:159], v[22:23], v[22:23], v[158:159]
v_pk_add_f32 v[156:157], v[22:23], v[156:157]
v_pk_fma_f32 v[162:163], v[70:71], v[70:71], v[162:163]
v_pk_add_f32 v[160:161], v[70:71], v[160:161]
v_pk_fma_f32 v[166:167], v[118:119], v[118:119], v[166:167]
v_pk_add_f32 v[164:165], v[118:119], v[164:165]
v_pk_fma_f32 v[158:159], v[24:25], v[24:25], v[158:159]
v_pk_add_f32 v[156:157], v[24:25], v[156:157]
v_pk_fma_f32 v[162:163], v[72:73], v[72:73], v[162:163]
v_pk_add_f32 v[160:161], v[72:73], v[160:161]
v_pk_fma_f32 v[166:167], v[120:121], v[120:121], v[166:167]
v_pk_add_f32 v[164:165], v[120:121], v[164:165]
v_pk_fma_f32 v[158:159], v[26:27], v[26:27], v[158:159]
v_pk_add_f32 v[156:157], v[26:27], v[156:157]
v_pk_fma_f32 v[162:163], v[74:75], v[74:75], v[162:163]
v_pk_add_f32 v[160:161], v[74:75], v[160:161]
v_pk_fma_f32 v[166:167], v[122:123], v[122:123], v[166:167]
v_pk_add_f32 v[164:165], v[122:123], v[164:165]
v_pk_fma_f32 v[158:159], v[28:29], v[28:29], v[158:159]
v_pk_add_f32 v[156:157], v[28:29], v[156:157]
v_pk_fma_f32 v[162:163], v[76:77], v[76:77], v[162:163]
v_pk_add_f32 v[160:161], v[76:77], v[160:161]
v_pk_fma_f32 v[166:167], v[124:125], v[124:125], v[166:167]
v_pk_add_f32 v[164:165], v[124:125], v[164:165]
v_pk_fma_f32 v[158:159], v[30:31], v[30:31], v[158:159]
v_pk_add_f32 v[156:157], v[30:31], v[156:157]
v_pk_fma_f32 v[162:163], v[78:79], v[78:79], v[162:163]
v_pk_add_f32 v[160:161], v[78:79], v[160:161]
v_pk_fma_f32 v[166:167], v[126:127], v[126:127], v[166:167]
v_pk_add_f32 v[164:165], v[126:127], v[164:165]
v_and_b32 v46, v218, v212
v_and_b32 v47, v219, v212
v_subrev_u32_e64 v238, s23, v248
v_add_u32 v239, 104448, v238
v_lshrrev_b32_e64 v242, 6, v238
v_bfe_u32 v243, v238, 5, 1
v_and_b32 v255, 6, v242
v_lshrrev_b32 v241, 4, v238
v_or_b32_e64 v255, v255, v243
v_bfe_u32 v243, v238, 4, 1
v_lshlrev_b32 v255, 4, v255
v_and_b32 v241, 48, v241
v_lshl_add_u32 v255, v242, 7, v255
v_add_u32 v241, 153600, v241
v_lshl_add_u32 v255, v243, 3, v255
v_add_u32 v255, 104448, v255
v_add_u32 v220, 69632, v220
v_add_u32 v221, 69632, v221
v_add_u32 v222, 69632, v222
v_add_u32 v223, 69632, v223
v_add_u32 v224, 69632, v224
v_add_u32 v225, 69632, v225
v_add_u32 v226, 69632, v226
v_add_u32 v227, 69632, v227
v_add_u32 v228, 69632, v228
v_add_u32 v229, 69632, v229
v_add_u32 v230, 69632, v230
v_add_u32 v231, 69632, v231
v_add_u32 v236, 69632, v236
v_add_u32 v237, 69632, v237
s_waitcnt vmcnt(7) lgkmcnt(0)
s_barrier
ds_read_b128 a[156:159], v220 offset:0
ds_read_b128 a[160:163], v220 offset:2304
ds_read_b128 a[164:167], v220 offset:4608
ds_read_b128 a[168:171], v224 offset:0
ds_read_b128 a[172:175], v224 offset:4608
ds_read_b128 a[176:179], v220 offset:6912
ds_read_b128 a[180:183], v224 offset:2304
ds_read_b128 a[184:187], v224 offset:6912
ds_read_b128 a[188:191], v220 offset:9216
ds_read_b128 a[192:195], v228 offset:9216
s_waitcnt lgkmcnt(9)
v_mfma_f32_16x16x32_bf16 v[32:35], v[92:95], a[156:159], 0
s_waitcnt lgkmcnt(8)
ds_read_b128 a[196:199], v221 offset:0
v_mfma_f32_16x16x32_bf16 v[32:35], v[140:143], a[160:163], v[32:35]
s_waitcnt lgkmcnt(8)
ds_read_b128 a[200:203], v221 offset:2304
v_mfma_f32_16x16x32_bf16 v[32:35], v[204:207], a[164:167], v[32:35]
s_waitcnt lgkmcnt(8)
v_mfma_f32_16x16x32_bf16 v[32:35], v[168:171], a[168:171], v[32:35]
s_waitcnt lgkmcnt(7)
ds_read_b128 a[204:207], v221 offset:4608
v_mfma_f32_16x16x32_bf16 v[32:35], v[172:175], a[172:175], v[32:35]
s_nop 0
ds_read_b128 a[208:211], v225 offset:0
v_mfma_f32_16x16x32_bf16 v[80:83], v[92:95], a[160:163], 0
s_nop 0
v_mfma_f32_16x16x32_bf16 v[80:83], v[140:143], a[164:167], v[80:83]
s_waitcnt lgkmcnt(2)
ds_read_b128 a[212:215], v225 offset:4608
v_mfma_f32_16x16x32_bf16 v[80:83], v[204:207], a[176:179], v[80:83]
ds_read_b128 a[216:219], v221 offset:6912
v_mfma_f32_16x16x32_bf16 v[80:83], v[168:171], a[180:183], v[80:83]
v_mfma_f32_16x16x32_bf16 v[80:83], v[172:175], a[184:187], v[80:83]
ds_read_b128 a[220:223], v225 offset:2304
v_mfma_f32_16x16x32_bf16 v[128:131], v[92:95], a[164:167], 0
ds_read_b128 a[224:227], v225 offset:6912
v_and_b32_e64 v92, v216, v213
v_mfma_f32_16x16x32_bf16 v[128:131], v[140:143], a[176:179], v[128:131]
v_and_b32 v93, v217, v213
v_and_b32 v94, v218, v213
v_mfma_f32_16x16x32_bf16 v[128:131], v[204:207], a[188:191], v[128:131]
ds_read_b128 a[228:231], v221 offset:9216
v_and_b32_e64 v95, v219, v213
v_mfma_f32_16x16x32_bf16 v[128:131], v[168:171], a[172:175], v[128:131]
ds_read_b128 a[232:235], v229 offset:9216
v_and_b32_e64 v140, v216, v214
v_mfma_f32_16x16x32_bf16 v[128:131], v[172:175], a[192:195], v[128:131]
v_and_b32 v141, v217, v214
v_and_b32 v142, v218, v214
v_mfma_f32_16x16x32_bf16 v[36:39], v[176:179], a[196:199], 0
ds_read_b128 a[156:159], v222 offset:0
v_and_b32 v143, v219, v214
v_mfma_f32_16x16x32_bf16 v[36:39], v[180:183], a[200:203], v[36:39]
s_waitcnt lgkmcnt(3)
ds_read_b128 a[160:163], v222 offset:2304
v_and_b32_e64 v204, v216, v215
v_mfma_f32_16x16x32_bf16 v[36:39], v[184:187], a[204:207], v[36:39]
v_and_b32 v205, v217, v215
v_and_b32 v206, v218, v215
v_mfma_f32_16x16x32_bf16 v[36:39], v[188:191], a[208:211], v[36:39]
ds_read_b128 a[164:167], v222 offset:4608
v_and_b32_e64 v207, v219, v215
v_mfma_f32_16x16x32_bf16 v[36:39], v[192:195], a[212:215], v[36:39]
ds_read_b128 a[168:171], v226 offset:0
v_and_b32_e64 v168, v216, v208
v_mfma_f32_16x16x32_bf16 v[84:87], v[176:179], a[200:203], 0
s_waitcnt vmcnt(0)
s_barrier
v_and_b32_e64 v169, v217, v208
v_mfma_f32_16x16x32_bf16 v[84:87], v[180:183], a[204:207], v[84:87]
ds_read_b128 a[172:175], v226 offset:4608
v_and_b32_e64 v170, v218, v208
v_mfma_f32_16x16x32_bf16 v[84:87], v[184:187], a[216:219], v[84:87]
ds_read_b128 a[176:179], v222 offset:6912
v_and_b32_e64 v171, v219, v208
v_mfma_f32_16x16x32_bf16 v[84:87], v[188:191], a[220:223], v[84:87]
ds_read_b64_tr_b16 a[96:97], v255 offset:0
v_and_b32_e64 v172, v216, v209
v_mfma_f32_16x16x32_bf16 v[84:87], v[192:195], a[224:227], v[84:87]
ds_read_b128 a[180:183], v226 offset:2304
v_and_b32_e64 v173, v217, v209
v_mfma_f32_16x16x32_bf16 v[132:135], v[176:179], a[204:207], 0
ds_read_b128 a[184:187], v226 offset:6912
v_and_b32 v174, v218, v209
v_mfma_f32_16x16x32_bf16 v[132:135], v[180:183], a[216:219], v[132:135]
s_waitcnt lgkmcnt(4)
ds_read_b64_tr_b16 a[98:99], v255 offset:2048
v_and_b32_e64 v175, v219, v209
v_mfma_f32_16x16x32_bf16 v[132:135], v[184:187], a[228:231], v[132:135]
ds_read_b128 a[188:191], v222 offset:9216
v_and_b32_e64 v176, v216, v210
v_mfma_f32_16x16x32_bf16 v[132:135], v[188:191], a[212:215], v[132:135]
ds_read_b128 a[192:195], v230 offset:9216
v_and_b32_e64 v177, v217, v210
v_mfma_f32_16x16x32_bf16 v[132:135], v[192:195], a[232:235], v[132:135]
ds_read_b64_tr_b16 a[100:101], v255 offset:4096
v_and_b32_e64 v178, v218, v210
v_mfma_f32_16x16x32_bf16 v[40:43], v[196:199], a[156:159], 0
ds_read_b128 a[196:199], v223 offset:0
v_and_b32_e64 v179, v219, v210
v_mfma_f32_16x16x32_bf16 v[40:43], v[200:203], a[160:163], v[40:43]
ds_read_b128 a[200:203], v223 offset:2304
v_and_b32_e64 v180, v216, v211
v_mfma_f32_16x16x32_bf16 v[40:43], v[44:47], a[164:167], v[40:43]
ds_read_b64_tr_b16 a[102:103], v255 offset:6144
v_and_b32_e64 v181, v217, v211
v_mfma_f32_16x16x32_bf16 v[40:43], v[92:95], a[168:171], v[40:43]
ds_read_b128 a[204:207], v223 offset:4608
v_and_b32_e64 v182, v218, v211
v_mfma_f32_16x16x32_bf16 v[40:43], v[140:143], a[172:175], v[40:43]
ds_read_b128 a[208:211], v227 offset:0
v_and_b32_e64 v183, v219, v211
v_mfma_f32_16x16x32_bf16 v[88:91], v[196:199], a[160:163], 0
ds_read_b64_tr_b16 a[104:105], v255 offset:8192
v_xor_b32 v209, 32, v255
v_mfma_f32_16x16x32_bf16 v[88:91], v[200:203], a[164:167], v[88:91]
s_waitcnt lgkmcnt(4)
ds_read_b128 a[212:215], v227 offset:4608
v_xor_b32_e64 v210, 64, v255
v_mfma_f32_16x16x32_bf16 v[88:91], v[44:47], a[176:179], v[88:91]
ds_read_b128 a[216:219], v223 offset:6912
v_xor_b32 v211, 96, v255
v_mfma_f32_16x16x32_bf16 v[88:91], v[92:95], a[180:183], v[88:91]
v_mfma_f32_16x16x32_bf16 v[88:91], v[140:143], a[184:187], v[88:91]
ds_read_b128 a[220:223], v227 offset:2304
v_mfma_f32_16x16x32_bf16 v[136:139], v[196:199], a[164:167], 0
ds_read_b128 a[224:227], v227 offset:6912
v_mfma_f32_16x16x32_bf16 v[136:139], v[200:203], a[176:179], v[136:139]
v_mfma_f32_16x16x32_bf16 v[136:139], v[44:47], a[188:191], v[136:139]
ds_read_b128 a[228:231], v223 offset:9216
v_mfma_f32_16x16x32_bf16 v[136:139], v[92:95], a[172:175], v[136:139]
ds_read_b128 a[232:235], v231 offset:9216
v_mfma_f32_16x16x32_bf16 v[136:139], v[140:143], a[192:195], v[136:139]
v_mfma_f32_16x16x32_bf16 v[44:47], v[204:207], a[196:199], 0
s_nop 0
ds_read_b64_tr_b16 a[106:107], v255 offset:10240
v_mfma_f32_16x16x32_bf16 v[44:47], v[168:171], a[200:203], v[44:47]
s_waitcnt lgkmcnt(3)
ds_read_b64_tr_b16 a[120:121], v255 offset:12288
v_mfma_f32_16x16x32_bf16 v[44:47], v[172:175], a[204:207], v[44:47]
ds_read_b64_tr_b16 a[122:123], v255 offset:14336
v_mfma_f32_16x16x32_bf16 v[44:47], v[176:179], a[208:211], v[44:47]
ds_read_b64_tr_b16 a[124:125], v255 offset:16384
v_mfma_f32_16x16x32_bf16 v[44:47], v[180:183], a[212:215], v[44:47]
ds_read_b64_tr_b16 a[126:127], v255 offset:18432
v_mfma_f32_16x16x32_bf16 v[92:95], v[204:207], a[200:203], 0
ds_read_b64_tr_b16 a[128:129], v255 offset:20480
v_mfma_f32_16x16x32_bf16 v[92:95], v[168:171], a[204:207], v[92:95]
ds_read_b64_tr_b16 a[130:131], v255 offset:22528
v_mfma_f32_16x16x32_bf16 v[92:95], v[172:175], a[216:219], v[92:95]
ds_read_b128 a[108:111], v236 offset:0
v_mfma_f32_16x16x32_bf16 v[92:95], v[176:179], a[220:223], v[92:95]
ds_read_b128 a[112:115], v236 offset:2304
v_mfma_f32_16x16x32_bf16 v[92:95], v[180:183], a[224:227], v[92:95]
ds_read_b128 a[116:119], v236 offset:4608
v_mfma_f32_16x16x32_bf16 v[140:143], v[204:207], a[204:207], 0
s_nop 0
ds_read_b128 a[132:135], v237 offset:0
v_mfma_f32_16x16x32_bf16 v[140:143], v[168:171], a[216:219], v[140:143]
s_waitcnt lgkmcnt(4)
ds_read_b128 a[136:139], v237 offset:2304
v_mfma_f32_16x16x32_bf16 v[140:143], v[172:175], a[228:231], v[140:143]
ds_read_b128 a[140:143], v237 offset:4608
v_mfma_f32_16x16x32_bf16 v[140:143], v[176:179], a[212:215], v[140:143]
v_mfma_f32_16x16x32_bf16 v[140:143], v[180:183], a[232:235], v[140:143]
ds_read_b128 v[216:219], v241 offset:0
v_pk_fma_f32 v[158:159], v[32:33], v[32:33], v[158:159]
v_pk_add_f32 v[156:157], v[32:33], v[156:157]
v_pk_fma_f32 v[162:163], v[80:81], v[80:81], v[162:163]
v_pk_add_f32 v[160:161], v[80:81], v[160:161]
v_pk_fma_f32 v[166:167], v[128:129], v[128:129], v[166:167]
v_pk_add_f32 v[164:165], v[128:129], v[164:165]
v_pk_fma_f32 v[158:159], v[34:35], v[34:35], v[158:159]
v_pk_add_f32 v[156:157], v[34:35], v[156:157]
v_pk_fma_f32 v[162:163], v[82:83], v[82:83], v[162:163]
v_pk_add_f32 v[160:161], v[82:83], v[160:161]
v_pk_fma_f32 v[166:167], v[130:131], v[130:131], v[166:167]
v_pk_add_f32 v[164:165], v[130:131], v[164:165]
v_pk_fma_f32 v[158:159], v[36:37], v[36:37], v[158:159]
v_pk_add_f32 v[156:157], v[36:37], v[156:157]
v_pk_fma_f32 v[162:163], v[84:85], v[84:85], v[162:163]
v_pk_add_f32 v[160:161], v[84:85], v[160:161]
v_pk_fma_f32 v[166:167], v[132:133], v[132:133], v[166:167]
v_pk_add_f32 v[164:165], v[132:133], v[164:165]
v_pk_fma_f32 v[158:159], v[38:39], v[38:39], v[158:159]
v_pk_add_f32 v[156:157], v[38:39], v[156:157]
v_pk_fma_f32 v[162:163], v[86:87], v[86:87], v[162:163]
v_pk_add_f32 v[160:161], v[86:87], v[160:161]
v_pk_fma_f32 v[166:167], v[134:135], v[134:135], v[166:167]
v_pk_add_f32 v[164:165], v[134:135], v[164:165]
v_pk_fma_f32 v[158:159], v[40:41], v[40:41], v[158:159]
v_pk_add_f32 v[156:157], v[40:41], v[156:157]
v_pk_fma_f32 v[162:163], v[88:89], v[88:89], v[162:163]
v_pk_add_f32 v[160:161], v[88:89], v[160:161]
v_pk_fma_f32 v[166:167], v[136:137], v[136:137], v[166:167]
v_pk_add_f32 v[164:165], v[136:137], v[164:165]
v_pk_fma_f32 v[158:159], v[42:43], v[42:43], v[158:159]
v_pk_add_f32 v[156:157], v[42:43], v[156:157]
v_pk_fma_f32 v[162:163], v[90:91], v[90:91], v[162:163]
v_pk_add_f32 v[160:161], v[90:91], v[160:161]
v_pk_fma_f32 v[166:167], v[138:139], v[138:139], v[166:167]
v_pk_add_f32 v[164:165], v[138:139], v[164:165]
v_pk_fma_f32 v[158:159], v[44:45], v[44:45], v[158:159]
v_pk_add_f32 v[156:157], v[44:45], v[156:157]
v_pk_fma_f32 v[162:163], v[92:93], v[92:93], v[162:163]
v_pk_add_f32 v[160:161], v[92:93], v[160:161]
v_pk_fma_f32 v[166:167], v[140:141], v[140:141], v[166:167]
v_pk_add_f32 v[164:165], v[140:141], v[164:165]
v_pk_fma_f32 v[158:159], v[46:47], v[46:47], v[158:159]
v_pk_add_f32 v[156:157], v[46:47], v[156:157]
v_pk_fma_f32 v[162:163], v[94:95], v[94:95], v[162:163]
v_pk_add_f32 v[160:161], v[94:95], v[160:161]
v_pk_fma_f32 v[166:167], v[142:143], v[142:143], v[166:167]
v_pk_add_f32 v[164:165], v[142:143], v[164:165]
v_add_f32 v156, v156, v157
v_add_f32 v158, v158, v159
v_add_f32 v160, v160, v161
v_add_f32 v162, v162, v163
v_add_f32 v164, v164, v165
v_add_f32 v166, v166, v167
v_permlane32_swap_b32 v156, v158
v_permlane32_swap_b32 v160, v162
v_permlane32_swap_b32 v164, v166
v_add_f32 v156, v156, v158
v_add_f32 v160, v160, v162
v_add_f32 v164, v164, v166
v_mov_b32 v165, v164
v_permlane16_swap_b32 v156, v160
s_nop 0
v_permlane16_swap_b32 v164, v165
v_add_f32 v156, v156, v160
v_add_f32 v157, v164, v165
s_nop 1
v_permlane32_swap_b32 v156, v157
s_mov_b32 s33, 0x3727c5ac
v_mul_f32 v159, 0xbbaaaaab, v156
v_mul_f32 v158, 0x3baaaaab, v157
v_fma_f32 v158, -v159, v159, v158
v_max_f32 v158, 0, v158
v_add_f32 v158, s33, v158
v_rsq_f32 v158, v158
s_nop 0
v_mul_f32 v156, 0x3eeff558, v158
v_mul_f32 v157, v159, v156
v_mov_b32 v160, v156
v_mov_b32 v161, v157
s_nop 0
v_permlane16_swap_b32 v156, v160
v_permlane16_swap_b32 v157, v161
v_mov_b32 v164, v156
v_mov_b32 v158, v160
v_mov_b32 v165, v157
v_mov_b32 v159, v161
v_permlane32_swap_b32 v156, v164
v_permlane32_swap_b32 v160, v158
v_permlane32_swap_b32 v157, v165
v_permlane32_swap_b32 v161, v159
v_pk_fma_f32 v[0:1], v[0:1], v[156:157], v[156:157] op_sel:[0,0,1] op_sel_hi:[1,0,1]
v_pk_fma_f32 v[2:3], v[2:3], v[156:157], v[156:157] op_sel:[0,0,1] op_sel_hi:[1,0,1]
v_pk_fma_f32 v[4:5], v[4:5], v[156:157], v[156:157] op_sel:[0,0,1] op_sel_hi:[1,0,1]
v_pk_fma_f32 v[6:7], v[6:7], v[156:157], v[156:157] op_sel:[0,0,1] op_sel_hi:[1,0,1]
v_pk_fma_f32 v[8:9], v[8:9], v[156:157], v[156:157] op_sel:[0,0,1] op_sel_hi:[1,0,1]
v_pk_fma_f32 v[10:11], v[10:11], v[156:157], v[156:157] op_sel:[0,0,1] op_sel_hi:[1,0,1]
v_pk_fma_f32 v[12:13], v[12:13], v[156:157], v[156:157] op_sel:[0,0,1] op_sel_hi:[1,0,1]
v_pk_fma_f32 v[14:15], v[14:15], v[156:157], v[156:157] op_sel:[0,0,1] op_sel_hi:[1,0,1]
v_pk_fma_f32 v[16:17], v[16:17], v[156:157], v[156:157] op_sel:[0,0,1] op_sel_hi:[1,0,1]
v_pk_fma_f32 v[18:19], v[18:19], v[156:157], v[156:157] op_sel:[0,0,1] op_sel_hi:[1,0,1]
v_pk_fma_f32 v[20:21], v[20:21], v[156:157], v[156:157] op_sel:[0,0,1] op_sel_hi:[1,0,1]
v_pk_fma_f32 v[22:23], v[22:23], v[156:157], v[156:157] op_sel:[0,0,1] op_sel_hi:[1,0,1]
v_pk_fma_f32 v[24:25], v[24:25], v[156:157], v[156:157] op_sel:[0,0,1] op_sel_hi:[1,0,1]
v_pk_fma_f32 v[26:27], v[26:27], v[156:157], v[156:157] op_sel:[0,0,1] op_sel_hi:[1,0,1]
v_pk_fma_f32 v[28:29], v[28:29], v[156:157], v[156:157] op_sel:[0,0,1] op_sel_hi:[1,0,1]
v_pk_fma_f32 v[30:31], v[30:31], v[156:157], v[156:157] op_sel:[0,0,1] op_sel_hi:[1,0,1]
v_pk_fma_f32 v[32:33], v[32:33], v[156:157], v[156:157] op_sel:[0,0,1] op_sel_hi:[1,0,1]
v_pk_fma_f32 v[34:35], v[34:35], v[156:157], v[156:157] op_sel:[0,0,1] op_sel_hi:[1,0,1]
v_pk_fma_f32 v[36:37], v[36:37], v[156:157], v[156:157] op_sel:[0,0,1] op_sel_hi:[1,0,1]
v_pk_fma_f32 v[38:39], v[38:39], v[156:157], v[156:157] op_sel:[0,0,1] op_sel_hi:[1,0,1]
v_pk_fma_f32 v[40:41], v[40:41], v[156:157], v[156:157] op_sel:[0,0,1] op_sel_hi:[1,0,1]
v_pk_fma_f32 v[42:43], v[42:43], v[156:157], v[156:157] op_sel:[0,0,1] op_sel_hi:[1,0,1]
v_pk_fma_f32 v[44:45], v[44:45], v[156:157], v[156:157] op_sel:[0,0,1] op_sel_hi:[1,0,1]
v_pk_fma_f32 v[46:47], v[46:47], v[156:157], v[156:157] op_sel:[0,0,1] op_sel_hi:[1,0,1]
v_pk_fma_f32 v[48:49], v[48:49], v[160:161], v[160:161] op_sel:[0,0,1] op_sel_hi:[1,0,1]
v_pk_fma_f32 v[50:51], v[50:51], v[160:161], v[160:161] op_sel:[0,0,1] op_sel_hi:[1,0,1]
v_pk_fma_f32 v[52:53], v[52:53], v[160:161], v[160:161] op_sel:[0,0,1] op_sel_hi:[1,0,1]
v_pk_fma_f32 v[54:55], v[54:55], v[160:161], v[160:161] op_sel:[0,0,1] op_sel_hi:[1,0,1]
v_pk_fma_f32 v[56:57], v[56:57], v[160:161], v[160:161] op_sel:[0,0,1] op_sel_hi:[1,0,1]
v_pk_fma_f32 v[58:59], v[58:59], v[160:161], v[160:161] op_sel:[0,0,1] op_sel_hi:[1,0,1]
v_pk_fma_f32 v[60:61], v[60:61], v[160:161], v[160:161] op_sel:[0,0,1] op_sel_hi:[1,0,1]
v_pk_fma_f32 v[62:63], v[62:63], v[160:161], v[160:161] op_sel:[0,0,1] op_sel_hi:[1,0,1]
v_pk_fma_f32 v[64:65], v[64:65], v[160:161], v[160:161] op_sel:[0,0,1] op_sel_hi:[1,0,1]
v_pk_fma_f32 v[66:67], v[66:67], v[160:161], v[160:161] op_sel:[0,0,1] op_sel_hi:[1,0,1]
v_pk_fma_f32 v[68:69], v[68:69], v[160:161], v[160:161] op_sel:[0,0,1] op_sel_hi:[1,0,1]
v_pk_fma_f32 v[70:71], v[70:71], v[160:161], v[160:161] op_sel:[0,0,1] op_sel_hi:[1,0,1]
v_pk_fma_f32 v[72:73], v[72:73], v[160:161], v[160:161] op_sel:[0,0,1] op_sel_hi:[1,0,1]
v_pk_fma_f32 v[74:75], v[74:75], v[160:161], v[160:161] op_sel:[0,0,1] op_sel_hi:[1,0,1]
v_pk_fma_f32 v[76:77], v[76:77], v[160:161], v[160:161] op_sel:[0,0,1] op_sel_hi:[1,0,1]
v_pk_fma_f32 v[78:79], v[78:79], v[160:161], v[160:161] op_sel:[0,0,1] op_sel_hi:[1,0,1]
v_pk_fma_f32 v[80:81], v[80:81], v[160:161], v[160:161] op_sel:[0,0,1] op_sel_hi:[1,0,1]
v_pk_fma_f32 v[82:83], v[82:83], v[160:161], v[160:161] op_sel:[0,0,1] op_sel_hi:[1,0,1]
v_pk_fma_f32 v[84:85], v[84:85], v[160:161], v[160:161] op_sel:[0,0,1] op_sel_hi:[1,0,1]
v_pk_fma_f32 v[86:87], v[86:87], v[160:161], v[160:161] op_sel:[0,0,1] op_sel_hi:[1,0,1]
v_pk_fma_f32 v[88:89], v[88:89], v[160:161], v[160:161] op_sel:[0,0,1] op_sel_hi:[1,0,1]
v_pk_fma_f32 v[90:91], v[90:91], v[160:161], v[160:161] op_sel:[0,0,1] op_sel_hi:[1,0,1]
v_pk_fma_f32 v[92:93], v[92:93], v[160:161], v[160:161] op_sel:[0,0,1] op_sel_hi:[1,0,1]
v_pk_fma_f32 v[94:95], v[94:95], v[160:161], v[160:161] op_sel:[0,0,1] op_sel_hi:[1,0,1]
s_mov_b32 s30, 589824
buffer_load_dwordx4 v[204:207], v238, s[12:15], s30 offen offset:0 sc1
v_pk_fma_f32 v[96:97], v[96:97], v[164:165], v[164:165] op_sel:[0,0,1] op_sel_hi:[1,0,1]
v_pk_fma_f32 v[98:99], v[98:99], v[164:165], v[164:165] op_sel:[0,0,1] op_sel_hi:[1,0,1]
v_pk_fma_f32 v[100:101], v[100:101], v[164:165], v[164:165] op_sel:[0,0,1] op_sel_hi:[1,0,1]
v_pk_fma_f32 v[102:103], v[102:103], v[164:165], v[164:165] op_sel:[0,0,1] op_sel_hi:[1,0,1]
v_pk_fma_f32 v[104:105], v[104:105], v[164:165], v[164:165] op_sel:[0,0,1] op_sel_hi:[1,0,1]
v_pk_fma_f32 v[106:107], v[106:107], v[164:165], v[164:165] op_sel:[0,0,1] op_sel_hi:[1,0,1]
v_pk_fma_f32 v[108:109], v[108:109], v[164:165], v[164:165] op_sel:[0,0,1] op_sel_hi:[1,0,1]
v_pk_fma_f32 v[110:111], v[110:111], v[164:165], v[164:165] op_sel:[0,0,1] op_sel_hi:[1,0,1]
v_pk_fma_f32 v[112:113], v[112:113], v[164:165], v[164:165] op_sel:[0,0,1] op_sel_hi:[1,0,1]
v_pk_fma_f32 v[114:115], v[114:115], v[164:165], v[164:165] op_sel:[0,0,1] op_sel_hi:[1,0,1]
v_pk_fma_f32 v[116:117], v[116:117], v[164:165], v[164:165] op_sel:[0,0,1] op_sel_hi:[1,0,1]
v_pk_fma_f32 v[118:119], v[118:119], v[164:165], v[164:165] op_sel:[0,0,1] op_sel_hi:[1,0,1]
v_pk_fma_f32 v[120:121], v[120:121], v[164:165], v[164:165] op_sel:[0,0,1] op_sel_hi:[1,0,1]
v_pk_fma_f32 v[122:123], v[122:123], v[164:165], v[164:165] op_sel:[0,0,1] op_sel_hi:[1,0,1]
v_pk_fma_f32 v[124:125], v[124:125], v[164:165], v[164:165] op_sel:[0,0,1] op_sel_hi:[1,0,1]
v_pk_fma_f32 v[126:127], v[126:127], v[164:165], v[164:165] op_sel:[0,0,1] op_sel_hi:[1,0,1]
v_pk_fma_f32 v[128:129], v[128:129], v[164:165], v[164:165] op_sel:[0,0,1] op_sel_hi:[1,0,1]
v_pk_fma_f32 v[130:131], v[130:131], v[164:165], v[164:165] op_sel:[0,0,1] op_sel_hi:[1,0,1]
v_pk_fma_f32 v[132:133], v[132:133], v[164:165], v[164:165] op_sel:[0,0,1] op_sel_hi:[1,0,1]
v_pk_fma_f32 v[134:135], v[134:135], v[164:165], v[164:165] op_sel:[0,0,1] op_sel_hi:[1,0,1]
v_pk_fma_f32 v[136:137], v[136:137], v[164:165], v[164:165] op_sel:[0,0,1] op_sel_hi:[1,0,1]
v_pk_fma_f32 v[138:139], v[138:139], v[164:165], v[164:165] op_sel:[0,0,1] op_sel_hi:[1,0,1]
v_pk_fma_f32 v[140:141], v[140:141], v[164:165], v[164:165] op_sel:[0,0,1] op_sel_hi:[1,0,1]
v_pk_fma_f32 v[142:143], v[142:143], v[164:165], v[164:165] op_sel:[0,0,1] op_sel_hi:[1,0,1]
v_cvt_pk_bf16_f32 v0, v0, v1
v_cvt_pk_bf16_f32 v1, v2, v3
v_cvt_pk_bf16_f32 v2, v4, v5
v_cvt_pk_bf16_f32 v3, v6, v7
v_cvt_pk_bf16_f32 v4, v8, v9
v_cvt_pk_bf16_f32 v5, v10, v11
v_cvt_pk_bf16_f32 v6, v12, v13
v_cvt_pk_bf16_f32 v7, v14, v15
v_cvt_pk_bf16_f32 v8, v16, v17
v_cvt_pk_bf16_f32 v9, v18, v19
v_cvt_pk_bf16_f32 v10, v20, v21
v_cvt_pk_bf16_f32 v11, v22, v23
v_cvt_pk_bf16_f32 v12, v24, v25
v_cvt_pk_bf16_f32 v13, v26, v27
v_cvt_pk_bf16_f32 v14, v28, v29
v_cvt_pk_bf16_f32 v15, v30, v31
v_cvt_pk_bf16_f32 v16, v32, v33
v_cvt_pk_bf16_f32 v17, v34, v35
v_cvt_pk_bf16_f32 v18, v36, v37
v_cvt_pk_bf16_f32 v19, v38, v39
v_cvt_pk_bf16_f32 v20, v40, v41
v_cvt_pk_bf16_f32 v21, v42, v43
v_cvt_pk_bf16_f32 v22, v44, v45
v_cvt_pk_bf16_f32 v23, v46, v47
v_cvt_pk_bf16_f32 v48, v48, v49
v_cvt_pk_bf16_f32 v49, v50, v51
v_cvt_pk_bf16_f32 v50, v52, v53
v_cvt_pk_bf16_f32 v51, v54, v55
v_cvt_pk_bf16_f32 v52, v56, v57
v_cvt_pk_bf16_f32 v53, v58, v59
v_cvt_pk_bf16_f32 v54, v60, v61
v_cvt_pk_bf16_f32 v55, v62, v63
v_cvt_pk_bf16_f32 v56, v64, v65
v_cvt_pk_bf16_f32 v57, v66, v67
v_cvt_pk_bf16_f32 v58, v68, v69
v_cvt_pk_bf16_f32 v59, v70, v71
v_cvt_pk_bf16_f32 v60, v72, v73
v_cvt_pk_bf16_f32 v61, v74, v75
v_cvt_pk_bf16_f32 v62, v76, v77
v_cvt_pk_bf16_f32 v63, v78, v79
v_cvt_pk_bf16_f32 v64, v80, v81
v_cvt_pk_bf16_f32 v65, v82, v83
v_cvt_pk_bf16_f32 v66, v84, v85
v_cvt_pk_bf16_f32 v67, v86, v87
v_cvt_pk_bf16_f32 v68, v88, v89
v_cvt_pk_bf16_f32 v69, v90, v91
v_cvt_pk_bf16_f32 v70, v92, v93
v_cvt_pk_bf16_f32 v71, v94, v95
s_mov_b64 vcc, -1
s_mov_b32 s62, 0
s_mov_b32 s63, s23
s_add_u32 s36, s23, 49152
s_xor_b32 s36, s36, s23
s_mov_b32 s37, 5
s_waitcnt lgkmcnt(0)
v_mfma_f32_16x16x32_bf16 v[24:27], a[96:99], v[0:3], v[216:219]
v_bfe_u32 v144, v238, 4, 4
v_bfe_u32 v145, v238, 8, 2
v_cvt_pk_bf16_f32 v96, v96, v97
v_cvt_pk_bf16_f32 v97, v98, v99
v_mfma_f32_16x16x32_bf16 v[24:27], a[100:103], v[4:7], v[24:27]
v_lshrrev_b32 v150, 1, v144
v_lshlrev_b32 v151, 1, v145
v_cvt_pk_bf16_f32 v98, v100, v101
v_cvt_pk_bf16_f32 v99, v102, v103
v_mfma_f32_16x16x32_bf16 v[24:27], a[104:107], v[8:11], v[24:27]
ds_read_b64_tr_b16 a[180:181], v209 offset:0
v_and_b32_e64 v146, 1, v144
v_cvt_pk_bf16_f32 v100, v104, v105
v_cvt_pk_bf16_f32 v101, v106, v107
v_mfma_f32_16x16x32_bf16 v[24:27], a[120:123], v[12:15], v[24:27]
ds_read_b64_tr_b16 a[182:183], v209 offset:2048
v_sub_u32_e64 v152, v150, v151
v_cvt_pk_bf16_f32 v102, v108, v109
v_mfma_f32_16x16x32_bf16 v[24:27], a[124:127], v[16:19], v[24:27]
ds_read_b64_tr_b16 a[184:185], v209 offset:4096
v_lshlrev_b32_e64 v147, 4, v146
v_cvt_pk_bf16_f32 v103, v110, v111
v_cvt_pk_bf16_f32 v104, v112, v113
v_mfma_f32_16x16x32_bf16 v[24:27], a[128:131], v[20:23], v[24:27]
ds_read_b64_tr_b16 a[186:187], v209 offset:6144
v_mov_b32 v148, 0x3f80
v_cvt_pk_bf16_f32 v105, v114, v115
v_cvt_pk_bf16_f32 v106, v116, v117
v_mfma_f32_16x16x32_bf16 v[28:31], a[128:131], v[68:71], v[216:219]
s_nop 0
ds_read_b64_tr_b16 a[188:189], v209 offset:8192
v_cmp_eq_u32 vcc, 0, v152
v_cvt_pk_bf16_f32 v107, v118, v119
v_mfma_f32_16x16x32_bf16 v[28:31], a[124:127], v[64:67], v[28:31]
ds_read_b64_tr_b16 a[190:191], v209 offset:10240
v_lshlrev_b32_e64 v149, v147, v148
v_cvt_pk_bf16_f32 v108, v120, v121
v_cvt_pk_bf16_f32 v109, v122, v123
v_mfma_f32_16x16x32_bf16 v[28:31], a[120:123], v[60:63], v[28:31]
ds_read_b64_tr_b16 a[192:193], v209 offset:12288
v_mov_b32 v194, 0
s_waitcnt vmcnt(0)
v_cmp_eq_u32 s[34:35], s43, v204
v_cvt_pk_bf16_f32 v110, v124, v125
v_cvt_pk_bf16_f32 v111, v126, v127
v_mfma_f32_16x16x32_bf16 v[28:31], a[104:107], v[56:59], v[28:31]
s_nop 0
ds_read_b64_tr_b16 a[194:195], v209 offset:14336
v_cndmask_b32 v192, 0, v149, vcc
v_cmp_eq_u32 s[64:65], s43, v205
v_cvt_pk_bf16_f32 v112, v128, v129
v_cvt_pk_bf16_f32 v113, v130, v131
v_mfma_f32_16x16x32_bf16 v[28:31], a[100:103], v[52:55], v[28:31]
s_nop 0
ds_read_b64_tr_b16 a[196:197], v209 offset:16384
v_cmp_eq_u32 vcc, 1, v152
v_cmp_eq_u32 s[66:67], s43, v206
v_cvt_pk_bf16_f32 v114, v132, v133
v_mfma_f32_16x16x32_bf16 v[28:31], a[96:99], v[48:51], v[28:31]
ds_read_b64_tr_b16 a[198:199], v209 offset:18432
v_mov_b32_e64 v196, 0
v_cmp_eq_u32 s[68:69], s43, v207
v_cvt_pk_bf16_f32 v115, v134, v135
v_cvt_pk_bf16_f32 v116, v136, v137
v_mfma_f32_16x16x32_bf16 v[32:35], a[96:99], v[96:99], v[216:219]
ds_read_b64_tr_b16 a[200:201], v209 offset:20480
v_mov_b32 v198, v192
v_cvt_pk_bf16_f32 v117, v138, v139
v_cvt_pk_bf16_f32 v118, v140, v141
v_mfma_f32_16x16x32_bf16 v[32:35], a[100:103], v[100:103], v[32:35]
s_and_b64 s[34:35], s[34:35], s[64:65]
ds_read_b64_tr_b16 a[202:203], v209 offset:22528
ds_read_b128 v[216:219], v241 offset:64
v_cvt_pk_bf16_f32 v119, v142, v143
v_mfma_f32_16x16x32_bf16 v[32:35], a[104:107], v[104:107], v[32:35]
s_and_b64 s[66:67], s[66:67], s[68:69]
v_cndmask_b32 v193, 0, v149, vcc
v_mov_b32_e64 v195, 0
v_mfma_f32_16x16x32_bf16 v[32:35], a[120:123], v[108:111], v[32:35]
s_and_b64 s[34:35], s[34:35], s[66:67]
v_mov_b32 v197, 0
v_mov_b32_e64 v199, v193
v_mfma_f32_16x16x32_bf16 v[32:35], a[124:127], v[112:115], v[32:35]
s_or_b32 s34, s34, 0x0fffffff
s_bitset1_b32 s35, 31
v_mfma_f32_16x16x32_bf16 v[32:35], a[128:131], v[116:119], v[32:35]
s_waitcnt lgkmcnt(0)
s_cmp_eq_u64 s[34:35], -1
s_cbranch_scc0 L_slow_p
L_fast_p:
v_pk_fma_f32 v[86:87], v[24:25], v[24:25], s[28:29]
v_pk_fma_f32 v[84:85], v[26:27], v[26:27], s[28:29]
v_pk_fma_f32 v[90:91], v[28:29], v[28:29], s[28:29]
v_pk_fma_f32 v[88:89], v[30:31], v[30:31], s[28:29]
s_nop 0
s_nop 0
v_pk_fma_f32 v[94:95], v[32:33], v[32:33], s[28:29]
v_pk_fma_f32 v[92:93], v[34:35], v[34:35], s[28:29]
v_pk_mul_f32 v[86:87], v[24:25], v[86:87] neg_lo:[1,0] neg_hi:[1,0]
v_pk_mul_f32 v[84:85], v[26:27], v[84:85] neg_lo:[1,0] neg_hi:[1,0]
v_pk_mul_f32 v[90:91], v[28:29], v[90:91] neg_lo:[1,0] neg_hi:[1,0]
v_pk_mul_f32 v[88:89], v[30:31], v[88:89] neg_lo:[1,0] neg_hi:[1,0]
v_pk_mul_f32 v[94:95], v[32:33], v[94:95] neg_lo:[1,0] neg_hi:[1,0]
v_pk_mul_f32 v[92:93], v[34:35], v[92:93] neg_lo:[1,0] neg_hi:[1,0]
v_mfma_f32_16x16x32_bf16 v[36:39], a[180:183], v[0:3], v[216:219]
s_add_u32 m0, s23, 129024
s_add_u32 s26, s62, 49152
ds_read_b128 a[204:207], v253 offset:0
v_exp_f32_e64 v88, v88
v_mfma_f32_16x16x32_bf16 v[36:39], a[184:187], v[4:7], v[36:39]
buffer_load_dwordx4 v248, s[12:15], s26 offen offset:0 sc1 lds
v_mfma_f32_16x16x32_bf16 v[36:39], a[188:191], v[8:11], v[36:39]
ds_read_b64_tr_b16 a[156:157], v210 offset:0
ds_read_b128 a[208:211], v253 offset:16
v_mfma_f32_16x16x32_bf16 v[36:39], a[192:195], v[12:15], v[36:39]
ds_read_b64_tr_b16 a[158:159], v210 offset:2048
v_exp_f32_e64 v89, v89
v_mfma_f32_16x16x32_bf16 v[36:39], a[196:199], v[16:19], v[36:39]
ds_read_b64_tr_b16 a[160:161], v210 offset:4096
v_exp_f32_e64 v94, v94
v_mfma_f32_16x16x32_bf16 v[36:39], a[200:203], v[20:23], v[36:39]
ds_read_b64_tr_b16 a[162:163], v210 offset:6144
v_exp_f32_e64 v95, v95
v_mfma_f32_16x16x32_bf16 v[40:43], a[200:203], v[68:71], v[216:219]
ds_read_b64_tr_b16 a[164:165], v210 offset:8192
v_exp_f32_e64 v92, v92
v_mfma_f32_16x16x32_bf16 v[40:43], a[196:199], v[64:67], v[40:43]
ds_read_b64_tr_b16 a[166:167], v210 offset:10240
v_exp_f32_e64 v93, v93
v_mfma_f32_16x16x32_bf16 v[40:43], a[192:195], v[60:63], v[40:43]
ds_read_b64_tr_b16 a[168:169], v210 offset:12288
v_mfma_f32_16x16x32_bf16 v[40:43], a[188:191], v[56:59], v[40:43]
ds_read_b64_tr_b16 a[170:171], v210 offset:14336
v_mfma_f32_16x16x32_bf16 v[40:43], a[184:187], v[52:55], v[40:43]
ds_read_b64_tr_b16 a[172:173], v210 offset:16384
v_mfma_f32_16x16x32_bf16 v[40:43], a[180:183], v[48:51], v[40:43]
ds_read_b64_tr_b16 a[174:175], v210 offset:18432
v_mfma_f32_16x16x32_bf16 v[44:47], a[180:183], v[96:99], v[216:219]
ds_read_b64_tr_b16 a[176:177], v210 offset:20480
v_mfma_f32_16x16x32_bf16 v[44:47], a[184:187], v[100:103], v[44:47]
ds_read_b64_tr_b16 a[178:179], v210 offset:22528
ds_read_b128 v[216:219], v241 offset:128
v_mfma_f32_16x16x32_bf16 v[44:47], a[188:191], v[104:107], v[44:47]
buffer_load_dwordx4 v248, s[12:15], s26 offen offset:1024 sc1 lds
v_mfma_f32_16x16x32_bf16 v[44:47], a[192:195], v[108:111], v[44:47]
buffer_load_dwordx4 v248, s[12:15], s26 offen offset:2048 sc1 lds
v_mfma_f32_16x16x32_bf16 v[44:47], a[196:199], v[112:115], v[44:47]
s_add_u32 m0, s23, 132096
s_add_u32 s26, s62, 52224
v_exp_f32 v86, v86
v_mfma_f32_16x16x32_bf16 v[44:47], a[200:203], v[116:119], v[44:47]
s_waitcnt lgkmcnt(2)
buffer_load_dwordx4 v248, s[12:15], s26 offen offset:0 sc1 lds
v_mfma_f32_16x16x32_bf16 a[0:3], v[192:195], a[12:15], a[204:207]
buffer_load_dwordx4 v248, s[12:15], s26 offen offset:1024 sc1 lds
v_mfma_f32_16x16x32_bf16 a[12:15], v[196:199], a[12:15], a[208:211]
buffer_load_dwordx4 v248, s[12:15], s26 offen offset:2048 sc1 lds
v_mfma_f32_16x16x32_bf16 a[4:7], v[192:195], a[16:19], a[204:207]
v_exp_f32_e64 v87, v87
v_mfma_f32_16x16x32_bf16 a[16:19], v[196:199], a[16:19], a[208:211]
v_exp_f32_e64 v84, v84
v_mfma_f32_16x16x32_bf16 a[8:11], v[192:195], a[20:23], a[204:207]
v_exp_f32_e64 v85, v85
v_mfma_f32_16x16x32_bf16 a[20:23], v[196:199], a[20:23], a[208:211]
s_waitcnt lgkmcnt(0)
v_exp_f32 v90, v90
v_exp_f32 v91, v91
v_add_f32 v86, 1.0, v86
v_add_f32_e64 v87, 1.0, v87
v_pk_add_f32 v[84:85], v[84:85], 1.0 op_sel_hi:[1,0]
v_pk_add_f32 v[90:91], v[90:91], 1.0 op_sel_hi:[1,0]
v_pk_add_f32 v[88:89], v[88:89], 1.0 op_sel_hi:[1,0]
v_pk_add_f32 v[94:95], v[94:95], 1.0 op_sel_hi:[1,0]
v_pk_add_f32 v[92:93], v[92:93], 1.0 op_sel_hi:[1,0]
v_pk_fma_f32 v[122:123], v[36:37], v[36:37], s[28:29]
v_pk_fma_f32 v[120:121], v[38:39], v[38:39], s[28:29]
v_pk_fma_f32 v[126:127], v[40:41], v[40:41], s[28:29]
v_pk_fma_f32 v[124:125], v[42:43], v[42:43], s[28:29]
v_pk_fma_f32 v[130:131], v[44:45], v[44:45], s[28:29]
v_pk_fma_f32 v[128:129], v[46:47], v[46:47], s[28:29]
v_pk_mul_f32 v[122:123], v[36:37], v[122:123] neg_lo:[1,0] neg_hi:[1,0]
v_pk_mul_f32 v[120:121], v[38:39], v[120:121] neg_lo:[1,0] neg_hi:[1,0]
v_pk_mul_f32 v[126:127], v[40:41], v[126:127] neg_lo:[1,0] neg_hi:[1,0]
v_pk_mul_f32 v[124:125], v[42:43], v[124:125] neg_lo:[1,0] neg_hi:[1,0]
v_pk_mul_f32 v[130:131], v[44:45], v[130:131] neg_lo:[1,0] neg_hi:[1,0]
v_pk_mul_f32 v[128:129], v[46:47], v[128:129] neg_lo:[1,0] neg_hi:[1,0]
v_mfma_f32_16x16x32_bf16 v[72:75], a[156:159], v[0:3], v[216:219]
s_add_u32 m0, s63, 0
s_add_u32 s26, s62, 24576
ds_read_b128 a[204:207], v253 offset:128
v_exp_f32 v124, v124
v_mfma_f32_16x16x32_bf16 v[72:75], a[160:163], v[4:7], v[72:75]
buffer_load_dwordx4 v248, s[12:15], s26 offen offset:0 sc1 lds
v_rcp_f32_e64 v88, v88
v_mfma_f32_16x16x32_bf16 v[72:75], a[164:167], v[8:11], v[72:75]
ds_read_b64_tr_b16 a[180:181], v211 offset:0
ds_read_b128 a[208:211], v253 offset:144
v_rcp_f32_e64 v89, v89
v_mfma_f32_16x16x32_bf16 v[72:75], a[168:171], v[12:15], v[72:75]
ds_read_b64_tr_b16 a[182:183], v211 offset:2048
v_exp_f32_e64 v125, v125
v_mfma_f32_16x16x32_bf16 v[72:75], a[172:175], v[16:19], v[72:75]
ds_read_b64_tr_b16 a[184:185], v211 offset:4096
v_exp_f32_e64 v130, v130
v_mfma_f32_16x16x32_bf16 v[72:75], a[176:179], v[20:23], v[72:75]
ds_read_b64_tr_b16 a[186:187], v211 offset:6144
v_exp_f32_e64 v131, v131
v_mfma_f32_16x16x32_bf16 v[76:79], a[176:179], v[68:71], v[216:219]
ds_read_b64_tr_b16 a[188:189], v211 offset:8192
v_exp_f32_e64 v128, v128
v_mfma_f32_16x16x32_bf16 v[76:79], a[172:175], v[64:67], v[76:79]
ds_read_b64_tr_b16 a[190:191], v211 offset:10240
v_exp_f32_e64 v129, v129
v_mfma_f32_16x16x32_bf16 v[76:79], a[168:171], v[60:63], v[76:79]
ds_read_b64_tr_b16 a[192:193], v211 offset:12288
v_rcp_f32_e64 v86, v86
v_mfma_f32_16x16x32_bf16 v[76:79], a[164:167], v[56:59], v[76:79]
ds_read_b64_tr_b16 a[194:195], v211 offset:14336
v_rcp_f32_e64 v87, v87
v_mfma_f32_16x16x32_bf16 v[76:79], a[160:163], v[52:55], v[76:79]
ds_read_b64_tr_b16 a[196:197], v211 offset:16384
v_rcp_f32_e64 v84, v84
v_mfma_f32_16x16x32_bf16 v[76:79], a[156:159], v[48:51], v[76:79]
ds_read_b64_tr_b16 a[198:199], v211 offset:18432
v_rcp_f32_e64 v85, v85
v_mfma_f32_16x16x32_bf16 v[80:83], a[156:159], v[96:99], v[216:219]
ds_read_b64_tr_b16 a[200:201], v211 offset:20480
v_rcp_f32_e64 v90, v90
v_mfma_f32_16x16x32_bf16 v[80:83], a[160:163], v[100:103], v[80:83]
ds_read_b64_tr_b16 a[202:203], v211 offset:22528
ds_read_b128 v[216:219], v241 offset:192
v_rcp_f32_e64 v94, v94
v_mfma_f32_16x16x32_bf16 v[80:83], a[164:167], v[104:107], v[80:83]
buffer_load_dwordx4 v248, s[12:15], s26 offen offset:1024 sc1 lds
v_rcp_f32_e64 v95, v95
v_mfma_f32_16x16x32_bf16 v[80:83], a[168:171], v[108:111], v[80:83]
buffer_load_dwordx4 v248, s[12:15], s26 offen offset:2048 sc1 lds
v_rcp_f32_e64 v92, v92
v_mfma_f32_16x16x32_bf16 v[80:83], a[172:175], v[112:115], v[80:83]
s_add_u32 m0, s63, 3072
s_add_u32 s26, s62, 27648
v_exp_f32_e64 v122, v122
v_rcp_f32 v93, v93
v_mfma_f32_16x16x32_bf16 v[80:83], a[176:179], v[116:119], v[80:83]
s_waitcnt lgkmcnt(2)
buffer_load_dwordx4 v248, s[12:15], s26 offen offset:0 sc1 lds
v_mfma_f32_16x16x32_bf16 a[24:27], v[192:195], a[36:39], a[204:207]
buffer_load_dwordx4 v248, s[12:15], s26 offen offset:1024 sc1 lds
v_mfma_f32_16x16x32_bf16 a[36:39], v[196:199], a[36:39], a[208:211]
buffer_load_dwordx4 v248, s[12:15], s26 offen offset:2048 sc1 lds
v_mfma_f32_16x16x32_bf16 a[28:31], v[192:195], a[40:43], a[204:207]
v_exp_f32_e64 v123, v123
v_mfma_f32_16x16x32_bf16 a[40:43], v[196:199], a[40:43], a[208:211]
v_exp_f32_e64 v120, v120
v_mfma_f32_16x16x32_bf16 a[32:35], v[192:195], a[44:47], a[204:207]
v_exp_f32_e64 v121, v121
v_mfma_f32_16x16x32_bf16 a[44:47], v[196:199], a[44:47], a[208:211]
s_waitcnt lgkmcnt(0)
v_exp_f32 v126, v126
v_exp_f32 v127, v127
v_rcp_f32 v91, v91
v_pk_add_f32 v[122:123], v[122:123], 1.0 op_sel_hi:[1,0]
v_pk_add_f32 v[120:121], v[120:121], 1.0 op_sel_hi:[1,0]
v_pk_add_f32 v[126:127], v[126:127], 1.0 op_sel_hi:[1,0]
v_pk_add_f32 v[124:125], v[124:125], 1.0 op_sel_hi:[1,0]
v_pk_add_f32 v[130:131], v[130:131], 1.0 op_sel_hi:[1,0]
v_pk_add_f32 v[128:129], v[128:129], 1.0 op_sel_hi:[1,0]
v_pk_mul_f32 v[86:87], v[24:25], v[86:87]
v_pk_mul_f32 v[84:85], v[26:27], v[84:85]
v_pk_mul_f32 v[90:91], v[28:29], v[90:91]
v_pk_mul_f32 v[88:89], v[30:31], v[88:89]
v_pk_mul_f32 v[94:95], v[32:33], v[94:95]
v_pk_mul_f32 v[92:93], v[34:35], v[92:93]
v_pk_fma_f32 v[134:135], v[72:73], v[72:73], s[28:29]
v_pk_fma_f32 v[132:133], v[74:75], v[74:75], s[28:29]
v_pk_fma_f32 v[138:139], v[76:77], v[76:77], s[28:29]
v_pk_fma_f32 v[136:137], v[78:79], v[78:79], s[28:29]
v_pk_fma_f32 v[142:143], v[80:81], v[80:81], s[28:29]
v_pk_fma_f32 v[140:141], v[82:83], v[82:83], s[28:29]
v_pk_mul_f32 v[134:135], v[72:73], v[134:135] neg_lo:[1,0] neg_hi:[1,0]
v_pk_mul_f32 v[132:133], v[74:75], v[132:133] neg_lo:[1,0] neg_hi:[1,0]
v_pk_mul_f32 v[138:139], v[76:77], v[138:139] neg_lo:[1,0] neg_hi:[1,0]
v_pk_mul_f32 v[136:137], v[78:79], v[136:137] neg_lo:[1,0] neg_hi:[1,0]
v_pk_mul_f32 v[142:143], v[80:81], v[142:143] neg_lo:[1,0] neg_hi:[1,0]
v_pk_mul_f32 v[140:141], v[82:83], v[140:141] neg_lo:[1,0] neg_hi:[1,0]
s_waitcnt vmcnt(6)
s_barrier
v_mfma_f32_16x16x32_bf16 v[144:147], a[180:183], v[0:3], v[216:219]
ds_read_b128 a[204:207], v253 offset:256
v_cvt_pk_bf16_f32 v168, v86, v87
v_rcp_f32_e64 v121, v121
v_mfma_f32_16x16x32_bf16 v[144:147], a[184:187], v[4:7], v[144:147]
ds_read_b128 a[156:159], v239 offset:24576
v_cvt_pk_bf16_f32 v169, v84, v85
v_rcp_f32_e64 v126, v126
v_mfma_f32_16x16x32_bf16 v[144:147], a[188:191], v[8:11], v[144:147]
s_add_u32 m0, s63, 24576
ds_read_b128 a[208:211], v253 offset:272
v_cvt_pk_bf16_f32 v172, v90, v91
v_rcp_f32_e64 v127, v127
v_mfma_f32_16x16x32_bf16 v[144:147], a[192:195], v[12:15], v[144:147]
s_add_u32 s26, s62, 73728
ds_read_b128 a[160:163], v239 offset:25600
v_cvt_pk_bf16_f32 v173, v88, v89
v_rcp_f32_e64 v124, v124
v_mfma_f32_16x16x32_bf16 v[144:147], a[196:199], v[16:19], v[144:147]
buffer_load_dwordx4 v248, s[12:15], s26 offen offset:0 sc1 lds
v_rcp_f32_e64 v125, v125
v_mfma_f32_16x16x32_bf16 v[144:147], a[200:203], v[20:23], v[144:147]
ds_read_b128 a[164:167], v239 offset:26624
v_cvt_pk_bf16_f32 v176, v94, v95
v_rcp_f32_e64 v130, v130
v_mfma_f32_16x16x32_bf16 v[148:151], a[200:203], v[68:71], v[216:219]
buffer_load_dwordx4 v248, s[12:15], s26 offen offset:1024 sc1 lds
v_rcp_f32_e64 v131, v131
v_mfma_f32_16x16x32_bf16 v[148:151], a[196:199], v[64:67], v[148:151]
ds_read_b128 a[168:171], v239 offset:27648
v_cvt_pk_bf16_f32 v177, v92, v93
v_rcp_f32_e64 v128, v128
v_mfma_f32_16x16x32_bf16 v[148:151], a[192:195], v[60:63], v[148:151]
v_exp_f32 v134, v134
v_rcp_f32 v129, v129
v_mfma_f32_16x16x32_bf16 v[148:151], a[188:191], v[56:59], v[148:151]
ds_read_b128 a[172:175], v239 offset:28672
v_exp_f32_e64 v141, v141
v_mfma_f32_16x16x32_bf16 v[148:151], a[184:187], v[52:55], v[148:151]
buffer_load_dwordx4 v248, s[12:15], s26 offen offset:2048 sc1 lds
v_mfma_f32_16x16x32_bf16 v[148:151], a[180:183], v[48:51], v[148:151]
ds_read_b128 a[176:179], v239 offset:29696
v_rcp_f32_e64 v122, v122
v_mfma_f32_16x16x32_bf16 v[152:155], a[180:183], v[96:99], v[216:219]
s_add_u32 m0, s63, 27648
v_exp_f32_e64 v135, v135
v_mfma_f32_16x16x32_bf16 v[152:155], a[184:187], v[100:103], v[152:155]
s_add_u32 s26, s62, 76800
ds_read_b128 v[216:219], v241 offset:256
v_rcp_f32_e64 v123, v123
v_mfma_f32_16x16x32_bf16 v[152:155], a[188:191], v[104:107], v[152:155]
buffer_load_dwordx4 v248, s[12:15], s26 offen offset:0 sc1 lds
v_mfma_f32_16x16x32_bf16 v[152:155], a[192:195], v[108:111], v[152:155]
v_exp_f32_e64 v132, v132
v_mfma_f32_16x16x32_bf16 v[152:155], a[196:199], v[112:115], v[152:155]
v_exp_f32_e64 v133, v133
v_mfma_f32_16x16x32_bf16 v[152:155], a[200:203], v[116:119], v[152:155]
s_waitcnt lgkmcnt(1)
v_exp_f32 v138, v138
v_mfma_f32_16x16x32_bf16 a[48:51], v[192:195], a[60:63], a[204:207]
buffer_load_dwordx4 v248, s[12:15], s26 offen offset:1024 sc1 lds
v_mfma_f32_16x16x32_bf16 a[60:63], v[196:199], a[60:63], a[208:211]
v_exp_f32_e64 v139, v139
v_mfma_f32_16x16x32_bf16 a[52:55], v[192:195], a[64:67], a[204:207]
v_exp_f32_e64 v136, v136
v_mfma_f32_16x16x32_bf16 a[64:67], v[196:199], a[64:67], a[208:211]
v_exp_f32_e64 v137, v137
v_mfma_f32_16x16x32_bf16 a[56:59], v[192:195], a[68:71], a[204:207]
v_exp_f32_e64 v142, v142
v_mfma_f32_16x16x32_bf16 a[68:71], v[196:199], a[68:71], a[208:211]
s_waitcnt lgkmcnt(0)
v_exp_f32 v143, v143
v_exp_f32 v140, v140
v_rcp_f32 v120, v120
v_pk_add_f32 v[134:135], v[134:135], 1.0 op_sel_hi:[1,0]
v_pk_add_f32 v[132:133], v[132:133], 1.0 op_sel_hi:[1,0]
v_pk_add_f32 v[138:139], v[138:139], 1.0 op_sel_hi:[1,0]
v_pk_add_f32 v[136:137], v[136:137], 1.0 op_sel_hi:[1,0]
v_pk_add_f32 v[142:143], v[142:143], 1.0 op_sel_hi:[1,0]
v_pk_add_f32 v[140:141], v[140:141], 1.0 op_sel_hi:[1,0]
v_pk_mul_f32 v[122:123], v[36:37], v[122:123]
v_pk_mul_f32 v[120:121], v[38:39], v[120:121]
v_pk_mul_f32 v[126:127], v[40:41], v[126:127]
v_pk_mul_f32 v[124:125], v[42:43], v[124:125]
v_pk_mul_f32 v[130:131], v[44:45], v[130:131]
v_pk_mul_f32 v[128:129], v[46:47], v[128:129]
v_pk_fma_f32 v[158:159], v[144:145], v[144:145], s[28:29]
v_pk_fma_f32 v[156:157], v[146:147], v[146:147], s[28:29]
v_pk_fma_f32 v[162:163], v[148:149], v[148:149], s[28:29]
v_pk_fma_f32 v[160:161], v[150:151], v[150:151], s[28:29]
v_pk_fma_f32 v[166:167], v[152:153], v[152:153], s[28:29]
v_pk_fma_f32 v[164:165], v[154:155], v[154:155], s[28:29]
v_pk_mul_f32 v[158:159], v[144:145], v[158:159] neg_lo:[1,0] neg_hi:[1,0]
v_pk_mul_f32 v[156:157], v[146:147], v[156:157] neg_lo:[1,0] neg_hi:[1,0]
v_pk_mul_f32 v[162:163], v[148:149], v[162:163] neg_lo:[1,0] neg_hi:[1,0]
v_pk_mul_f32 v[160:161], v[150:151], v[160:161] neg_lo:[1,0] neg_hi:[1,0]
v_pk_mul_f32 v[166:167], v[152:153], v[166:167] neg_lo:[1,0] neg_hi:[1,0]
v_pk_mul_f32 v[164:165], v[154:155], v[164:165] neg_lo:[1,0] neg_hi:[1,0]
v_mfma_f32_16x16x32_bf16 v[24:27], a[156:159], v[0:3], v[216:219]
ds_read_b128 a[204:207], v253 offset:384
s_add_u32 s30, s20, 163072
v_mov_b32 v250, 0
v_rcp_f32 v136, v136
v_mfma_f32_16x16x32_bf16 v[24:27], a[160:163], v[4:7], v[24:27]
ds_read_b128 a[180:183], v239 offset:30720
v_mov_b32 v251, s30
v_rcp_f32 v137, v137
v_mfma_f32_16x16x32_bf16 v[24:27], a[164:167], v[8:11], v[24:27]
ds_read_b128 a[208:211], v253 offset:400
v_mov_b32 v252, 163072
v_rcp_f32_e64 v142, v142
v_mfma_f32_16x16x32_bf16 v[24:27], a[168:171], v[12:15], v[24:27]
ds_read_b128 a[184:187], v239 offset:31744
v_cvt_pk_bf16_f32 v170, v122, v123
v_rcp_f32_e64 v143, v143
v_mfma_f32_16x16x32_bf16 v[24:27], a[172:175], v[16:19], v[24:27]
ds_read_b128 a[212:215], v253 offset:512
v_cvt_pk_bf16_f32 v171, v120, v121
v_rcp_f32_e64 v140, v140
v_mfma_f32_16x16x32_bf16 v[24:27], a[176:179], v[20:23], v[24:27]
ds_read_b128 a[188:191], v239 offset:32768
v_cvt_pk_bf16_f32 v174, v126, v127
v_rcp_f32_e64 v141, v141
v_mfma_f32_16x16x32_bf16 v[28:31], a[176:179], v[68:71], v[216:219]
ds_read_b128 a[216:219], v253 offset:528
v_cvt_pk_bf16_f32 v175, v124, v125
v_mfma_f32_16x16x32_bf16 v[28:31], a[172:175], v[64:67], v[28:31]
ds_read_b128 a[192:195], v239 offset:33792
v_cvt_pk_bf16_f32 v178, v130, v131
v_mfma_f32_16x16x32_bf16 v[28:31], a[168:171], v[60:63], v[28:31]
buffer_load_dwordx4 v248, s[12:15], s26 offen offset:2048 sc1 lds
v_mfma_f32_16x16x32_bf16 v[28:31], a[164:167], v[56:59], v[28:31]
s_add_u32 m0, s23, 104448
s_add_u32 s26, s62, 98304
ds_read_b128 a[196:199], v239 offset:34816
v_cvt_pk_bf16_f32 v179, v128, v129
v_mfma_f32_16x16x32_bf16 v[28:31], a[160:163], v[52:55], v[28:31]
buffer_load_dwordx4 v248, s[12:15], s26 offen offset:0 sc1 lds
v_mfma_f32_16x16x32_bf16 v[28:31], a[156:159], v[48:51], v[28:31]
ds_read_b128 a[200:203], v239 offset:35840
v_rcp_f32_e64 v134, v134
v_mfma_f32_16x16x32_bf16 v[32:35], a[156:159], v[96:99], v[216:219]
buffer_load_dwordx4 v248, s[12:15], s26 offen offset:1024 sc1 lds
v_mfma_f32_16x16x32_bf16 v[32:35], a[160:163], v[100:103], v[32:35]
ds_read_b128 v[216:219], v241 offset:320
v_rcp_f32_e64 v135, v135
v_mfma_f32_16x16x32_bf16 v[32:35], a[164:167], v[104:107], v[32:35]
ds_write_b8 v251, v250
v_rcp_f32_e64 v132, v132
v_mfma_f32_16x16x32_bf16 v[32:35], a[168:171], v[108:111], v[32:35]
ds_write_b8 v251, v250 offset:4
v_rcp_f32_e64 v133, v133
v_mfma_f32_16x16x32_bf16 v[32:35], a[172:175], v[112:115], v[32:35]
v_mov_b32_e64 v250, 1
s_mov_b32 s33, 0x01010101
v_rcp_f32_e64 v138, v138
v_mfma_f32_16x16x32_bf16 v[32:35], a[176:179], v[116:119], v[32:35]
s_waitcnt lgkmcnt(3)
v_exp_f32 v158, v158
v_mfma_f32_16x16x32_bf16 a[72:75], v[192:195], a[84:87], a[204:207]
buffer_load_dwordx4 v248, s[12:15], s26 offen offset:2048 sc1 lds
v_mfma_f32_16x16x32_bf16 a[84:87], v[196:199], a[84:87], a[208:211]
v_exp_f32_e64 v159, v159
v_mfma_f32_16x16x32_bf16 a[76:79], v[192:195], a[88:91], a[204:207]
v_exp_f32_e64 v156, v156
v_mfma_f32_16x16x32_bf16 a[88:91], v[196:199], a[88:91], a[208:211]
s_add_u32 m0, s23, 107520
v_exp_f32_e64 v157, v157
v_mfma_f32_16x16x32_bf16 a[80:83], v[192:195], a[92:95], a[204:207]
s_add_u32 s26, s62, 101376
v_exp_f32_e64 v162, v162
v_mfma_f32_16x16x32_bf16 a[92:95], v[196:199], a[92:95], a[208:211]
buffer_load_dwordx4 v248, s[12:15], s26 offen offset:0 sc1 lds
v_mfma_f32_16x16x32_bf16 a[96:99], v[192:195], a[108:111], a[212:215]
v_exp_f32_e64 v163, v163
v_mfma_f32_16x16x32_bf16 a[108:111], v[196:199], a[108:111], a[216:219]
v_exp_f32_e64 v160, v160
v_mfma_f32_16x16x32_bf16 a[100:103], v[192:195], a[112:115], a[212:215]
v_exp_f32_e64 v161, v161
v_mfma_f32_16x16x32_bf16 a[112:115], v[196:199], a[112:115], a[216:219]
v_exp_f32_e64 v166, v166
v_mfma_f32_16x16x32_bf16 a[104:107], v[192:195], a[116:119], a[212:215]
v_exp_f32_e64 v167, v167
v_mfma_f32_16x16x32_bf16 a[116:119], v[196:199], a[116:119], a[216:219]
s_waitcnt lgkmcnt(0)
v_exp_f32 v164, v164
v_exp_f32 v165, v165
v_rcp_f32 v139, v139
v_pk_add_f32 v[158:159], v[158:159], 1.0 op_sel_hi:[1,0]
v_pk_add_f32 v[156:157], v[156:157], 1.0 op_sel_hi:[1,0]
v_pk_add_f32 v[162:163], v[162:163], 1.0 op_sel_hi:[1,0]
v_pk_add_f32 v[160:161], v[160:161], 1.0 op_sel_hi:[1,0]
v_pk_add_f32 v[166:167], v[166:167], 1.0 op_sel_hi:[1,0]
v_pk_add_f32 v[164:165], v[164:165], 1.0 op_sel_hi:[1,0]
v_pk_mul_f32 v[134:135], v[72:73], v[134:135]
v_pk_mul_f32 v[132:133], v[74:75], v[132:133]
v_pk_mul_f32 v[138:139], v[76:77], v[138:139]
v_pk_mul_f32 v[136:137], v[78:79], v[136:137]
v_pk_mul_f32 v[142:143], v[80:81], v[142:143]
v_pk_mul_f32 v[140:141], v[82:83], v[140:141]
v_pk_fma_f32 v[86:87], v[24:25], v[24:25], s[28:29]
v_pk_fma_f32 v[84:85], v[26:27], v[26:27], s[28:29]
v_pk_fma_f32 v[90:91], v[28:29], v[28:29], s[28:29]
v_pk_fma_f32 v[88:89], v[30:31], v[30:31], s[28:29]
v_pk_fma_f32 v[94:95], v[32:33], v[32:33], s[28:29]
v_pk_fma_f32 v[92:93], v[34:35], v[34:35], s[28:29]
v_pk_mul_f32 v[86:87], v[24:25], v[86:87] neg_lo:[1,0] neg_hi:[1,0]
v_pk_mul_f32 v[84:85], v[26:27], v[84:85] neg_lo:[1,0] neg_hi:[1,0]
v_pk_mul_f32 v[90:91], v[28:29], v[90:91] neg_lo:[1,0] neg_hi:[1,0]
v_pk_mul_f32 v[88:89], v[30:31], v[88:89] neg_lo:[1,0] neg_hi:[1,0]
v_pk_mul_f32 v[94:95], v[32:33], v[94:95] neg_lo:[1,0] neg_hi:[1,0]
v_pk_mul_f32 v[92:93], v[34:35], v[92:93] neg_lo:[1,0] neg_hi:[1,0]
v_mfma_f32_16x16x32_bf16 v[36:39], a[180:183], v[0:3], v[216:219]
ds_read_b128 a[204:207], v253 offset:640
v_cvt_pk_bf16_f32 v180, v134, v135
v_rcp_f32_e64 v157, v157
v_mfma_f32_16x16x32_bf16 v[36:39], a[184:187], v[4:7], v[36:39]
ds_read_b128 a[156:159], v239 offset:36864
v_cvt_pk_bf16_f32 v181, v132, v133
v_rcp_f32_e64 v162, v162
v_mfma_f32_16x16x32_bf16 v[36:39], a[188:191], v[8:11], v[36:39]
ds_read_b128 a[208:211], v253 offset:656
v_cvt_pk_bf16_f32 v184, v138, v139
v_rcp_f32_e64 v163, v163
v_mfma_f32_16x16x32_bf16 v[36:39], a[192:195], v[12:15], v[36:39]
ds_read_b128 a[160:163], v239 offset:37888
v_cvt_pk_bf16_f32 v185, v136, v137
v_rcp_f32_e64 v160, v160
v_mfma_f32_16x16x32_bf16 v[36:39], a[196:199], v[16:19], v[36:39]
buffer_load_dwordx4 v248, s[12:15], s26 offen offset:1024 sc1 lds
v_rcp_f32_e64 v161, v161
v_mfma_f32_16x16x32_bf16 v[36:39], a[200:203], v[20:23], v[36:39]
ds_read_b128 a[164:167], v239 offset:38912
v_cvt_pk_bf16_f32 v188, v142, v143
v_rcp_f32_e64 v166, v166
v_mfma_f32_16x16x32_bf16 v[40:43], a[200:203], v[68:71], v[216:219]
buffer_load_dwordx4 v248, s[12:15], s26 offen offset:2048 sc1 lds
v_rcp_f32 v167, v167
v_mfma_f32_16x16x32_bf16 v[40:43], a[196:199], v[64:67], v[40:43]
s_xor_b32 s63, s63, s36
ds_read_b128 a[168:171], v239 offset:39936
v_cvt_pk_bf16_f32 v189, v140, v141
v_rcp_f32_e64 v164, v164
v_mfma_f32_16x16x32_bf16 v[40:43], a[192:195], v[60:63], v[40:43]
s_add_u32 m0, s63, 0
v_exp_f32 v86, v86
v_rcp_f32_e64 v165, v165
v_mfma_f32_16x16x32_bf16 v[40:43], a[188:191], v[56:59], v[40:43]
s_add_u32 s26, s62, 122880
ds_read_b128 a[172:175], v239 offset:40960
v_exp_f32_e64 v93, v93
v_mfma_f32_16x16x32_bf16 v[40:43], a[184:187], v[52:55], v[40:43]
buffer_load_dwordx4 v248, s[12:15], s26 offen offset:0 sc1 lds
v_mfma_f32_16x16x32_bf16 v[40:43], a[180:183], v[48:51], v[40:43]
ds_read_b128 a[176:179], v239 offset:41984
v_rcp_f32_e64 v158, v158
v_mfma_f32_16x16x32_bf16 v[44:47], a[180:183], v[96:99], v[216:219]
v_exp_f32_e64 v87, v87
v_mfma_f32_16x16x32_bf16 v[44:47], a[184:187], v[100:103], v[44:47]
ds_read_b128 v[216:219], v241 offset:384
v_rcp_f32_e64 v159, v159
v_mfma_f32_16x16x32_bf16 v[44:47], a[188:191], v[104:107], v[44:47]
buffer_load_dwordx4 v248, s[12:15], s26 offen offset:1024 sc1 lds
v_mfma_f32_16x16x32_bf16 v[44:47], a[192:195], v[108:111], v[44:47]
v_exp_f32_e64 v84, v84
v_mfma_f32_16x16x32_bf16 v[44:47], a[196:199], v[112:115], v[44:47]
v_exp_f32_e64 v85, v85
v_mfma_f32_16x16x32_bf16 v[44:47], a[200:203], v[116:119], v[44:47]
s_waitcnt lgkmcnt(1)
v_exp_f32 v90, v90
v_mfma_f32_16x16x32_bf16 a[120:123], v[192:195], a[132:135], a[204:207]
buffer_load_dwordx4 v248, s[12:15], s26 offen offset:2048 sc1 lds
v_mfma_f32_16x16x32_bf16 a[132:135], v[196:199], a[132:135], a[208:211]
v_exp_f32_e64 v91, v91
v_mfma_f32_16x16x32_bf16 a[124:127], v[192:195], a[136:139], a[204:207]
v_exp_f32_e64 v88, v88
v_mfma_f32_16x16x32_bf16 a[136:139], v[196:199], a[136:139], a[208:211]
v_exp_f32_e64 v89, v89
v_mfma_f32_16x16x32_bf16 a[128:131], v[192:195], a[140:143], a[204:207]
v_exp_f32_e64 v94, v94
v_mfma_f32_16x16x32_bf16 a[140:143], v[196:199], a[140:143], a[208:211]
s_waitcnt vmcnt(15) lgkmcnt(0)
s_barrier
v_exp_f32 v95, v95
v_exp_f32 v92, v92
v_rcp_f32_e64 v156, v156
v_pk_add_f32 v[86:87], v[86:87], 1.0 op_sel_hi:[1,0]
v_pk_add_f32 v[84:85], v[84:85], 1.0 op_sel_hi:[1,0]
v_pk_add_f32 v[90:91], v[90:91], 1.0 op_sel_hi:[1,0]
v_pk_add_f32 v[88:89], v[88:89], 1.0 op_sel_hi:[1,0]
v_pk_add_f32 v[94:95], v[94:95], 1.0 op_sel_hi:[1,0]
v_pk_add_f32 v[92:93], v[92:93], 1.0 op_sel_hi:[1,0]
v_pk_mul_f32 v[158:159], v[144:145], v[158:159]
v_pk_mul_f32 v[156:157], v[146:147], v[156:157]
v_pk_mul_f32 v[162:163], v[148:149], v[162:163]
v_pk_mul_f32 v[160:161], v[150:151], v[160:161]
v_pk_mul_f32 v[166:167], v[152:153], v[166:167]
v_pk_mul_f32 v[164:165], v[154:155], v[164:165]
v_pk_fma_f32 v[122:123], v[36:37], v[36:37], s[28:29]
v_pk_fma_f32 v[120:121], v[38:39], v[38:39], s[28:29]
v_pk_fma_f32 v[126:127], v[40:41], v[40:41], s[28:29]
v_pk_fma_f32 v[124:125], v[42:43], v[42:43], s[28:29]
v_pk_fma_f32 v[130:131], v[44:45], v[44:45], s[28:29]
v_pk_fma_f32 v[128:129], v[46:47], v[46:47], s[28:29]
v_pk_mul_f32 v[122:123], v[36:37], v[122:123] neg_lo:[1,0] neg_hi:[1,0]
v_pk_mul_f32 v[120:121], v[38:39], v[120:121] neg_lo:[1,0] neg_hi:[1,0]
v_pk_mul_f32 v[126:127], v[40:41], v[126:127] neg_lo:[1,0] neg_hi:[1,0]
v_pk_mul_f32 v[124:125], v[42:43], v[124:125] neg_lo:[1,0] neg_hi:[1,0]
v_pk_mul_f32 v[130:131], v[44:45], v[130:131] neg_lo:[1,0] neg_hi:[1,0]
v_pk_mul_f32 v[128:129], v[46:47], v[128:129] neg_lo:[1,0] neg_hi:[1,0]
v_mfma_f32_16x16x32_bf16 v[72:75], a[156:159], v[0:3], v[216:219]
s_add_u32 m0, s63, 3072
ds_read_b128 a[180:183], v238 offset:0
v_cvt_pk_bf16_f32 v182, v158, v159
v_mfma_f32_16x16x32_bf16 v[72:75], a[160:163], v[4:7], v[72:75]
s_add_u32 s26, s62, 125952
v_exp_f32_e64 v122, v122
v_mfma_f32_16x16x32_bf16 v[72:75], a[164:167], v[8:11], v[72:75]
buffer_load_dwordx4 v248, s[12:15], s26 offen offset:0 sc1 lds
v_mfma_f32_16x16x32_bf16 v[72:75], a[168:171], v[12:15], v[72:75]
ds_read_b128 a[184:187], v238 offset:1024
v_cvt_pk_bf16_f32 v183, v156, v157
v_mfma_f32_16x16x32_bf16 v[72:75], a[172:175], v[16:19], v[72:75]
v_exp_f32_e64 v123, v123
v_mfma_f32_16x16x32_bf16 v[72:75], a[176:179], v[20:23], v[72:75]
v_exp_f32_e64 v120, v120
v_mfma_f32_16x16x32_bf16 v[76:79], a[176:179], v[68:71], v[216:219]
ds_read_b128 a[188:191], v238 offset:2048
v_cvt_pk_bf16_f32 v186, v162, v163
v_mfma_f32_16x16x32_bf16 v[76:79], a[172:175], v[64:67], v[76:79]
v_exp_f32_e64 v121, v121
v_mfma_f32_16x16x32_bf16 v[76:79], a[168:171], v[60:63], v[76:79]
v_exp_f32_e64 v126, v126
v_mfma_f32_16x16x32_bf16 v[76:79], a[164:167], v[56:59], v[76:79]
ds_read_b128 a[192:195], v238 offset:3072
v_cvt_pk_bf16_f32 v187, v160, v161
v_mfma_f32_16x16x32_bf16 v[76:79], a[160:163], v[52:55], v[76:79]
v_exp_f32_e64 v127, v127
v_mfma_f32_16x16x32_bf16 v[76:79], a[156:159], v[48:51], v[76:79]
v_exp_f32_e64 v124, v124
v_mfma_f32_16x16x32_bf16 v[80:83], a[156:159], v[96:99], v[216:219]
ds_read_b128 a[196:199], v238 offset:4096
v_cvt_pk_bf16_f32 v190, v166, v167
v_mfma_f32_16x16x32_bf16 v[80:83], a[160:163], v[100:103], v[80:83]
buffer_load_dwordx4 v248, s[12:15], s26 offen offset:1024 sc1 lds
v_mfma_f32_16x16x32_bf16 v[80:83], a[164:167], v[104:107], v[80:83]
v_exp_f32_e64 v125, v125
v_mfma_f32_16x16x32_bf16 v[80:83], a[168:171], v[108:111], v[80:83]
ds_read_b128 a[200:203], v238 offset:5120
v_cvt_pk_bf16_f32 v191, v164, v165
v_mfma_f32_16x16x32_bf16 v[80:83], a[172:175], v[112:115], v[80:83]
v_exp_f32_e64 v130, v130
v_mfma_f32_16x16x32_bf16 v[80:83], a[176:179], v[116:119], v[80:83]
s_waitcnt lgkmcnt(1)
v_exp_f32 v131, v131
v_mfma_f32_16x16x32_bf16 a[0:3], a[180:183], v[168:171], a[0:3]
ds_read_b128 a[156:159], v239 offset:43008
ds_read_b128 a[160:163], v239 offset:44032
v_mfma_f32_16x16x32_bf16 a[0:3], a[184:187], v[180:183], a[0:3]
v_exp_f32_e64 v128, v128
v_mfma_f32_16x16x32_bf16 a[4:7], a[184:187], v[184:187], a[4:7]
v_exp_f32_e64 v129, v129
v_mfma_f32_16x16x32_bf16 a[4:7], a[180:183], v[172:175], a[4:7]
ds_read_b128 a[164:167], v239 offset:45056
ds_read_b128 a[168:171], v239 offset:46080
v_mfma_f32_16x16x32_bf16 a[8:11], a[180:183], v[176:179], a[8:11]
v_rcp_f32_e64 v86, v86
v_mfma_f32_16x16x32_bf16 a[8:11], a[184:187], v[188:191], a[8:11]
buffer_load_dwordx4 v248, s[12:15], s26 offen offset:2048 sc1 lds
v_mfma_f32_16x16x32_bf16 a[20:23], a[192:195], v[188:191], a[20:23]
ds_read_b128 a[172:175], v239 offset:47104
ds_read_b128 a[176:179], v239 offset:48128
v_mfma_f32_16x16x32_bf16 a[20:23], a[188:191], v[176:179], a[20:23]
v_rcp_f32_e64 v87, v87
v_mfma_f32_16x16x32_bf16 a[16:19], a[188:191], v[172:175], a[16:19]
ds_read_b128 v[216:219], v241 offset:448
v_mfma_f32_16x16x32_bf16 a[16:19], a[192:195], v[184:187], a[16:19]
v_rcp_f32_e64 v84, v84
v_mfma_f32_16x16x32_bf16 a[12:15], a[192:195], v[180:183], a[12:15]
v_rcp_f32_e64 v85, v85
v_mfma_f32_16x16x32_bf16 a[12:15], a[188:191], v[168:171], a[12:15]
v_rcp_f32_e64 v90, v90
v_mfma_f32_16x16x32_bf16 a[24:27], a[196:199], v[168:171], a[24:27]
s_waitcnt lgkmcnt(1)
v_rcp_f32 v91, v91
v_mfma_f32_16x16x32_bf16 a[24:27], a[200:203], v[180:183], a[24:27]
v_rcp_f32_e64 v88, v88
v_mfma_f32_16x16x32_bf16 a[28:31], a[200:203], v[184:187], a[28:31]
v_rcp_f32_e64 v89, v89
v_mfma_f32_16x16x32_bf16 a[28:31], a[196:199], v[172:175], a[28:31]
v_rcp_f32_e64 v94, v94
v_mfma_f32_16x16x32_bf16 a[32:35], a[196:199], v[176:179], a[32:35]
v_rcp_f32 v95, v95
v_mfma_f32_16x16x32_bf16 a[32:35], a[200:203], v[188:191], a[32:35]
s_waitcnt vmcnt(6) lgkmcnt(0)
ds_write_b8 v251, v250 offset:0
v_rcp_f32 v92, v92
v_rcp_f32 v93, v93
v_pk_add_f32 v[122:123], v[122:123], 1.0 op_sel_hi:[1,0]
v_pk_add_f32 v[120:121], v[120:121], 1.0 op_sel_hi:[1,0]
v_pk_add_f32 v[126:127], v[126:127], 1.0 op_sel_hi:[1,0]
v_pk_add_f32 v[124:125], v[124:125], 1.0 op_sel_hi:[1,0]
v_pk_add_f32 v[130:131], v[130:131], 1.0 op_sel_hi:[1,0]
v_pk_add_f32 v[128:129], v[128:129], 1.0 op_sel_hi:[1,0]
v_pk_mul_f32 v[86:87], v[24:25], v[86:87]
v_pk_mul_f32 v[84:85], v[26:27], v[84:85]
v_pk_mul_f32 v[90:91], v[28:29], v[90:91]
v_pk_mul_f32 v[88:89], v[30:31], v[88:89]
v_pk_mul_f32 v[94:95], v[32:33], v[94:95]
v_pk_mul_f32 v[92:93], v[34:35], v[92:93]
v_pk_fma_f32 v[134:135], v[72:73], v[72:73], s[28:29]
v_pk_fma_f32 v[132:133], v[74:75], v[74:75], s[28:29]
v_pk_fma_f32 v[138:139], v[76:77], v[76:77], s[28:29]
v_pk_fma_f32 v[136:137], v[78:79], v[78:79], s[28:29]
v_pk_fma_f32 v[142:143], v[80:81], v[80:81], s[28:29]
v_pk_fma_f32 v[140:141], v[82:83], v[82:83], s[28:29]
v_pk_mul_f32 v[134:135], v[72:73], v[134:135] neg_lo:[1,0] neg_hi:[1,0]
v_pk_mul_f32 v[132:133], v[74:75], v[132:133] neg_lo:[1,0] neg_hi:[1,0]
v_pk_mul_f32 v[138:139], v[76:77], v[138:139] neg_lo:[1,0] neg_hi:[1,0]
v_pk_mul_f32 v[136:137], v[78:79], v[136:137] neg_lo:[1,0] neg_hi:[1,0]
v_pk_mul_f32 v[142:143], v[80:81], v[142:143] neg_lo:[1,0] neg_hi:[1,0]
v_pk_mul_f32 v[140:141], v[82:83], v[140:141] neg_lo:[1,0] neg_hi:[1,0]
.p2align 6
L_loop_p:
v_mfma_f32_16x16x32_bf16 v[144:147], a[156:159], v[0:3], v[216:219]
ds_read_b128 a[180:183], v238 offset:6144
v_cvt_pk_bf16_f32 v192, v86, v87
v_mfma_f32_16x16x32_bf16 v[144:147], a[160:163], v[4:7], v[144:147]
v_exp_f32_e64 v134, v134
v_mfma_f32_16x16x32_bf16 v[144:147], a[164:167], v[8:11], v[144:147]
v_exp_f32_e64 v135, v135
v_mfma_f32_16x16x32_bf16 v[144:147], a[168:171], v[12:15], v[144:147]
ds_read_b128 a[184:187], v238 offset:7168
v_cvt_pk_bf16_f32 v193, v84, v85
v_mfma_f32_16x16x32_bf16 v[144:147], a[172:175], v[16:19], v[144:147]
v_exp_f32_e64 v132, v132
v_mfma_f32_16x16x32_bf16 v[144:147], a[176:179], v[20:23], v[144:147]
v_exp_f32_e64 v133, v133
v_mfma_f32_16x16x32_bf16 v[148:151], a[176:179], v[68:71], v[216:219]
ds_read_b128 a[188:191], v238 offset:8192
v_cvt_pk_bf16_f32 v196, v90, v91
v_mfma_f32_16x16x32_bf16 v[148:151], a[172:175], v[64:67], v[148:151]
v_exp_f32_e64 v138, v138
v_mfma_f32_16x16x32_bf16 v[148:151], a[168:171], v[60:63], v[148:151]
ds_read_b32 v255, v252 offset:0
v_cvt_pk_bf16_f32 v197, v88, v89
v_mfma_f32_16x16x32_bf16 v[148:151], a[164:167], v[56:59], v[148:151]
ds_read_b128 a[192:195], v238 offset:9216
v_cvt_pk_bf16_f32 v200, v94, v95
v_mfma_f32_16x16x32_bf16 v[148:151], a[160:163], v[52:55], v[148:151]
v_exp_f32_e64 v139, v139
v_mfma_f32_16x16x32_bf16 v[148:151], a[156:159], v[48:51], v[148:151]
v_exp_f32_e64 v136, v136
v_mfma_f32_16x16x32_bf16 v[152:155], a[156:159], v[96:99], v[216:219]
ds_read_b128 a[196:199], v238 offset:10240
v_cvt_pk_bf16_f32 v201, v92, v93
v_mfma_f32_16x16x32_bf16 v[152:155], a[160:163], v[100:103], v[152:155]
v_exp_f32_e64 v137, v137
v_mfma_f32_16x16x32_bf16 v[152:155], a[164:167], v[104:107], v[152:155]
v_exp_f32_e64 v142, v142
v_mfma_f32_16x16x32_bf16 v[152:155], a[168:171], v[108:111], v[152:155]
s_waitcnt lgkmcnt(1)
ds_read_b128 a[200:203], v238 offset:11264
v_cmp_ne_u32 vcc, s33, v255
v_mfma_f32_16x16x32_bf16 v[152:155], a[172:175], v[112:115], v[152:155]
v_exp_f32_e64 v143, v143
v_mfma_f32_16x16x32_bf16 v[152:155], a[176:179], v[116:119], v[152:155]
s_cbranch_vccnz L_sy_p7
L_syb_p7:
v_exp_f32 v140, v140
v_mfma_f32_16x16x32_bf16 a[36:39], a[180:183], v[168:171], a[36:39]
ds_read_b128 a[156:159], v239 offset:0
ds_read_b128 a[160:163], v239 offset:1024
v_mfma_f32_16x16x32_bf16 a[36:39], a[184:187], v[180:183], a[36:39]
v_exp_f32_e64 v141, v141
v_mfma_f32_16x16x32_bf16 a[40:43], a[184:187], v[184:187], a[40:43]
v_rcp_f32_e64 v122, v122
v_mfma_f32_16x16x32_bf16 a[40:43], a[180:183], v[172:175], a[40:43]
ds_read_b128 a[164:167], v239 offset:2048
ds_read_b128 a[168:171], v239 offset:3072
v_mfma_f32_16x16x32_bf16 a[44:47], a[180:183], v[176:179], a[44:47]
v_rcp_f32_e64 v123, v123
v_mfma_f32_16x16x32_bf16 a[44:47], a[184:187], v[188:191], a[44:47]
v_rcp_f32_e64 v120, v120
v_mfma_f32_16x16x32_bf16 a[56:59], a[192:195], v[188:191], a[56:59]
ds_read_b128 a[172:175], v239 offset:4096
ds_read_b128 a[176:179], v239 offset:5120
v_mfma_f32_16x16x32_bf16 a[56:59], a[188:191], v[176:179], a[56:59]
s_add_u32 m0, s23, 129024
v_rcp_f32_e64 v121, v121
v_mfma_f32_16x16x32_bf16 a[52:55], a[188:191], v[172:175], a[52:55]
s_add_u32 s26, s62, 147456
ds_read_b128 v[216:219], v241 offset:512
v_rcp_f32_e64 v129, v129
v_mfma_f32_16x16x32_bf16 a[52:55], a[192:195], v[184:187], a[52:55]
buffer_load_dwordx4 v248, s[12:15], s26 offen offset:0 sc1 lds
v_mfma_f32_16x16x32_bf16 a[48:51], a[192:195], v[180:183], a[48:51]
v_rcp_f32_e64 v126, v126
v_mfma_f32_16x16x32_bf16 a[48:51], a[188:191], v[168:171], a[48:51]
s_waitcnt lgkmcnt(1)
v_rcp_f32 v127, v127
v_mfma_f32_16x16x32_bf16 a[60:63], a[196:199], v[168:171], a[60:63]
buffer_load_dwordx4 v248, s[12:15], s26 offen offset:1024 sc1 lds
v_mfma_f32_16x16x32_bf16 a[60:63], a[200:203], v[180:183], a[60:63]
v_rcp_f32_e64 v124, v124
v_mfma_f32_16x16x32_bf16 a[64:67], a[200:203], v[184:187], a[64:67]
v_rcp_f32_e64 v125, v125
v_mfma_f32_16x16x32_bf16 a[64:67], a[196:199], v[172:175], a[64:67]
buffer_load_dwordx4 v248, s[12:15], s26 offen offset:2048 sc1 lds
v_mfma_f32_16x16x32_bf16 a[68:71], a[196:199], v[176:179], a[68:71]
v_rcp_f32_e64 v130, v130
v_mfma_f32_16x16x32_bf16 a[68:71], a[200:203], v[188:191], a[68:71]
s_waitcnt lgkmcnt(0)
v_rcp_f32 v131, v131
v_rcp_f32 v128, v128
v_add_f32 v134, 1.0, v134
v_add_f32_e64 v135, 1.0, v135
v_pk_add_f32 v[132:133], v[132:133], 1.0 op_sel_hi:[1,0]
v_pk_add_f32 v[138:139], v[138:139], 1.0 op_sel_hi:[1,0]
v_pk_add_f32 v[136:137], v[136:137], 1.0 op_sel_hi:[1,0]
v_pk_add_f32 v[142:143], v[142:143], 1.0 op_sel_hi:[1,0]
v_pk_add_f32 v[140:141], v[140:141], 1.0 op_sel_hi:[1,0]
v_pk_mul_f32 v[122:123], v[36:37], v[122:123]
v_pk_mul_f32 v[120:121], v[38:39], v[120:121]
v_pk_mul_f32 v[126:127], v[40:41], v[126:127]
v_pk_mul_f32 v[124:125], v[42:43], v[124:125]
v_pk_mul_f32 v[130:131], v[44:45], v[130:131]
v_pk_mul_f32 v[128:129], v[46:47], v[128:129]
v_pk_fma_f32 v[158:159], v[144:145], v[144:145], s[28:29]
v_pk_fma_f32 v[156:157], v[146:147], v[146:147], s[28:29]
v_pk_fma_f32 v[162:163], v[148:149], v[148:149], s[28:29]
v_pk_fma_f32 v[160:161], v[150:151], v[150:151], s[28:29]
v_pk_fma_f32 v[166:167], v[152:153], v[152:153], s[28:29]
v_pk_fma_f32 v[164:165], v[154:155], v[154:155], s[28:29]
v_pk_mul_f32 v[158:159], v[144:145], v[158:159] neg_lo:[1,0] neg_hi:[1,0]
v_pk_mul_f32 v[156:157], v[146:147], v[156:157] neg_lo:[1,0] neg_hi:[1,0]
v_pk_mul_f32 v[162:163], v[148:149], v[162:163] neg_lo:[1,0] neg_hi:[1,0]
v_pk_mul_f32 v[160:161], v[150:151], v[160:161] neg_lo:[1,0] neg_hi:[1,0]
v_pk_mul_f32 v[166:167], v[152:153], v[166:167] neg_lo:[1,0] neg_hi:[1,0]
v_pk_mul_f32 v[164:165], v[154:155], v[164:165] neg_lo:[1,0] neg_hi:[1,0]
v_mfma_f32_16x16x32_bf16 v[24:27], a[156:159], v[0:3], v[216:219]
ds_read_b128 a[180:183], v238 offset:12288
v_add_u32_e64 v250, 1, v250
v_mfma_f32_16x16x32_bf16 v[24:27], a[160:163], v[4:7], v[24:27]
s_add_u32 s33, s33, 0x01010101
v_cvt_pk_bf16_f32 v203, v128, v129
v_rcp_f32_e64 v141, v141
v_mfma_f32_16x16x32_bf16 v[24:27], a[164:167], v[8:11], v[24:27]
v_exp_f32_e64 v158, v158
v_mfma_f32_16x16x32_bf16 v[24:27], a[168:171], v[12:15], v[24:27]
ds_read_b128 a[184:187], v238 offset:13312
v_cvt_pk_bf16_f32 v194, v122, v123
v_mfma_f32_16x16x32_bf16 v[24:27], a[172:175], v[16:19], v[24:27]
v_exp_f32_e64 v159, v159
v_mfma_f32_16x16x32_bf16 v[24:27], a[176:179], v[20:23], v[24:27]
v_exp_f32_e64 v156, v156
v_mfma_f32_16x16x32_bf16 v[28:31], a[176:179], v[68:71], v[216:219]
ds_read_b128 a[188:191], v238 offset:14336
v_cvt_pk_bf16_f32 v195, v120, v121
v_mfma_f32_16x16x32_bf16 v[28:31], a[172:175], v[64:67], v[28:31]
v_exp_f32_e64 v157, v157
v_mfma_f32_16x16x32_bf16 v[28:31], a[168:171], v[60:63], v[28:31]
v_exp_f32_e64 v162, v162
v_mfma_f32_16x16x32_bf16 v[28:31], a[164:167], v[56:59], v[28:31]
ds_read_b128 a[192:195], v238 offset:15360
v_cvt_pk_bf16_f32 v198, v126, v127
v_mfma_f32_16x16x32_bf16 v[28:31], a[160:163], v[52:55], v[28:31]
v_exp_f32_e64 v163, v163
v_mfma_f32_16x16x32_bf16 v[28:31], a[156:159], v[48:51], v[28:31]
v_exp_f32_e64 v160, v160
v_mfma_f32_16x16x32_bf16 v[32:35], a[156:159], v[96:99], v[216:219]
ds_read_b128 a[196:199], v238 offset:16384
v_cvt_pk_bf16_f32 v199, v124, v125
v_mfma_f32_16x16x32_bf16 v[32:35], a[160:163], v[100:103], v[32:35]
v_exp_f32_e64 v161, v161
v_mfma_f32_16x16x32_bf16 v[32:35], a[164:167], v[104:107], v[32:35]
v_exp_f32_e64 v166, v166
v_mfma_f32_16x16x32_bf16 v[32:35], a[168:171], v[108:111], v[32:35]
ds_read_b128 a[200:203], v238 offset:17408
v_cvt_pk_bf16_f32 v202, v130, v131
v_mfma_f32_16x16x32_bf16 v[32:35], a[172:175], v[112:115], v[32:35]
v_exp_f32_e64 v167, v167
v_mfma_f32_16x16x32_bf16 v[32:35], a[176:179], v[116:119], v[32:35]
s_waitcnt lgkmcnt(1)
v_exp_f32 v164, v164
v_mfma_f32_16x16x32_bf16 a[72:75], a[180:183], v[168:171], a[72:75]
ds_read_b128 a[156:159], v239 offset:6144
ds_read_b128 a[160:163], v239 offset:7168
v_mfma_f32_16x16x32_bf16 a[72:75], a[184:187], v[180:183], a[72:75]
v_exp_f32_e64 v165, v165
v_mfma_f32_16x16x32_bf16 a[76:79], a[184:187], v[184:187], a[76:79]
v_rcp_f32_e64 v134, v134
v_mfma_f32_16x16x32_bf16 a[76:79], a[180:183], v[172:175], a[76:79]
ds_read_b128 a[164:167], v239 offset:8192
ds_read_b128 a[168:171], v239 offset:9216
v_mfma_f32_16x16x32_bf16 a[80:83], a[180:183], v[176:179], a[80:83]
v_rcp_f32_e64 v135, v135
v_mfma_f32_16x16x32_bf16 a[80:83], a[184:187], v[188:191], a[80:83]
v_rcp_f32_e64 v132, v132
v_mfma_f32_16x16x32_bf16 a[92:95], a[192:195], v[188:191], a[92:95]
ds_read_b128 a[172:175], v239 offset:10240
ds_read_b128 a[176:179], v239 offset:11264
v_mfma_f32_16x16x32_bf16 a[92:95], a[188:191], v[176:179], a[92:95]
s_add_u32 m0, s23, 132096
v_rcp_f32_e64 v133, v133
v_mfma_f32_16x16x32_bf16 a[88:91], a[188:191], v[172:175], a[88:91]
s_add_u32 s26, s62, 150528
ds_read_b128 v[216:219], v241 offset:576
v_add_f32_e64 v158, 1.0, v158
v_mfma_f32_16x16x32_bf16 a[88:91], a[192:195], v[184:187], a[88:91]
buffer_load_dwordx4 v248, s[12:15], s26 offen offset:0 sc1 lds
v_mfma_f32_16x16x32_bf16 a[84:87], a[192:195], v[180:183], a[84:87]
v_rcp_f32_e64 v138, v138
v_mfma_f32_16x16x32_bf16 a[84:87], a[188:191], v[168:171], a[84:87]
v_rcp_f32 v139, v139
v_mfma_f32_16x16x32_bf16 a[96:99], a[196:199], v[168:171], a[96:99]
s_waitcnt lgkmcnt(1)
buffer_load_dwordx4 v248, s[12:15], s26 offen offset:1024 sc1 lds
v_mfma_f32_16x16x32_bf16 a[96:99], a[200:203], v[180:183], a[96:99]
v_rcp_f32_e64 v136, v136
v_mfma_f32_16x16x32_bf16 a[100:103], a[200:203], v[184:187], a[100:103]
v_rcp_f32_e64 v137, v137
v_mfma_f32_16x16x32_bf16 a[100:103], a[196:199], v[172:175], a[100:103]
buffer_load_dwordx4 v248, s[12:15], s26 offen offset:2048 sc1 lds
v_mfma_f32_16x16x32_bf16 a[104:107], a[196:199], v[176:179], a[104:107]
v_rcp_f32_e64 v142, v142
v_mfma_f32_16x16x32_bf16 a[104:107], a[200:203], v[188:191], a[104:107]
s_waitcnt lgkmcnt(0)
v_rcp_f32 v143, v143
v_rcp_f32 v140, v140
v_add_f32 v159, 1.0, v159
v_pk_add_f32 v[156:157], v[156:157], 1.0 op_sel_hi:[1,0]
v_pk_add_f32 v[162:163], v[162:163], 1.0 op_sel_hi:[1,0]
v_pk_add_f32 v[160:161], v[160:161], 1.0 op_sel_hi:[1,0]
v_pk_add_f32 v[166:167], v[166:167], 1.0 op_sel_hi:[1,0]
v_pk_add_f32 v[164:165], v[164:165], 1.0 op_sel_hi:[1,0]
v_pk_mul_f32 v[134:135], v[72:73], v[134:135]
v_pk_mul_f32 v[132:133], v[74:75], v[132:133]
v_pk_mul_f32 v[138:139], v[76:77], v[138:139]
v_pk_mul_f32 v[136:137], v[78:79], v[136:137]
v_pk_mul_f32 v[142:143], v[80:81], v[142:143]
v_pk_mul_f32 v[140:141], v[82:83], v[140:141]
v_pk_fma_f32 v[86:87], v[24:25], v[24:25], s[28:29]
v_pk_fma_f32 v[84:85], v[26:27], v[26:27], s[28:29]
v_pk_fma_f32 v[90:91], v[28:29], v[28:29], s[28:29]
v_pk_fma_f32 v[88:89], v[30:31], v[30:31], s[28:29]
v_pk_fma_f32 v[94:95], v[32:33], v[32:33], s[28:29]
v_pk_fma_f32 v[92:93], v[34:35], v[34:35], s[28:29]
v_pk_mul_f32 v[86:87], v[24:25], v[86:87] neg_lo:[1,0] neg_hi:[1,0]
v_pk_mul_f32 v[84:85], v[26:27], v[84:85] neg_lo:[1,0] neg_hi:[1,0]
v_pk_mul_f32 v[90:91], v[28:29], v[90:91] neg_lo:[1,0] neg_hi:[1,0]
v_pk_mul_f32 v[88:89], v[30:31], v[88:89] neg_lo:[1,0] neg_hi:[1,0]
v_pk_mul_f32 v[94:95], v[32:33], v[94:95] neg_lo:[1,0] neg_hi:[1,0]
v_pk_mul_f32 v[92:93], v[34:35], v[92:93] neg_lo:[1,0] neg_hi:[1,0]
v_mfma_f32_16x16x32_bf16 v[36:39], a[156:159], v[0:3], v[216:219]
ds_read_b128 a[180:183], v238 offset:18432
v_cvt_pk_bf16_f32 v204, v134, v135
v_mfma_f32_16x16x32_bf16 v[36:39], a[160:163], v[4:7], v[36:39]
v_exp_f32_e64 v86, v86
v_mfma_f32_16x16x32_bf16 v[36:39], a[164:167], v[8:11], v[36:39]
v_exp_f32_e64 v87, v87
v_mfma_f32_16x16x32_bf16 v[36:39], a[168:171], v[12:15], v[36:39]
ds_read_b128 a[184:187], v238 offset:19456
v_cvt_pk_bf16_f32 v205, v132, v133
v_mfma_f32_16x16x32_bf16 v[36:39], a[172:175], v[16:19], v[36:39]
v_exp_f32_e64 v84, v84
v_mfma_f32_16x16x32_bf16 v[36:39], a[176:179], v[20:23], v[36:39]
v_exp_f32_e64 v85, v85
v_mfma_f32_16x16x32_bf16 v[40:43], a[176:179], v[68:71], v[216:219]
ds_read_b128 a[188:191], v238 offset:20480
v_cvt_pk_bf16_f32 v208, v138, v139
v_mfma_f32_16x16x32_bf16 v[40:43], a[172:175], v[64:67], v[40:43]
v_exp_f32_e64 v90, v90
v_mfma_f32_16x16x32_bf16 v[40:43], a[168:171], v[60:63], v[40:43]
v_exp_f32_e64 v91, v91
v_mfma_f32_16x16x32_bf16 v[40:43], a[164:167], v[56:59], v[40:43]
ds_read_b128 a[192:195], v238 offset:21504
v_cvt_pk_bf16_f32 v209, v136, v137
v_mfma_f32_16x16x32_bf16 v[40:43], a[160:163], v[52:55], v[40:43]
v_exp_f32_e64 v88, v88
v_mfma_f32_16x16x32_bf16 v[40:43], a[156:159], v[48:51], v[40:43]
v_exp_f32_e64 v89, v89
v_mfma_f32_16x16x32_bf16 v[44:47], a[156:159], v[96:99], v[216:219]
ds_read_b128 a[196:199], v238 offset:22528
v_cvt_pk_bf16_f32 v212, v142, v143
v_mfma_f32_16x16x32_bf16 v[44:47], a[160:163], v[100:103], v[44:47]
v_exp_f32_e64 v94, v94
v_mfma_f32_16x16x32_bf16 v[44:47], a[164:167], v[104:107], v[44:47]
v_exp_f32_e64 v95, v95
v_mfma_f32_16x16x32_bf16 v[44:47], a[168:171], v[108:111], v[44:47]
ds_read_b128 a[200:203], v238 offset:23552
v_cvt_pk_bf16_f32 v213, v140, v141
v_mfma_f32_16x16x32_bf16 v[44:47], a[172:175], v[112:115], v[44:47]
v_exp_f32_e64 v92, v92
v_mfma_f32_16x16x32_bf16 v[44:47], a[176:179], v[116:119], v[44:47]
s_waitcnt lgkmcnt(1)
v_exp_f32 v93, v93
v_mfma_f32_16x16x32_bf16 a[108:111], a[180:183], v[168:171], a[108:111]
ds_read_b128 a[156:159], v239 offset:12288
ds_read_b128 a[160:163], v239 offset:13312
v_mfma_f32_16x16x32_bf16 a[108:111], a[184:187], v[180:183], a[108:111]
v_rcp_f32_e64 v158, v158
v_mfma_f32_16x16x32_bf16 a[112:115], a[184:187], v[184:187], a[112:115]
v_rcp_f32_e64 v159, v159
v_mfma_f32_16x16x32_bf16 a[112:115], a[180:183], v[172:175], a[112:115]
ds_read_b128 a[164:167], v239 offset:14336
ds_read_b128 a[168:171], v239 offset:15360
v_mfma_f32_16x16x32_bf16 a[116:119], a[180:183], v[176:179], a[116:119]
v_rcp_f32_e64 v156, v156
v_mfma_f32_16x16x32_bf16 a[116:119], a[184:187], v[188:191], a[116:119]
v_rcp_f32_e64 v157, v157
v_mfma_f32_16x16x32_bf16 a[128:131], a[192:195], v[188:191], a[128:131]
ds_read_b128 a[172:175], v239 offset:16384
ds_read_b128 a[176:179], v239 offset:17408
v_mfma_f32_16x16x32_bf16 a[128:131], a[188:191], v[176:179], a[128:131]
s_add_u32 m0, s63, 24576
v_rcp_f32_e64 v162, v162
v_mfma_f32_16x16x32_bf16 a[124:127], a[188:191], v[172:175], a[124:127]
s_add_u32 s26, s62, 172032
ds_read_b128 v[216:219], v241 offset:640
v_add_f32_e64 v86, 1.0, v86
v_mfma_f32_16x16x32_bf16 a[124:127], a[192:195], v[184:187], a[124:127]
buffer_load_dwordx4 v248, s[12:15], s26 offen offset:0 sc1 lds
v_mfma_f32_16x16x32_bf16 a[120:123], a[192:195], v[180:183], a[120:123]
v_rcp_f32_e64 v163, v163
v_mfma_f32_16x16x32_bf16 a[120:123], a[188:191], v[168:171], a[120:123]
v_rcp_f32 v160, v160
v_mfma_f32_16x16x32_bf16 a[132:135], a[196:199], v[168:171], a[132:135]
s_waitcnt lgkmcnt(1)
buffer_load_dwordx4 v248, s[12:15], s26 offen offset:1024 sc1 lds
v_mfma_f32_16x16x32_bf16 a[132:135], a[200:203], v[180:183], a[132:135]
v_rcp_f32_e64 v161, v161
v_mfma_f32_16x16x32_bf16 a[136:139], a[200:203], v[184:187], a[136:139]
v_rcp_f32_e64 v166, v166
v_mfma_f32_16x16x32_bf16 a[136:139], a[196:199], v[172:175], a[136:139]
buffer_load_dwordx4 v248, s[12:15], s26 offen offset:2048 sc1 lds
v_mfma_f32_16x16x32_bf16 a[140:143], a[196:199], v[176:179], a[140:143]
v_rcp_f32_e64 v167, v167
v_mfma_f32_16x16x32_bf16 a[140:143], a[200:203], v[188:191], a[140:143]
s_waitcnt lgkmcnt(0)
v_rcp_f32 v164, v164
v_rcp_f32 v165, v165
v_add_f32 v87, 1.0, v87
v_pk_add_f32 v[84:85], v[84:85], 1.0 op_sel_hi:[1,0]
v_pk_add_f32 v[90:91], v[90:91], 1.0 op_sel_hi:[1,0]
v_pk_add_f32 v[88:89], v[88:89], 1.0 op_sel_hi:[1,0]
v_pk_add_f32 v[94:95], v[94:95], 1.0 op_sel_hi:[1,0]
v_pk_add_f32 v[92:93], v[92:93], 1.0 op_sel_hi:[1,0]
v_pk_mul_f32 v[158:159], v[144:145], v[158:159]
v_pk_mul_f32 v[156:157], v[146:147], v[156:157]
v_pk_mul_f32 v[162:163], v[148:149], v[162:163]
v_pk_mul_f32 v[160:161], v[150:151], v[160:161]
v_pk_mul_f32 v[166:167], v[152:153], v[166:167]
v_pk_mul_f32 v[164:165], v[154:155], v[164:165]
v_pk_fma_f32 v[122:123], v[36:37], v[36:37], s[28:29]
v_pk_fma_f32 v[120:121], v[38:39], v[38:39], s[28:29]
v_pk_fma_f32 v[126:127], v[40:41], v[40:41], s[28:29]
v_pk_fma_f32 v[124:125], v[42:43], v[42:43], s[28:29]
v_pk_fma_f32 v[130:131], v[44:45], v[44:45], s[28:29]
v_pk_fma_f32 v[128:129], v[46:47], v[46:47], s[28:29]
v_pk_mul_f32 v[122:123], v[36:37], v[122:123] neg_lo:[1,0] neg_hi:[1,0]
v_pk_mul_f32 v[120:121], v[38:39], v[120:121] neg_lo:[1,0] neg_hi:[1,0]
v_pk_mul_f32 v[126:127], v[40:41], v[126:127] neg_lo:[1,0] neg_hi:[1,0]
v_pk_mul_f32 v[124:125], v[42:43], v[124:125] neg_lo:[1,0] neg_hi:[1,0]
v_pk_mul_f32 v[130:131], v[44:45], v[130:131] neg_lo:[1,0] neg_hi:[1,0]
v_pk_mul_f32 v[128:129], v[46:47], v[128:129] neg_lo:[1,0] neg_hi:[1,0]
v_mfma_f32_16x16x32_bf16 v[72:75], a[156:159], v[0:3], v[216:219]
ds_read_b128 a[180:183], v238 offset:24576
v_cvt_pk_bf16_f32 v206, v158, v159
v_mfma_f32_16x16x32_bf16 v[72:75], a[160:163], v[4:7], v[72:75]
v_exp_f32_e64 v122, v122
v_mfma_f32_16x16x32_bf16 v[72:75], a[164:167], v[8:11], v[72:75]
v_exp_f32_e64 v123, v123
v_mfma_f32_16x16x32_bf16 v[72:75], a[168:171], v[12:15], v[72:75]
ds_read_b128 a[184:187], v238 offset:25600
v_cvt_pk_bf16_f32 v207, v156, v157
v_mfma_f32_16x16x32_bf16 v[72:75], a[172:175], v[16:19], v[72:75]
v_exp_f32_e64 v120, v120
v_mfma_f32_16x16x32_bf16 v[72:75], a[176:179], v[20:23], v[72:75]
v_exp_f32_e64 v121, v121
v_mfma_f32_16x16x32_bf16 v[76:79], a[176:179], v[68:71], v[216:219]
ds_read_b128 a[188:191], v238 offset:26624
v_cvt_pk_bf16_f32 v210, v162, v163
v_mfma_f32_16x16x32_bf16 v[76:79], a[172:175], v[64:67], v[76:79]
v_exp_f32_e64 v126, v126
v_mfma_f32_16x16x32_bf16 v[76:79], a[168:171], v[60:63], v[76:79]
v_exp_f32_e64 v127, v127
v_mfma_f32_16x16x32_bf16 v[76:79], a[164:167], v[56:59], v[76:79]
ds_read_b128 a[192:195], v238 offset:27648
v_cvt_pk_bf16_f32 v211, v160, v161
v_mfma_f32_16x16x32_bf16 v[76:79], a[160:163], v[52:55], v[76:79]
v_exp_f32_e64 v124, v124
v_mfma_f32_16x16x32_bf16 v[76:79], a[156:159], v[48:51], v[76:79]
v_exp_f32_e64 v125, v125
v_mfma_f32_16x16x32_bf16 v[80:83], a[156:159], v[96:99], v[216:219]
ds_read_b128 a[196:199], v238 offset:28672
v_cvt_pk_bf16_f32 v214, v166, v167
v_mfma_f32_16x16x32_bf16 v[80:83], a[160:163], v[100:103], v[80:83]
v_exp_f32_e64 v130, v130
v_mfma_f32_16x16x32_bf16 v[80:83], a[164:167], v[104:107], v[80:83]
v_exp_f32_e64 v131, v131
v_mfma_f32_16x16x32_bf16 v[80:83], a[168:171], v[108:111], v[80:83]
ds_read_b128 a[200:203], v238 offset:29696
v_cvt_pk_bf16_f32 v215, v164, v165
v_mfma_f32_16x16x32_bf16 v[80:83], a[172:175], v[112:115], v[80:83]
v_exp_f32_e64 v128, v128
v_mfma_f32_16x16x32_bf16 v[80:83], a[176:179], v[116:119], v[80:83]
s_waitcnt lgkmcnt(1)
v_exp_f32 v129, v129
v_mfma_f32_16x16x32_bf16 a[0:3], a[180:183], v[192:195], a[0:3]
ds_read_b128 a[156:159], v239 offset:18432
ds_read_b128 a[160:163], v239 offset:19456
v_mfma_f32_16x16x32_bf16 a[0:3], a[184:187], v[204:207], a[0:3]
v_rcp_f32_e64 v86, v86
v_mfma_f32_16x16x32_bf16 a[4:7], a[184:187], v[208:211], a[4:7]
v_rcp_f32_e64 v87, v87
v_mfma_f32_16x16x32_bf16 a[4:7], a[180:183], v[196:199], a[4:7]
ds_read_b128 a[164:167], v239 offset:20480
ds_read_b128 a[168:171], v239 offset:21504
v_mfma_f32_16x16x32_bf16 a[8:11], a[180:183], v[200:203], a[8:11]
v_rcp_f32_e64 v84, v84
v_mfma_f32_16x16x32_bf16 a[8:11], a[184:187], v[212:215], a[8:11]
v_rcp_f32_e64 v85, v85
v_mfma_f32_16x16x32_bf16 a[20:23], a[192:195], v[212:215], a[20:23]
ds_read_b128 a[172:175], v239 offset:22528
ds_read_b128 a[176:179], v239 offset:23552
v_mfma_f32_16x16x32_bf16 a[20:23], a[188:191], v[200:203], a[20:23]
s_add_u32 m0, s63, 27648
v_rcp_f32_e64 v90, v90
v_mfma_f32_16x16x32_bf16 a[16:19], a[188:191], v[196:199], a[16:19]
s_add_u32 s26, s62, 175104
ds_read_b128 v[216:219], v241 offset:704
v_mfma_f32_16x16x32_bf16 a[16:19], a[192:195], v[208:211], a[16:19]
buffer_load_dwordx4 v248, s[12:15], s26 offen offset:0 sc1 lds
v_mfma_f32_16x16x32_bf16 a[12:15], a[192:195], v[204:207], a[12:15]
v_rcp_f32_e64 v91, v91
v_mfma_f32_16x16x32_bf16 a[12:15], a[188:191], v[192:195], a[12:15]
v_rcp_f32 v88, v88
v_mfma_f32_16x16x32_bf16 a[24:27], a[196:199], v[192:195], a[24:27]
s_waitcnt lgkmcnt(1)
buffer_load_dwordx4 v248, s[12:15], s26 offen offset:1024 sc1 lds
v_mfma_f32_16x16x32_bf16 a[24:27], a[200:203], v[204:207], a[24:27]
v_rcp_f32_e64 v89, v89
v_mfma_f32_16x16x32_bf16 a[28:31], a[200:203], v[208:211], a[28:31]
v_rcp_f32_e64 v94, v94
v_mfma_f32_16x16x32_bf16 a[28:31], a[196:199], v[196:199], a[28:31]
buffer_load_dwordx4 v248, s[12:15], s26 offen offset:2048 sc1 lds
v_mfma_f32_16x16x32_bf16 a[32:35], a[196:199], v[200:203], a[32:35]
v_rcp_f32 v95, v95
v_mfma_f32_16x16x32_bf16 a[32:35], a[200:203], v[212:215], a[32:35]
s_waitcnt vmcnt(6) lgkmcnt(0)
ds_write_b8 v251, v250 offset:4
v_rcp_f32 v92, v92
v_rcp_f32 v93, v93
v_pk_add_f32 v[122:123], v[122:123], 1.0 op_sel_hi:[1,0]
v_pk_add_f32 v[120:121], v[120:121], 1.0 op_sel_hi:[1,0]
v_pk_add_f32 v[126:127], v[126:127], 1.0 op_sel_hi:[1,0]
v_pk_add_f32 v[124:125], v[124:125], 1.0 op_sel_hi:[1,0]
v_pk_add_f32 v[130:131], v[130:131], 1.0 op_sel_hi:[1,0]
v_pk_add_f32 v[128:129], v[128:129], 1.0 op_sel_hi:[1,0]
v_pk_mul_f32 v[86:87], v[24:25], v[86:87]
v_pk_mul_f32 v[84:85], v[26:27], v[84:85]
v_pk_mul_f32 v[90:91], v[28:29], v[90:91]
v_pk_mul_f32 v[88:89], v[30:31], v[88:89]
v_pk_mul_f32 v[94:95], v[32:33], v[94:95]
v_pk_mul_f32 v[92:93], v[34:35], v[92:93]
v_pk_fma_f32 v[134:135], v[72:73], v[72:73], s[28:29]
v_pk_fma_f32 v[132:133], v[74:75], v[74:75], s[28:29]
v_pk_fma_f32 v[138:139], v[76:77], v[76:77], s[28:29]
v_pk_fma_f32 v[136:137], v[78:79], v[78:79], s[28:29]
v_pk_fma_f32 v[142:143], v[80:81], v[80:81], s[28:29]
v_pk_fma_f32 v[140:141], v[82:83], v[82:83], s[28:29]
v_pk_mul_f32 v[134:135], v[72:73], v[134:135] neg_lo:[1,0] neg_hi:[1,0]
v_pk_mul_f32 v[132:133], v[74:75], v[132:133] neg_lo:[1,0] neg_hi:[1,0]
v_pk_mul_f32 v[138:139], v[76:77], v[138:139] neg_lo:[1,0] neg_hi:[1,0]
v_pk_mul_f32 v[136:137], v[78:79], v[136:137] neg_lo:[1,0] neg_hi:[1,0]
v_pk_mul_f32 v[142:143], v[80:81], v[142:143] neg_lo:[1,0] neg_hi:[1,0]
v_pk_mul_f32 v[140:141], v[82:83], v[140:141] neg_lo:[1,0] neg_hi:[1,0]
v_mfma_f32_16x16x32_bf16 v[144:147], a[156:159], v[0:3], v[216:219]
ds_read_b128 a[180:183], v238 offset:30720
v_cvt_pk_bf16_f32 v168, v86, v87
v_mfma_f32_16x16x32_bf16 v[144:147], a[160:163], v[4:7], v[144:147]
v_exp_f32_e64 v134, v134
v_mfma_f32_16x16x32_bf16 v[144:147], a[164:167], v[8:11], v[144:147]
v_exp_f32_e64 v135, v135
v_mfma_f32_16x16x32_bf16 v[144:147], a[168:171], v[12:15], v[144:147]
ds_read_b128 a[184:187], v238 offset:31744
v_cvt_pk_bf16_f32 v169, v84, v85
v_mfma_f32_16x16x32_bf16 v[144:147], a[172:175], v[16:19], v[144:147]
v_exp_f32_e64 v132, v132
v_mfma_f32_16x16x32_bf16 v[144:147], a[176:179], v[20:23], v[144:147]
v_exp_f32_e64 v133, v133
v_mfma_f32_16x16x32_bf16 v[148:151], a[176:179], v[68:71], v[216:219]
ds_read_b128 a[188:191], v238 offset:32768
v_cvt_pk_bf16_f32 v172, v90, v91
v_mfma_f32_16x16x32_bf16 v[148:151], a[172:175], v[64:67], v[148:151]
v_exp_f32_e64 v138, v138
v_mfma_f32_16x16x32_bf16 v[148:151], a[168:171], v[60:63], v[148:151]
ds_read_b32 v255, v252 offset:4
v_cvt_pk_bf16_f32 v173, v88, v89
v_mfma_f32_16x16x32_bf16 v[148:151], a[164:167], v[56:59], v[148:151]
ds_read_b128 a[192:195], v238 offset:33792
v_cvt_pk_bf16_f32 v176, v94, v95
v_mfma_f32_16x16x32_bf16 v[148:151], a[160:163], v[52:55], v[148:151]
v_exp_f32_e64 v139, v139
v_mfma_f32_16x16x32_bf16 v[148:151], a[156:159], v[48:51], v[148:151]
v_exp_f32_e64 v136, v136
v_mfma_f32_16x16x32_bf16 v[152:155], a[156:159], v[96:99], v[216:219]
ds_read_b128 a[196:199], v238 offset:34816
v_cvt_pk_bf16_f32 v177, v92, v93
v_mfma_f32_16x16x32_bf16 v[152:155], a[160:163], v[100:103], v[152:155]
v_exp_f32_e64 v137, v137
v_mfma_f32_16x16x32_bf16 v[152:155], a[164:167], v[104:107], v[152:155]
v_exp_f32_e64 v142, v142
v_mfma_f32_16x16x32_bf16 v[152:155], a[168:171], v[108:111], v[152:155]
s_waitcnt lgkmcnt(1)
ds_read_b128 a[200:203], v238 offset:35840
v_cmp_ne_u32 vcc, s33, v255
v_mfma_f32_16x16x32_bf16 v[152:155], a[172:175], v[112:115], v[152:155]
v_exp_f32_e64 v143, v143
v_mfma_f32_16x16x32_bf16 v[152:155], a[176:179], v[116:119], v[152:155]
s_cbranch_vccnz L_sy_p11
L_syb_p11:
v_exp_f32 v140, v140
v_mfma_f32_16x16x32_bf16 a[36:39], a[180:183], v[192:195], a[36:39]
ds_read_b128 a[156:159], v239 offset:24576
ds_read_b128 a[160:163], v239 offset:25600
v_mfma_f32_16x16x32_bf16 a[36:39], a[184:187], v[204:207], a[36:39]
v_exp_f32_e64 v141, v141
v_mfma_f32_16x16x32_bf16 a[40:43], a[184:187], v[208:211], a[40:43]
v_rcp_f32_e64 v122, v122
v_mfma_f32_16x16x32_bf16 a[40:43], a[180:183], v[196:199], a[40:43]
ds_read_b128 a[164:167], v239 offset:26624
ds_read_b128 a[168:171], v239 offset:27648
v_mfma_f32_16x16x32_bf16 a[44:47], a[180:183], v[200:203], a[44:47]
v_rcp_f32_e64 v123, v123
v_mfma_f32_16x16x32_bf16 a[44:47], a[184:187], v[212:215], a[44:47]
v_rcp_f32_e64 v120, v120
v_mfma_f32_16x16x32_bf16 a[56:59], a[192:195], v[212:215], a[56:59]
ds_read_b128 a[172:175], v239 offset:28672
ds_read_b128 a[176:179], v239 offset:29696
v_mfma_f32_16x16x32_bf16 a[56:59], a[188:191], v[200:203], a[56:59]
s_add_u32 m0, s23, 104448
v_rcp_f32_e64 v121, v121
v_mfma_f32_16x16x32_bf16 a[52:55], a[188:191], v[196:199], a[52:55]
s_add_u32 s26, s62, 196608
ds_read_b128 v[216:219], v241 offset:768
v_rcp_f32_e64 v129, v129
v_mfma_f32_16x16x32_bf16 a[52:55], a[192:195], v[208:211], a[52:55]
buffer_load_dwordx4 v248, s[12:15], s26 offen offset:0 sc1 lds
v_mfma_f32_16x16x32_bf16 a[48:51], a[192:195], v[204:207], a[48:51]
v_rcp_f32_e64 v126, v126
v_mfma_f32_16x16x32_bf16 a[48:51], a[188:191], v[192:195], a[48:51]
s_waitcnt lgkmcnt(1)
v_rcp_f32 v127, v127
v_mfma_f32_16x16x32_bf16 a[60:63], a[196:199], v[192:195], a[60:63]
buffer_load_dwordx4 v248, s[12:15], s26 offen offset:1024 sc1 lds
v_mfma_f32_16x16x32_bf16 a[60:63], a[200:203], v[204:207], a[60:63]
v_rcp_f32_e64 v124, v124
v_mfma_f32_16x16x32_bf16 a[64:67], a[200:203], v[208:211], a[64:67]
v_rcp_f32_e64 v125, v125
v_mfma_f32_16x16x32_bf16 a[64:67], a[196:199], v[196:199], a[64:67]
buffer_load_dwordx4 v248, s[12:15], s26 offen offset:2048 sc1 lds
v_mfma_f32_16x16x32_bf16 a[68:71], a[196:199], v[200:203], a[68:71]
v_rcp_f32_e64 v130, v130
v_mfma_f32_16x16x32_bf16 a[68:71], a[200:203], v[212:215], a[68:71]
s_waitcnt lgkmcnt(0)
v_rcp_f32 v131, v131
v_rcp_f32 v128, v128
v_add_f32 v134, 1.0, v134
v_add_f32_e64 v135, 1.0, v135
v_pk_add_f32 v[132:133], v[132:133], 1.0 op_sel_hi:[1,0]
v_pk_add_f32 v[138:139], v[138:139], 1.0 op_sel_hi:[1,0]
v_pk_add_f32 v[136:137], v[136:137], 1.0 op_sel_hi:[1,0]
v_pk_add_f32 v[142:143], v[142:143], 1.0 op_sel_hi:[1,0]
v_pk_add_f32 v[140:141], v[140:141], 1.0 op_sel_hi:[1,0]
v_pk_mul_f32 v[122:123], v[36:37], v[122:123]
v_pk_mul_f32 v[120:121], v[38:39], v[120:121]
v_pk_mul_f32 v[126:127], v[40:41], v[126:127]
v_pk_mul_f32 v[124:125], v[42:43], v[124:125]
v_pk_mul_f32 v[130:131], v[44:45], v[130:131]
v_pk_mul_f32 v[128:129], v[46:47], v[128:129]
v_pk_fma_f32 v[158:159], v[144:145], v[144:145], s[28:29]
v_pk_fma_f32 v[156:157], v[146:147], v[146:147], s[28:29]
v_pk_fma_f32 v[162:163], v[148:149], v[148:149], s[28:29]
v_pk_fma_f32 v[160:161], v[150:151], v[150:151], s[28:29]
v_pk_fma_f32 v[166:167], v[152:153], v[152:153], s[28:29]
v_pk_fma_f32 v[164:165], v[154:155], v[154:155], s[28:29]
v_pk_mul_f32 v[158:159], v[144:145], v[158:159] neg_lo:[1,0] neg_hi:[1,0]
v_pk_mul_f32 v[156:157], v[146:147], v[156:157] neg_lo:[1,0] neg_hi:[1,0]
v_pk_mul_f32 v[162:163], v[148:149], v[162:163] neg_lo:[1,0] neg_hi:[1,0]
v_pk_mul_f32 v[160:161], v[150:151], v[160:161] neg_lo:[1,0] neg_hi:[1,0]
v_pk_mul_f32 v[166:167], v[152:153], v[166:167] neg_lo:[1,0] neg_hi:[1,0]
v_pk_mul_f32 v[164:165], v[154:155], v[164:165] neg_lo:[1,0] neg_hi:[1,0]
v_mfma_f32_16x16x32_bf16 v[24:27], a[156:159], v[0:3], v[216:219]
ds_read_b128 a[180:183], v238 offset:36864
v_cvt_pk_bf16_f32 v170, v122, v123
v_mfma_f32_16x16x32_bf16 v[24:27], a[160:163], v[4:7], v[24:27]
v_exp_f32_e64 v158, v158
v_mfma_f32_16x16x32_bf16 v[24:27], a[164:167], v[8:11], v[24:27]
v_exp_f32_e64 v159, v159
v_mfma_f32_16x16x32_bf16 v[24:27], a[168:171], v[12:15], v[24:27]
ds_read_b128 a[184:187], v238 offset:37888
v_cvt_pk_bf16_f32 v171, v120, v121
v_mfma_f32_16x16x32_bf16 v[24:27], a[172:175], v[16:19], v[24:27]
v_exp_f32_e64 v156, v156
v_mfma_f32_16x16x32_bf16 v[24:27], a[176:179], v[20:23], v[24:27]
v_exp_f32_e64 v157, v157
v_mfma_f32_16x16x32_bf16 v[28:31], a[176:179], v[68:71], v[216:219]
ds_read_b128 a[188:191], v238 offset:38912
v_cvt_pk_bf16_f32 v174, v126, v127
v_mfma_f32_16x16x32_bf16 v[28:31], a[172:175], v[64:67], v[28:31]
v_exp_f32_e64 v162, v162
v_mfma_f32_16x16x32_bf16 v[28:31], a[168:171], v[60:63], v[28:31]
v_exp_f32_e64 v163, v163
v_mfma_f32_16x16x32_bf16 v[28:31], a[164:167], v[56:59], v[28:31]
ds_read_b128 a[192:195], v238 offset:39936
v_cvt_pk_bf16_f32 v175, v124, v125
v_mfma_f32_16x16x32_bf16 v[28:31], a[160:163], v[52:55], v[28:31]
v_exp_f32_e64 v160, v160
v_mfma_f32_16x16x32_bf16 v[28:31], a[156:159], v[48:51], v[28:31]
v_exp_f32_e64 v161, v161
v_mfma_f32_16x16x32_bf16 v[32:35], a[156:159], v[96:99], v[216:219]
ds_read_b128 a[196:199], v238 offset:40960
v_cvt_pk_bf16_f32 v178, v130, v131
v_mfma_f32_16x16x32_bf16 v[32:35], a[160:163], v[100:103], v[32:35]
v_exp_f32_e64 v166, v166
v_mfma_f32_16x16x32_bf16 v[32:35], a[164:167], v[104:107], v[32:35]
v_exp_f32_e64 v167, v167
v_mfma_f32_16x16x32_bf16 v[32:35], a[168:171], v[108:111], v[32:35]
ds_read_b128 a[200:203], v238 offset:41984
v_cvt_pk_bf16_f32 v179, v128, v129
v_mfma_f32_16x16x32_bf16 v[32:35], a[172:175], v[112:115], v[32:35]
v_exp_f32_e64 v164, v164
v_mfma_f32_16x16x32_bf16 v[32:35], a[176:179], v[116:119], v[32:35]
s_waitcnt lgkmcnt(1)
v_exp_f32 v165, v165
v_mfma_f32_16x16x32_bf16 a[72:75], a[180:183], v[192:195], a[72:75]
ds_read_b128 a[156:159], v239 offset:30720
ds_read_b128 a[160:163], v239 offset:31744
v_mfma_f32_16x16x32_bf16 a[72:75], a[184:187], v[204:207], a[72:75]
v_rcp_f32_e64 v134, v134
v_mfma_f32_16x16x32_bf16 a[76:79], a[184:187], v[208:211], a[76:79]
v_rcp_f32_e64 v135, v135
v_mfma_f32_16x16x32_bf16 a[76:79], a[180:183], v[196:199], a[76:79]
ds_read_b128 a[164:167], v239 offset:32768
ds_read_b128 a[168:171], v239 offset:33792
v_mfma_f32_16x16x32_bf16 a[80:83], a[180:183], v[200:203], a[80:83]
v_rcp_f32_e64 v132, v132
v_mfma_f32_16x16x32_bf16 a[80:83], a[184:187], v[212:215], a[80:83]
v_rcp_f32_e64 v133, v133
v_mfma_f32_16x16x32_bf16 a[92:95], a[192:195], v[212:215], a[92:95]
ds_read_b128 a[172:175], v239 offset:34816
ds_read_b128 a[176:179], v239 offset:35840
v_mfma_f32_16x16x32_bf16 a[92:95], a[188:191], v[200:203], a[92:95]
s_add_u32 m0, s23, 107520
v_rcp_f32_e64 v138, v138
v_mfma_f32_16x16x32_bf16 a[88:91], a[188:191], v[196:199], a[88:91]
s_add_u32 s26, s62, 199680
ds_read_b128 v[216:219], v241 offset:832
v_add_f32_e64 v158, 1.0, v158
v_mfma_f32_16x16x32_bf16 a[88:91], a[192:195], v[208:211], a[88:91]
buffer_load_dwordx4 v248, s[12:15], s26 offen offset:0 sc1 lds
v_mfma_f32_16x16x32_bf16 a[84:87], a[192:195], v[204:207], a[84:87]
v_rcp_f32_e64 v139, v139
v_mfma_f32_16x16x32_bf16 a[84:87], a[188:191], v[192:195], a[84:87]
v_rcp_f32 v136, v136
v_mfma_f32_16x16x32_bf16 a[96:99], a[196:199], v[192:195], a[96:99]
s_waitcnt lgkmcnt(1)
buffer_load_dwordx4 v248, s[12:15], s26 offen offset:1024 sc1 lds
v_mfma_f32_16x16x32_bf16 a[96:99], a[200:203], v[204:207], a[96:99]
v_rcp_f32_e64 v137, v137
v_mfma_f32_16x16x32_bf16 a[100:103], a[200:203], v[208:211], a[100:103]
v_rcp_f32_e64 v142, v142
v_mfma_f32_16x16x32_bf16 a[100:103], a[196:199], v[196:199], a[100:103]
buffer_load_dwordx4 v248, s[12:15], s26 offen offset:2048 sc1 lds
v_mfma_f32_16x16x32_bf16 a[104:107], a[196:199], v[200:203], a[104:107]
v_rcp_f32_e64 v143, v143
v_mfma_f32_16x16x32_bf16 a[104:107], a[200:203], v[212:215], a[104:107]
s_waitcnt lgkmcnt(0)
v_rcp_f32 v140, v140
v_rcp_f32 v141, v141
v_add_f32 v159, 1.0, v159
v_pk_add_f32 v[156:157], v[156:157], 1.0 op_sel_hi:[1,0]
v_pk_add_f32 v[162:163], v[162:163], 1.0 op_sel_hi:[1,0]
v_pk_add_f32 v[160:161], v[160:161], 1.0 op_sel_hi:[1,0]
v_pk_add_f32 v[166:167], v[166:167], 1.0 op_sel_hi:[1,0]
v_pk_add_f32 v[164:165], v[164:165], 1.0 op_sel_hi:[1,0]
v_pk_mul_f32 v[134:135], v[72:73], v[134:135]
v_pk_mul_f32 v[132:133], v[74:75], v[132:133]
v_pk_mul_f32 v[138:139], v[76:77], v[138:139]
v_pk_mul_f32 v[136:137], v[78:79], v[136:137]
v_pk_mul_f32 v[142:143], v[80:81], v[142:143]
v_pk_mul_f32 v[140:141], v[82:83], v[140:141]
v_pk_fma_f32 v[86:87], v[24:25], v[24:25], s[28:29]
v_pk_fma_f32 v[84:85], v[26:27], v[26:27], s[28:29]
v_pk_fma_f32 v[90:91], v[28:29], v[28:29], s[28:29]
v_pk_fma_f32 v[88:89], v[30:31], v[30:31], s[28:29]
v_pk_fma_f32 v[94:95], v[32:33], v[32:33], s[28:29]
v_pk_fma_f32 v[92:93], v[34:35], v[34:35], s[28:29]
v_pk_mul_f32 v[86:87], v[24:25], v[86:87] neg_lo:[1,0] neg_hi:[1,0]
v_pk_mul_f32 v[84:85], v[26:27], v[84:85] neg_lo:[1,0] neg_hi:[1,0]
v_pk_mul_f32 v[90:91], v[28:29], v[90:91] neg_lo:[1,0] neg_hi:[1,0]
v_pk_mul_f32 v[88:89], v[30:31], v[88:89] neg_lo:[1,0] neg_hi:[1,0]
v_pk_mul_f32 v[94:95], v[32:33], v[94:95] neg_lo:[1,0] neg_hi:[1,0]
v_pk_mul_f32 v[92:93], v[34:35], v[92:93] neg_lo:[1,0] neg_hi:[1,0]
v_mfma_f32_16x16x32_bf16 v[36:39], a[156:159], v[0:3], v[216:219]
ds_read_b128 a[180:183], v238 offset:43008
v_cvt_pk_bf16_f32 v180, v134, v135
v_mfma_f32_16x16x32_bf16 v[36:39], a[160:163], v[4:7], v[36:39]
v_exp_f32_e64 v86, v86
v_mfma_f32_16x16x32_bf16 v[36:39], a[164:167], v[8:11], v[36:39]
v_exp_f32_e64 v87, v87
v_mfma_f32_16x16x32_bf16 v[36:39], a[168:171], v[12:15], v[36:39]
ds_read_b128 a[184:187], v238 offset:44032
v_cvt_pk_bf16_f32 v181, v132, v133
v_mfma_f32_16x16x32_bf16 v[36:39], a[172:175], v[16:19], v[36:39]
v_exp_f32_e64 v84, v84
v_mfma_f32_16x16x32_bf16 v[36:39], a[176:179], v[20:23], v[36:39]
v_exp_f32_e64 v85, v85
v_mfma_f32_16x16x32_bf16 v[40:43], a[176:179], v[68:71], v[216:219]
ds_read_b128 a[188:191], v238 offset:45056
v_cvt_pk_bf16_f32 v184, v138, v139
v_mfma_f32_16x16x32_bf16 v[40:43], a[172:175], v[64:67], v[40:43]
v_exp_f32_e64 v90, v90
v_mfma_f32_16x16x32_bf16 v[40:43], a[168:171], v[60:63], v[40:43]
v_exp_f32_e64 v91, v91
v_mfma_f32_16x16x32_bf16 v[40:43], a[164:167], v[56:59], v[40:43]
ds_read_b128 a[192:195], v238 offset:46080
v_cvt_pk_bf16_f32 v185, v136, v137
v_mfma_f32_16x16x32_bf16 v[40:43], a[160:163], v[52:55], v[40:43]
v_exp_f32_e64 v88, v88
v_mfma_f32_16x16x32_bf16 v[40:43], a[156:159], v[48:51], v[40:43]
v_exp_f32_e64 v89, v89
v_mfma_f32_16x16x32_bf16 v[44:47], a[156:159], v[96:99], v[216:219]
ds_read_b128 a[196:199], v238 offset:47104
v_cvt_pk_bf16_f32 v188, v142, v143
v_mfma_f32_16x16x32_bf16 v[44:47], a[160:163], v[100:103], v[44:47]
v_exp_f32_e64 v94, v94
v_mfma_f32_16x16x32_bf16 v[44:47], a[164:167], v[104:107], v[44:47]
v_exp_f32_e64 v95, v95
v_mfma_f32_16x16x32_bf16 v[44:47], a[168:171], v[108:111], v[44:47]
ds_read_b128 a[200:203], v238 offset:48128
v_cvt_pk_bf16_f32 v189, v140, v141
v_mfma_f32_16x16x32_bf16 v[44:47], a[172:175], v[112:115], v[44:47]
v_xor_b32 v238, 49152, v238
v_rcp_f32_e64 v165, v165
v_mfma_f32_16x16x32_bf16 v[44:47], a[176:179], v[116:119], v[44:47]
s_waitcnt lgkmcnt(1)
v_exp_f32 v92, v92
v_mfma_f32_16x16x32_bf16 a[108:111], a[180:183], v[192:195], a[108:111]
ds_read_b128 a[156:159], v239 offset:36864
ds_read_b128 a[160:163], v239 offset:37888
v_mfma_f32_16x16x32_bf16 a[108:111], a[184:187], v[204:207], a[108:111]
v_exp_f32_e64 v93, v93
v_mfma_f32_16x16x32_bf16 a[112:115], a[184:187], v[208:211], a[112:115]
v_rcp_f32_e64 v158, v158
v_mfma_f32_16x16x32_bf16 a[112:115], a[180:183], v[196:199], a[112:115]
ds_read_b128 a[164:167], v239 offset:38912
ds_read_b128 a[168:171], v239 offset:39936
v_mfma_f32_16x16x32_bf16 a[116:119], a[180:183], v[200:203], a[116:119]
v_rcp_f32_e64 v159, v159
v_mfma_f32_16x16x32_bf16 a[116:119], a[184:187], v[212:215], a[116:119]
v_rcp_f32 v156, v156
v_mfma_f32_16x16x32_bf16 a[128:131], a[192:195], v[212:215], a[128:131]
s_xor_b32 s63, s63, s36
ds_read_b128 a[172:175], v239 offset:40960
ds_read_b128 a[176:179], v239 offset:41984
v_mfma_f32_16x16x32_bf16 a[128:131], a[188:191], v[200:203], a[128:131]
s_add_u32 m0, s63, 0
v_rcp_f32 v157, v157
v_mfma_f32_16x16x32_bf16 a[124:127], a[188:191], v[196:199], a[124:127]
s_add_u32 s26, s62, 221184
ds_read_b128 v[216:219], v241 offset:896
v_add_f32_e64 v86, 1.0, v86
v_mfma_f32_16x16x32_bf16 a[124:127], a[192:195], v[208:211], a[124:127]
buffer_load_dwordx4 v248, s[12:15], s26 offen offset:0 sc1 lds
v_mfma_f32_16x16x32_bf16 a[120:123], a[192:195], v[204:207], a[120:123]
v_rcp_f32_e64 v162, v162
v_mfma_f32_16x16x32_bf16 a[120:123], a[188:191], v[192:195], a[120:123]
v_rcp_f32 v163, v163
v_mfma_f32_16x16x32_bf16 a[132:135], a[196:199], v[192:195], a[132:135]
s_waitcnt lgkmcnt(1)
buffer_load_dwordx4 v248, s[12:15], s26 offen offset:1024 sc1 lds
v_mfma_f32_16x16x32_bf16 a[132:135], a[200:203], v[204:207], a[132:135]
v_rcp_f32_e64 v160, v160
v_mfma_f32_16x16x32_bf16 a[136:139], a[200:203], v[208:211], a[136:139]
v_rcp_f32_e64 v161, v161
v_mfma_f32_16x16x32_bf16 a[136:139], a[196:199], v[196:199], a[136:139]
buffer_load_dwordx4 v248, s[12:15], s26 offen offset:2048 sc1 lds
v_mfma_f32_16x16x32_bf16 a[140:143], a[196:199], v[200:203], a[140:143]
v_rcp_f32_e64 v166, v166
v_mfma_f32_16x16x32_bf16 a[140:143], a[200:203], v[212:215], a[140:143]
s_waitcnt lgkmcnt(0)
v_rcp_f32 v167, v167
v_rcp_f32 v164, v164
v_add_f32 v87, 1.0, v87
v_pk_add_f32 v[84:85], v[84:85], 1.0 op_sel_hi:[1,0]
v_pk_add_f32 v[90:91], v[90:91], 1.0 op_sel_hi:[1,0]
v_pk_add_f32 v[88:89], v[88:89], 1.0 op_sel_hi:[1,0]
v_pk_add_f32 v[94:95], v[94:95], 1.0 op_sel_hi:[1,0]
v_pk_add_f32 v[92:93], v[92:93], 1.0 op_sel_hi:[1,0]
v_pk_mul_f32 v[158:159], v[144:145], v[158:159]
v_pk_mul_f32 v[156:157], v[146:147], v[156:157]
v_pk_mul_f32 v[162:163], v[148:149], v[162:163]
v_pk_mul_f32 v[160:161], v[150:151], v[160:161]
v_pk_mul_f32 v[166:167], v[152:153], v[166:167]
v_pk_mul_f32 v[164:165], v[154:155], v[164:165]
v_pk_fma_f32 v[122:123], v[36:37], v[36:37], s[28:29]
v_pk_fma_f32 v[120:121], v[38:39], v[38:39], s[28:29]
v_pk_fma_f32 v[126:127], v[40:41], v[40:41], s[28:29]
v_pk_fma_f32 v[124:125], v[42:43], v[42:43], s[28:29]
v_pk_fma_f32 v[130:131], v[44:45], v[44:45], s[28:29]
v_pk_fma_f32 v[128:129], v[46:47], v[46:47], s[28:29]
v_pk_mul_f32 v[122:123], v[36:37], v[122:123] neg_lo:[1,0] neg_hi:[1,0]
v_pk_mul_f32 v[120:121], v[38:39], v[120:121] neg_lo:[1,0] neg_hi:[1,0]
v_pk_mul_f32 v[126:127], v[40:41], v[126:127] neg_lo:[1,0] neg_hi:[1,0]
v_pk_mul_f32 v[124:125], v[42:43], v[124:125] neg_lo:[1,0] neg_hi:[1,0]
v_pk_mul_f32 v[130:131], v[44:45], v[130:131] neg_lo:[1,0] neg_hi:[1,0]
v_pk_mul_f32 v[128:129], v[46:47], v[128:129] neg_lo:[1,0] neg_hi:[1,0]
v_mfma_f32_16x16x32_bf16 v[72:75], a[156:159], v[0:3], v[216:219]
ds_read_b128 a[180:183], v238 offset:0
v_cvt_pk_bf16_f32 v182, v158, v159
v_mfma_f32_16x16x32_bf16 v[72:75], a[160:163], v[4:7], v[72:75]
v_exp_f32_e64 v122, v122
v_mfma_f32_16x16x32_bf16 v[72:75], a[164:167], v[8:11], v[72:75]
v_exp_f32_e64 v123, v123
v_mfma_f32_16x16x32_bf16 v[72:75], a[168:171], v[12:15], v[72:75]
ds_read_b128 a[184:187], v238 offset:1024
v_cvt_pk_bf16_f32 v183, v156, v157
v_mfma_f32_16x16x32_bf16 v[72:75], a[172:175], v[16:19], v[72:75]
v_exp_f32_e64 v120, v120
v_mfma_f32_16x16x32_bf16 v[72:75], a[176:179], v[20:23], v[72:75]
v_exp_f32_e64 v121, v121
v_mfma_f32_16x16x32_bf16 v[76:79], a[176:179], v[68:71], v[216:219]
ds_read_b128 a[188:191], v238 offset:2048
v_cvt_pk_bf16_f32 v186, v162, v163
v_mfma_f32_16x16x32_bf16 v[76:79], a[172:175], v[64:67], v[76:79]
v_exp_f32_e64 v126, v126
v_mfma_f32_16x16x32_bf16 v[76:79], a[168:171], v[60:63], v[76:79]
v_exp_f32_e64 v127, v127
v_mfma_f32_16x16x32_bf16 v[76:79], a[164:167], v[56:59], v[76:79]
ds_read_b128 a[192:195], v238 offset:3072
v_cvt_pk_bf16_f32 v187, v160, v161
v_mfma_f32_16x16x32_bf16 v[76:79], a[160:163], v[52:55], v[76:79]
v_exp_f32_e64 v124, v124
v_mfma_f32_16x16x32_bf16 v[76:79], a[156:159], v[48:51], v[76:79]
v_exp_f32_e64 v125, v125
v_mfma_f32_16x16x32_bf16 v[80:83], a[156:159], v[96:99], v[216:219]
ds_read_b128 a[196:199], v238 offset:4096
v_cvt_pk_bf16_f32 v190, v166, v167
v_mfma_f32_16x16x32_bf16 v[80:83], a[160:163], v[100:103], v[80:83]
v_exp_f32_e64 v130, v130
v_mfma_f32_16x16x32_bf16 v[80:83], a[164:167], v[104:107], v[80:83]
v_exp_f32_e64 v131, v131
v_mfma_f32_16x16x32_bf16 v[80:83], a[168:171], v[108:111], v[80:83]
ds_read_b128 a[200:203], v238 offset:5120
v_cvt_pk_bf16_f32 v191, v164, v165
v_mfma_f32_16x16x32_bf16 v[80:83], a[172:175], v[112:115], v[80:83]
v_exp_f32_e64 v128, v128
v_mfma_f32_16x16x32_bf16 v[80:83], a[176:179], v[116:119], v[80:83]
s_waitcnt lgkmcnt(1)
v_exp_f32 v129, v129
v_mfma_f32_16x16x32_bf16 a[0:3], a[180:183], v[168:171], a[0:3]
ds_read_b128 a[156:159], v239 offset:43008
ds_read_b128 a[160:163], v239 offset:44032
v_mfma_f32_16x16x32_bf16 a[0:3], a[184:187], v[180:183], a[0:3]
v_rcp_f32_e64 v86, v86
v_mfma_f32_16x16x32_bf16 a[4:7], a[184:187], v[184:187], a[4:7]
v_rcp_f32_e64 v87, v87
v_mfma_f32_16x16x32_bf16 a[4:7], a[180:183], v[172:175], a[4:7]
ds_read_b128 a[164:167], v239 offset:45056
ds_read_b128 a[168:171], v239 offset:46080
v_mfma_f32_16x16x32_bf16 a[8:11], a[180:183], v[176:179], a[8:11]
v_rcp_f32_e64 v84, v84
v_mfma_f32_16x16x32_bf16 a[8:11], a[184:187], v[188:191], a[8:11]
v_rcp_f32_e64 v85, v85
v_mfma_f32_16x16x32_bf16 a[20:23], a[192:195], v[188:191], a[20:23]
ds_read_b128 a[172:175], v239 offset:47104
ds_read_b128 a[176:179], v239 offset:48128
v_mfma_f32_16x16x32_bf16 a[20:23], a[188:191], v[176:179], a[20:23]
s_add_u32 m0, s63, 3072
v_rcp_f32_e64 v90, v90
v_mfma_f32_16x16x32_bf16 a[16:19], a[188:191], v[172:175], a[16:19]
s_add_u32 s26, s62, 224256
ds_read_b128 v[216:219], v241 offset:960
v_rcp_f32_e64 v93, v93
v_mfma_f32_16x16x32_bf16 a[16:19], a[192:195], v[184:187], a[16:19]
s_add_u32 s62, s62, 98304
buffer_load_dwordx4 v248, s[12:15], s26 offen offset:0 sc1 lds
v_mfma_f32_16x16x32_bf16 a[12:15], a[192:195], v[180:183], a[12:15]
s_sub_u32 s37, s37, 1
v_add_u32 v241, 512, v241
v_mfma_f32_16x16x32_bf16 a[12:15], a[188:191], v[168:171], a[12:15]
s_cmp_lg_u32 s37, 0
v_rcp_f32 v91, v91
v_mfma_f32_16x16x32_bf16 a[24:27], a[196:199], v[168:171], a[24:27]
s_waitcnt lgkmcnt(1)
buffer_load_dwordx4 v248, s[12:15], s26 offen offset:1024 sc1 lds
v_mfma_f32_16x16x32_bf16 a[24:27], a[200:203], v[180:183], a[24:27]
v_rcp_f32_e64 v88, v88
v_mfma_f32_16x16x32_bf16 a[28:31], a[200:203], v[184:187], a[28:31]
v_rcp_f32_e64 v89, v89
v_mfma_f32_16x16x32_bf16 a[28:31], a[196:199], v[172:175], a[28:31]
buffer_load_dwordx4 v248, s[12:15], s26 offen offset:2048 sc1 lds
v_mfma_f32_16x16x32_bf16 a[32:35], a[196:199], v[176:179], a[32:35]
v_rcp_f32 v94, v94
v_mfma_f32_16x16x32_bf16 a[32:35], a[200:203], v[188:191], a[32:35]
s_waitcnt vmcnt(6) lgkmcnt(0)
ds_write_b8 v251, v250 offset:0
v_rcp_f32 v95, v95
v_rcp_f32 v92, v92
v_pk_add_f32 v[122:123], v[122:123], 1.0 op_sel_hi:[1,0]
v_pk_add_f32 v[120:121], v[120:121], 1.0 op_sel_hi:[1,0]
v_pk_add_f32 v[126:127], v[126:127], 1.0 op_sel_hi:[1,0]
v_pk_add_f32 v[124:125], v[124:125], 1.0 op_sel_hi:[1,0]
v_pk_add_f32 v[130:131], v[130:131], 1.0 op_sel_hi:[1,0]
v_pk_add_f32 v[128:129], v[128:129], 1.0 op_sel_hi:[1,0]
v_pk_mul_f32 v[86:87], v[24:25], v[86:87]
v_pk_mul_f32 v[84:85], v[26:27], v[84:85]
v_pk_mul_f32 v[90:91], v[28:29], v[90:91]
v_pk_mul_f32 v[88:89], v[30:31], v[88:89]
v_pk_mul_f32 v[94:95], v[32:33], v[94:95]
v_pk_mul_f32 v[92:93], v[34:35], v[92:93]
v_pk_fma_f32 v[134:135], v[72:73], v[72:73], s[28:29]
v_pk_fma_f32 v[132:133], v[74:75], v[74:75], s[28:29]
v_pk_fma_f32 v[138:139], v[76:77], v[76:77], s[28:29]
v_pk_fma_f32 v[136:137], v[78:79], v[78:79], s[28:29]
v_pk_fma_f32 v[142:143], v[80:81], v[80:81], s[28:29]
v_pk_fma_f32 v[140:141], v[82:83], v[82:83], s[28:29]
v_pk_mul_f32 v[134:135], v[72:73], v[134:135] neg_lo:[1,0] neg_hi:[1,0]
v_pk_mul_f32 v[132:133], v[74:75], v[132:133] neg_lo:[1,0] neg_hi:[1,0]
v_pk_mul_f32 v[138:139], v[76:77], v[138:139] neg_lo:[1,0] neg_hi:[1,0]
v_pk_mul_f32 v[136:137], v[78:79], v[136:137] neg_lo:[1,0] neg_hi:[1,0]
v_pk_mul_f32 v[142:143], v[80:81], v[142:143] neg_lo:[1,0] neg_hi:[1,0]
v_pk_mul_f32 v[140:141], v[82:83], v[140:141] neg_lo:[1,0] neg_hi:[1,0]
s_cbranch_scc1 L_loop_p
v_mfma_f32_16x16x32_bf16 v[144:147], a[156:159], v[0:3], v[216:219]
s_nop 0
ds_read_b128 a[180:183], v238 offset:6144
v_cvt_pk_bf16_f32 v192, v86, v87
v_mfma_f32_16x16x32_bf16 v[144:147], a[160:163], v[4:7], v[144:147]
v_exp_f32_e64 v134, v134
v_mfma_f32_16x16x32_bf16 v[144:147], a[164:167], v[8:11], v[144:147]
v_exp_f32_e64 v135, v135
v_mfma_f32_16x16x32_bf16 v[144:147], a[168:171], v[12:15], v[144:147]
ds_read_b128 a[184:187], v238 offset:7168
v_cvt_pk_bf16_f32 v193, v84, v85
v_mfma_f32_16x16x32_bf16 v[144:147], a[172:175], v[16:19], v[144:147]
v_exp_f32_e64 v132, v132
v_mfma_f32_16x16x32_bf16 v[144:147], a[176:179], v[20:23], v[144:147]
v_exp_f32_e64 v133, v133
v_mfma_f32_16x16x32_bf16 v[148:151], a[176:179], v[68:71], v[216:219]
ds_read_b128 a[188:191], v238 offset:8192
v_cvt_pk_bf16_f32 v196, v90, v91
v_mfma_f32_16x16x32_bf16 v[148:151], a[172:175], v[64:67], v[148:151]
v_exp_f32_e64 v138, v138
v_mfma_f32_16x16x32_bf16 v[148:151], a[168:171], v[60:63], v[148:151]
ds_read_b32 v255, v252 offset:0
v_cvt_pk_bf16_f32 v197, v88, v89
v_mfma_f32_16x16x32_bf16 v[148:151], a[164:167], v[56:59], v[148:151]
ds_read_b128 a[192:195], v238 offset:9216
v_cvt_pk_bf16_f32 v200, v94, v95
v_mfma_f32_16x16x32_bf16 v[148:151], a[160:163], v[52:55], v[148:151]
v_exp_f32_e64 v139, v139
v_mfma_f32_16x16x32_bf16 v[148:151], a[156:159], v[48:51], v[148:151]
v_exp_f32_e64 v136, v136
v_mfma_f32_16x16x32_bf16 v[152:155], a[156:159], v[96:99], v[216:219]
ds_read_b128 a[196:199], v238 offset:10240
v_cvt_pk_bf16_f32 v201, v92, v93
v_mfma_f32_16x16x32_bf16 v[152:155], a[160:163], v[100:103], v[152:155]
v_exp_f32_e64 v137, v137
v_mfma_f32_16x16x32_bf16 v[152:155], a[164:167], v[104:107], v[152:155]
v_exp_f32_e64 v142, v142
v_mfma_f32_16x16x32_bf16 v[152:155], a[168:171], v[108:111], v[152:155]
s_waitcnt lgkmcnt(1)
ds_read_b128 a[200:203], v238 offset:11264
v_cmp_ne_u32 vcc, s33, v255
v_mfma_f32_16x16x32_bf16 v[152:155], a[172:175], v[112:115], v[152:155]
v_exp_f32_e64 v143, v143
v_mfma_f32_16x16x32_bf16 v[152:155], a[176:179], v[116:119], v[152:155]
s_cbranch_vccnz L_sy_p47
L_syb_p47:
v_exp_f32 v140, v140
v_mfma_f32_16x16x32_bf16 a[36:39], a[180:183], v[168:171], a[36:39]
ds_read_b128 a[156:159], v238 offset:12288
v_mbcnt_lo_u32_b32 v208, -1, 0
v_mfma_f32_16x16x32_bf16 a[36:39], a[184:187], v[180:183], a[36:39]
v_exp_f32_e64 v141, v141
v_mfma_f32_16x16x32_bf16 a[40:43], a[184:187], v[184:187], a[40:43]
ds_read_b128 a[160:163], v238 offset:13312
v_mbcnt_hi_u32_b32 v208, -1, v208
s_mul_i32 s31, s20, 12288
s_add_u32 s31, s31, 104448
v_mfma_f32_16x16x32_bf16 a[40:43], a[180:183], v[172:175], a[40:43]
v_rcp_f32_e64 v122, v122
v_mfma_f32_16x16x32_bf16 a[44:47], a[180:183], v[176:179], a[44:47]
ds_read_b128 a[164:167], v238 offset:14336
v_lshrrev_b32_e64 v209, 3, v208
v_mfma_f32_16x16x32_bf16 a[44:47], a[184:187], v[188:191], a[44:47]
v_rcp_f32_e64 v123, v123
v_mfma_f32_16x16x32_bf16 a[56:59], a[192:195], v[188:191], a[56:59]
ds_read_b128 a[168:171], v238 offset:15360
v_and_b32_e64 v210, 7, v208
v_mfma_f32_16x16x32_bf16 a[56:59], a[188:191], v[176:179], a[56:59]
v_rcp_f32_e64 v120, v120
v_mfma_f32_16x16x32_bf16 a[52:55], a[188:191], v[172:175], a[52:55]
ds_read_b128 a[172:175], v238 offset:16384
v_xor_b32_e64 v210, v210, v209
v_mfma_f32_16x16x32_bf16 a[52:55], a[192:195], v[184:187], a[52:55]
v_rcp_f32_e64 v121, v121
v_mfma_f32_16x16x32_bf16 a[48:51], a[192:195], v[180:183], a[48:51]
ds_read_b128 a[176:179], v238 offset:17408
v_mul_u32_u24 v209, 384, v209
v_mfma_f32_16x16x32_bf16 a[48:51], a[188:191], v[168:171], a[48:51]
s_waitcnt lgkmcnt(2)
v_rcp_f32 v126, v126
v_mfma_f32_16x16x32_bf16 a[60:63], a[196:199], v[168:171], a[60:63]
v_rcp_f32_e64 v127, v127
v_mfma_f32_16x16x32_bf16 a[60:63], a[200:203], v[180:183], a[60:63]
v_rcp_f32_e64 v124, v124
v_mfma_f32_16x16x32_bf16 a[64:67], a[200:203], v[184:187], a[64:67]
v_rcp_f32_e64 v125, v125
v_mfma_f32_16x16x32_bf16 a[64:67], a[196:199], v[172:175], a[64:67]
v_rcp_f32_e64 v130, v130
v_mfma_f32_16x16x32_bf16 a[68:71], a[196:199], v[176:179], a[68:71]
v_rcp_f32_e64 v131, v131
v_mfma_f32_16x16x32_bf16 a[68:71], a[200:203], v[188:191], a[68:71]
s_waitcnt lgkmcnt(0)
v_rcp_f32 v128, v128
v_rcp_f32_e64 v129, v129
v_lshl_add_u32 v250, v210, 4, v209
v_pk_add_f32 v[134:135], v[134:135], 1.0 op_sel_hi:[1,0]
v_pk_add_f32 v[132:133], v[132:133], 1.0 op_sel_hi:[1,0]
v_pk_add_f32 v[138:139], v[138:139], 1.0 op_sel_hi:[1,0]
v_pk_add_f32 v[136:137], v[136:137], 1.0 op_sel_hi:[1,0]
v_pk_add_f32 v[142:143], v[142:143], 1.0 op_sel_hi:[1,0]
v_pk_add_f32 v[140:141], v[140:141], 1.0 op_sel_hi:[1,0]
v_pk_mul_f32 v[122:123], v[36:37], v[122:123]
v_pk_mul_f32 v[120:121], v[38:39], v[120:121]
v_pk_mul_f32 v[126:127], v[40:41], v[126:127]
v_pk_mul_f32 v[124:125], v[42:43], v[124:125]
v_pk_mul_f32 v[130:131], v[44:45], v[130:131]
v_pk_mul_f32 v[128:129], v[46:47], v[128:129]
v_pk_fma_f32 v[158:159], v[144:145], v[144:145], s[28:29]
v_pk_fma_f32 v[156:157], v[146:147], v[146:147], s[28:29]
v_pk_fma_f32 v[162:163], v[148:149], v[148:149], s[28:29]
v_pk_fma_f32 v[160:161], v[150:151], v[150:151], s[28:29]
v_pk_fma_f32 v[166:167], v[152:153], v[152:153], s[28:29]
v_pk_fma_f32 v[164:165], v[154:155], v[154:155], s[28:29]
v_pk_mul_f32 v[158:159], v[144:145], v[158:159] neg_lo:[1,0] neg_hi:[1,0]
v_pk_mul_f32 v[156:157], v[146:147], v[156:157] neg_lo:[1,0] neg_hi:[1,0]
v_pk_mul_f32 v[162:163], v[148:149], v[162:163] neg_lo:[1,0] neg_hi:[1,0]
v_pk_mul_f32 v[160:161], v[150:151], v[160:161] neg_lo:[1,0] neg_hi:[1,0]
v_pk_mul_f32 v[166:167], v[152:153], v[166:167] neg_lo:[1,0] neg_hi:[1,0]
v_pk_mul_f32 v[164:165], v[154:155], v[164:165] neg_lo:[1,0] neg_hi:[1,0]
v_mfma_f32_16x16x32_bf16 a[72:75], a[156:159], v[168:171], a[72:75]
ds_read_b128 a[180:183], v238 offset:18432
v_cvt_pk_bf16_f32 v194, v122, v123
v_rcp_f32_e64 v132, v132
v_mfma_f32_16x16x32_bf16 a[72:75], a[160:163], v[180:183], a[72:75]
v_exp_f32 v158, v158
v_rcp_f32 v133, v133
v_mfma_f32_16x16x32_bf16 a[76:79], a[160:163], v[184:187], a[76:79]
ds_read_b128 a[184:187], v238 offset:19456
v_cvt_pk_bf16_f32 v195, v120, v121
v_rcp_f32_e64 v138, v138
v_mfma_f32_16x16x32_bf16 a[76:79], a[156:159], v[172:175], a[76:79]
v_exp_f32 v159, v159
v_rcp_f32 v139, v139
v_mfma_f32_16x16x32_bf16 a[80:83], a[156:159], v[176:179], a[80:83]
ds_read_b128 a[188:191], v238 offset:20480
v_cvt_pk_bf16_f32 v198, v126, v127
v_rcp_f32_e64 v136, v136
v_mfma_f32_16x16x32_bf16 a[80:83], a[160:163], v[188:191], a[80:83]
v_exp_f32 v156, v156
v_rcp_f32 v137, v137
v_mfma_f32_16x16x32_bf16 a[92:95], a[168:171], v[188:191], a[92:95]
ds_read_b128 a[192:195], v238 offset:21504
v_cvt_pk_bf16_f32 v199, v124, v125
v_rcp_f32_e64 v142, v142
v_mfma_f32_16x16x32_bf16 a[92:95], a[164:167], v[176:179], a[92:95]
v_exp_f32 v157, v157
v_rcp_f32 v143, v143
v_mfma_f32_16x16x32_bf16 a[88:91], a[164:167], v[172:175], a[88:91]
ds_read_b128 a[196:199], v238 offset:22528
v_cvt_pk_bf16_f32 v202, v130, v131
v_rcp_f32_e64 v140, v140
v_mfma_f32_16x16x32_bf16 a[88:91], a[168:171], v[184:187], a[88:91]
v_exp_f32 v162, v162
v_rcp_f32 v141, v141
v_mfma_f32_16x16x32_bf16 a[84:87], a[168:171], v[180:183], a[84:87]
ds_read_b128 a[200:203], v238 offset:23552
v_cvt_pk_bf16_f32 v203, v128, v129
v_lshl_add_u32 v253, v208, 4, s31
v_mfma_f32_16x16x32_bf16 a[84:87], a[164:167], v[168:171], a[84:87]
v_exp_f32 v163, v163
v_lshlrev_b32 v255, 4, v208
v_mfma_f32_16x16x32_bf16 a[96:99], a[172:175], v[168:171], a[96:99]
v_exp_f32 v160, v160
v_and_b32 v209, 15, v208
v_mfma_f32_16x16x32_bf16 a[96:99], a[176:179], v[180:183], a[96:99]
v_exp_f32 v161, v161
v_lshrrev_b32 v210, 4, v208
v_mfma_f32_16x16x32_bf16 a[100:103], a[176:179], v[184:187], a[100:103]
v_exp_f32 v166, v166
v_and_b32 v208, 7, v208
v_mfma_f32_16x16x32_bf16 a[100:103], a[172:175], v[172:175], a[100:103]
v_exp_f32 v167, v167
v_xor_b32 v210, v210, v208
v_mfma_f32_16x16x32_bf16 a[104:107], a[172:175], v[176:179], a[104:107]
v_exp_f32 v164, v164
v_lshlrev_b32 v210, 4, v210
v_lshl_add_u32 v210, v209, 7, v210
v_mfma_f32_16x16x32_bf16 a[104:107], a[176:179], v[188:191], a[104:107]
s_waitcnt lgkmcnt(0)
v_exp_f32 v165, v165
v_rcp_f32 v134, v134
v_rcp_f32 v135, v135
v_add_u32 v251, s31, v210
v_xor_b32 v252, 64, v251
s_waitcnt vmcnt(0)
s_barrier
v_pk_add_f32 v[158:159], v[158:159], 1.0 op_sel_hi:[1,0]
v_pk_add_f32 v[156:157], v[156:157], 1.0 op_sel_hi:[1,0]
v_pk_add_f32 v[162:163], v[162:163], 1.0 op_sel_hi:[1,0]
v_pk_add_f32 v[160:161], v[160:161], 1.0 op_sel_hi:[1,0]
v_pk_add_f32 v[166:167], v[166:167], 1.0 op_sel_hi:[1,0]
v_pk_add_f32 v[164:165], v[164:165], 1.0 op_sel_hi:[1,0]
v_pk_mul_f32 v[134:135], v[72:73], v[134:135]
v_pk_mul_f32 v[132:133], v[74:75], v[132:133]
v_pk_mul_f32 v[138:139], v[76:77], v[138:139]
v_pk_mul_f32 v[136:137], v[78:79], v[136:137]
v_pk_mul_f32 v[142:143], v[80:81], v[142:143]
v_pk_mul_f32 v[140:141], v[82:83], v[140:141]
v_mfma_f32_16x16x32_bf16 a[108:111], a[180:183], v[168:171], a[108:111]
ds_read_b128 a[156:159], v238 offset:24576
v_cvt_pk_bf16_f32 v204, v134, v135
v_mfma_f32_16x16x32_bf16 a[108:111], a[184:187], v[180:183], a[108:111]
v_rcp_f32_e64 v158, v158
v_mfma_f32_16x16x32_bf16 a[112:115], a[184:187], v[184:187], a[112:115]
ds_read_b128 a[160:163], v238 offset:25600
v_cvt_pk_bf16_f32 v205, v132, v133
v_mfma_f32_16x16x32_bf16 a[112:115], a[180:183], v[172:175], a[112:115]
v_rcp_f32_e64 v159, v159
v_mfma_f32_16x16x32_bf16 a[116:119], a[180:183], v[176:179], a[116:119]
ds_read_b128 a[164:167], v238 offset:26624
v_cvt_pk_bf16_f32 v208, v138, v139
v_mfma_f32_16x16x32_bf16 a[116:119], a[184:187], v[188:191], a[116:119]
v_rcp_f32_e64 v156, v156
v_mfma_f32_16x16x32_bf16 a[128:131], a[192:195], v[188:191], a[128:131]
ds_read_b128 a[168:171], v238 offset:27648
v_cvt_pk_bf16_f32 v209, v136, v137
v_mfma_f32_16x16x32_bf16 a[128:131], a[188:191], v[176:179], a[128:131]
v_rcp_f32_e64 v157, v157
v_mfma_f32_16x16x32_bf16 a[124:127], a[188:191], v[172:175], a[124:127]
ds_read_b128 a[172:175], v238 offset:28672
v_cvt_pk_bf16_f32 v212, v142, v143
v_mfma_f32_16x16x32_bf16 a[124:127], a[192:195], v[184:187], a[124:127]
v_rcp_f32_e64 v162, v162
v_mfma_f32_16x16x32_bf16 a[120:123], a[192:195], v[180:183], a[120:123]
ds_read_b128 a[176:179], v238 offset:29696
v_cvt_pk_bf16_f32 v213, v140, v141
v_mfma_f32_16x16x32_bf16 a[120:123], a[188:191], v[168:171], a[120:123]
v_rcp_f32_e64 v163, v163
v_mfma_f32_16x16x32_bf16 a[132:135], a[196:199], v[168:171], a[132:135]
ds_read_b128 a[228:231], v238 offset:30720
v_rcp_f32_e64 v165, v165
v_mfma_f32_16x16x32_bf16 a[132:135], a[200:203], v[180:183], a[132:135]
ds_read_b128 a[232:235], v238 offset:31744
v_mfma_f32_16x16x32_bf16 a[136:139], a[200:203], v[184:187], a[136:139]
v_rcp_f32_e64 v160, v160
v_mfma_f32_16x16x32_bf16 a[136:139], a[196:199], v[172:175], a[136:139]
v_rcp_f32_e64 v161, v161
v_mfma_f32_16x16x32_bf16 a[140:143], a[196:199], v[176:179], a[140:143]
v_rcp_f32_e64 v166, v166
v_mfma_f32_16x16x32_bf16 a[140:143], a[200:203], v[188:191], a[140:143]
s_waitcnt lgkmcnt(0)
v_rcp_f32 v167, v167
v_rcp_f32_e64 v164, v164
v_pk_mul_f32 v[158:159], v[144:145], v[158:159]
v_pk_mul_f32 v[156:157], v[146:147], v[156:157]
v_pk_mul_f32 v[162:163], v[148:149], v[162:163]
v_pk_mul_f32 v[160:161], v[150:151], v[160:161]
v_pk_mul_f32 v[166:167], v[152:153], v[166:167]
v_pk_mul_f32 v[164:165], v[154:155], v[164:165]
v_cvt_pk_bf16_f32 v206, v158, v159
v_cvt_pk_bf16_f32 v207, v156, v157
v_cvt_pk_bf16_f32 v210, v162, v163
v_cvt_pk_bf16_f32 v211, v160, v161
v_cvt_pk_bf16_f32 v214, v166, v167
v_cvt_pk_bf16_f32 v215, v164, v165
v_mfma_f32_16x16x32_bf16 a[0:3], a[156:159], v[192:195], a[0:3]
ds_read_b128 a[180:183], v238 offset:32768
ds_read_b128 a[184:187], v238 offset:33792
v_mfma_f32_16x16x32_bf16 a[0:3], a[160:163], v[204:207], a[0:3]
ds_read_b128 a[188:191], v238 offset:34816
ds_read_b128 a[192:195], v238 offset:35840
v_mfma_f32_16x16x32_bf16 a[12:15], a[168:171], v[204:207], a[12:15]
ds_read_b128 a[196:199], v238 offset:36864
ds_read_b128 a[200:203], v238 offset:37888
v_mfma_f32_16x16x32_bf16 a[12:15], a[164:167], v[192:195], a[12:15]
ds_read_b128 a[236:239], v238 offset:38912
ds_read_b128 a[240:243], v238 offset:39936
v_mfma_f32_16x16x32_bf16 a[24:27], a[172:175], v[192:195], a[24:27]
v_accvgpr_read_b32 v156, a140
v_accvgpr_read_b32 v157, a141
v_mfma_f32_16x16x32_bf16 a[24:27], a[176:179], v[204:207], a[24:27]
v_accvgpr_read_b32 v158, a142
v_accvgpr_read_b32 v159, a143
v_mfma_f32_16x16x32_bf16 a[36:39], a[232:235], v[204:207], a[36:39]
v_accvgpr_read_b32 v152, a128
v_accvgpr_read_b32 v153, a129
v_mfma_f32_16x16x32_bf16 a[36:39], a[228:231], v[192:195], a[36:39]
v_accvgpr_read_b32 v154, a130
v_accvgpr_read_b32 v155, a131
v_mfma_f32_16x16x32_bf16 a[4:7], a[156:159], v[196:199], a[4:7]
v_accvgpr_read_b32 v148, a116
v_accvgpr_read_b32 v149, a117
v_mfma_f32_16x16x32_bf16 a[4:7], a[160:163], v[208:211], a[4:7]
v_accvgpr_read_b32 v40, a0
v_accvgpr_read_b32 v41, a1
v_accvgpr_read_b32 v42, a2
v_accvgpr_read_b32 v43, a3
v_mfma_f32_16x16x32_bf16 a[16:19], a[168:171], v[208:211], a[16:19]
v_accvgpr_read_b32 v44, a12
v_accvgpr_read_b32 v45, a13
v_accvgpr_read_b32 v46, a14
v_accvgpr_read_b32 v47, a15
v_mfma_f32_16x16x32_bf16 a[16:19], a[164:167], v[196:199], a[16:19]
v_cvt_pk_bf16_f32 v16, v40, v41
v_cvt_pk_bf16_f32 v17, v42, v43
v_cvt_pk_bf16_f32 v18, v44, v45
v_cvt_pk_bf16_f32 v19, v46, v47
v_mfma_f32_16x16x32_bf16 a[28:31], a[172:175], v[196:199], a[28:31]
ds_write_b128 v251, v[16:19] offset:0
v_accvgpr_read_b32 v48, a24
v_accvgpr_read_b32 v49, a25
v_accvgpr_read_b32 v50, a26
v_mfma_f32_16x16x32_bf16 a[28:31], a[176:179], v[208:211], a[28:31]
v_accvgpr_read_b32 v51, a27
v_accvgpr_read_b32 v52, a36
v_accvgpr_read_b32 v53, a37
v_mfma_f32_16x16x32_bf16 a[40:43], a[232:235], v[208:211], a[40:43]
v_accvgpr_read_b32 v54, a38
v_accvgpr_read_b32 v55, a39
v_cvt_pk_bf16_f32 v20, v48, v49
v_mfma_f32_16x16x32_bf16 a[40:43], a[228:231], v[196:199], a[40:43]
v_cvt_pk_bf16_f32 v21, v50, v51
v_cvt_pk_bf16_f32 v22, v52, v53
v_cvt_pk_bf16_f32 v23, v54, v55
v_mfma_f32_16x16x32_bf16 a[8:11], a[156:159], v[200:203], a[8:11]
ds_write_b128 v252, v[20:23] offset:0
v_accvgpr_read_b32 v150, a118
v_mfma_f32_16x16x32_bf16 a[8:11], a[160:163], v[212:215], a[8:11]
ds_read_b128 v[24:27], v253 offset:0
ds_read_b128 v[28:31], v253 offset:1024
v_accvgpr_read_b32 v56, a4
v_accvgpr_read_b32 v57, a5
v_accvgpr_read_b32 v58, a6
v_accvgpr_read_b32 v59, a7
v_mfma_f32_16x16x32_bf16 a[20:23], a[168:171], v[212:215], a[20:23]
v_accvgpr_read_b32 v60, a16
v_accvgpr_read_b32 v61, a17
v_accvgpr_read_b32 v62, a18
v_accvgpr_read_b32 v63, a19
v_mfma_f32_16x16x32_bf16 a[20:23], a[164:167], v[200:203], a[20:23]
v_cvt_pk_bf16_f32 v12, v56, v57
v_cvt_pk_bf16_f32 v13, v58, v59
v_cvt_pk_bf16_f32 v14, v60, v61
v_cvt_pk_bf16_f32 v15, v62, v63
v_mfma_f32_16x16x32_bf16 a[32:35], a[172:175], v[200:203], a[32:35]
ds_write_b128 v251, v[12:15] offset:2048
v_accvgpr_read_b32 v64, a28
v_accvgpr_read_b32 v65, a29
v_accvgpr_read_b32 v66, a30
v_mfma_f32_16x16x32_bf16 a[32:35], a[176:179], v[212:215], a[32:35]
s_waitcnt lgkmcnt(2)
buffer_store_dwordx4 v[24:27], v250, s[16:19], s44 offen offset:0 sc1
v_accvgpr_read_b32 v67, a31
v_accvgpr_read_b32 v68, a40
v_accvgpr_read_b32 v69, a41
v_mfma_f32_16x16x32_bf16 a[44:47], a[232:235], v[212:215], a[44:47]
s_waitcnt lgkmcnt(1)
buffer_store_dwordx4 v[28:31], v250, s[16:19], s44 offen offset:3072 sc1
v_accvgpr_read_b32 v70, a42
v_accvgpr_read_b32 v71, a43
v_cvt_pk_bf16_f32 v16, v64, v65
v_mfma_f32_16x16x32_bf16 a[44:47], a[228:231], v[200:203], a[44:47]
v_cvt_pk_bf16_f32 v17, v66, v67
v_cvt_pk_bf16_f32 v18, v68, v69
v_cvt_pk_bf16_f32 v19, v70, v71
v_mfma_f32_16x16x32_bf16 a[48:51], a[180:183], v[192:195], a[48:51]
ds_write_b128 v252, v[16:19] offset:2048
v_accvgpr_read_b32 v151, a119
v_mfma_f32_16x16x32_bf16 a[48:51], a[184:187], v[204:207], a[48:51]
ds_read_b128 v[32:35], v253 offset:2048
ds_read_b128 v[36:39], v253 offset:3072
v_accvgpr_read_b32 v72, a8
v_accvgpr_read_b32 v73, a9
v_accvgpr_read_b32 v74, a10
v_accvgpr_read_b32 v75, a11
v_mfma_f32_16x16x32_bf16 a[60:63], a[192:195], v[204:207], a[60:63]
ds_read_b128 a[156:159], v238 offset:40960
ds_read_b128 a[160:163], v238 offset:41984
v_accvgpr_read_b32 v76, a20
v_accvgpr_read_b32 v77, a21
v_accvgpr_read_b32 v78, a22
v_accvgpr_read_b32 v79, a23
v_mfma_f32_16x16x32_bf16 a[60:63], a[188:191], v[192:195], a[60:63]
ds_read_b128 a[164:167], v238 offset:43008
ds_read_b128 a[168:171], v238 offset:44032
v_cvt_pk_bf16_f32 v20, v72, v73
v_cvt_pk_bf16_f32 v21, v74, v75
v_cvt_pk_bf16_f32 v22, v76, v77
v_cvt_pk_bf16_f32 v23, v78, v79
v_mfma_f32_16x16x32_bf16 a[72:75], a[196:199], v[192:195], a[72:75]
ds_write_b128 v251, v[20:23] offset:4096
v_accvgpr_read_b32 v80, a32
v_accvgpr_read_b32 v81, a33
v_accvgpr_read_b32 v82, a34
v_mfma_f32_16x16x32_bf16 a[72:75], a[200:203], v[204:207], a[72:75]
s_waitcnt lgkmcnt(6)
buffer_store_dwordx4 v[32:35], v250, s[16:19], s45 offen offset:0 sc1
v_accvgpr_read_b32 v83, a35
v_accvgpr_read_b32 v84, a44
v_accvgpr_read_b32 v85, a45
v_mfma_f32_16x16x32_bf16 a[84:87], a[240:243], v[204:207], a[84:87]
s_waitcnt lgkmcnt(5)
buffer_store_dwordx4 v[36:39], v250, s[16:19], s45 offen offset:3072 sc1
v_accvgpr_read_b32 v86, a46
v_accvgpr_read_b32 v87, a47
v_cvt_pk_bf16_f32 v12, v80, v81
v_mfma_f32_16x16x32_bf16 a[84:87], a[236:239], v[192:195], a[84:87]
ds_read_b128 a[172:175], v238 offset:45056
ds_read_b128 a[176:179], v238 offset:46080
v_cvt_pk_bf16_f32 v13, v82, v83
v_cvt_pk_bf16_f32 v14, v84, v85
v_cvt_pk_bf16_f32 v15, v86, v87
v_mfma_f32_16x16x32_bf16 a[52:55], a[180:183], v[196:199], a[52:55]
ds_write_b128 v252, v[12:15] offset:4096
v_accvgpr_read_b32 v144, a104
v_mfma_f32_16x16x32_bf16 a[52:55], a[184:187], v[208:211], a[52:55]
ds_read_b128 v[24:27], v253 offset:4096
ds_read_b128 v[28:31], v253 offset:5120
v_accvgpr_read_b32 v88, a48
v_accvgpr_read_b32 v89, a49
v_accvgpr_read_b32 v90, a50
v_accvgpr_read_b32 v91, a51
v_mfma_f32_16x16x32_bf16 a[64:67], a[192:195], v[208:211], a[64:67]
ds_read_b128 a[228:231], v238 offset:47104
ds_read_b128 a[232:235], v238 offset:48128
v_accvgpr_read_b32 v92, a60
v_accvgpr_read_b32 v93, a61
v_accvgpr_read_b32 v94, a62
v_accvgpr_read_b32 v95, a63
v_mfma_f32_16x16x32_bf16 a[64:67], a[188:191], v[196:199], a[64:67]
v_cvt_pk_bf16_f32 v16, v88, v89
v_cvt_pk_bf16_f32 v17, v90, v91
v_cvt_pk_bf16_f32 v18, v92, v93
v_cvt_pk_bf16_f32 v19, v94, v95
v_mfma_f32_16x16x32_bf16 a[76:79], a[196:199], v[196:199], a[76:79]
ds_write_b128 v251, v[16:19] offset:6144
v_accvgpr_read_b32 v96, a72
v_accvgpr_read_b32 v97, a73
v_accvgpr_read_b32 v98, a74
v_mfma_f32_16x16x32_bf16 a[76:79], a[200:203], v[208:211], a[76:79]
s_waitcnt lgkmcnt(4)
buffer_store_dwordx4 v[24:27], v250, s[16:19], s46 offen offset:0 sc1
v_accvgpr_read_b32 v99, a75
v_accvgpr_read_b32 v100, a84
v_accvgpr_read_b32 v101, a85
v_mfma_f32_16x16x32_bf16 a[88:91], a[240:243], v[208:211], a[88:91]
s_waitcnt lgkmcnt(3)
buffer_store_dwordx4 v[28:31], v250, s[16:19], s46 offen offset:3072 sc1
v_accvgpr_read_b32 v102, a86
v_accvgpr_read_b32 v103, a87
v_cvt_pk_bf16_f32 v20, v96, v97
v_mfma_f32_16x16x32_bf16 a[88:91], a[236:239], v[196:199], a[88:91]
v_cvt_pk_bf16_f32 v21, v98, v99
v_cvt_pk_bf16_f32 v22, v100, v101
v_cvt_pk_bf16_f32 v23, v102, v103
v_mfma_f32_16x16x32_bf16 a[56:59], a[180:183], v[200:203], a[56:59]
ds_write_b128 v252, v[20:23] offset:6144
v_accvgpr_read_b32 v145, a105
v_mfma_f32_16x16x32_bf16 a[56:59], a[184:187], v[212:215], a[56:59]
ds_read_b128 v[32:35], v253 offset:6144
ds_read_b128 v[36:39], v253 offset:7168
v_accvgpr_read_b32 v104, a52
v_accvgpr_read_b32 v105, a53
v_accvgpr_read_b32 v106, a54
v_accvgpr_read_b32 v107, a55
v_mfma_f32_16x16x32_bf16 a[68:71], a[192:195], v[212:215], a[68:71]
v_accvgpr_read_b32 v108, a64
v_accvgpr_read_b32 v109, a65
v_accvgpr_read_b32 v110, a66
v_accvgpr_read_b32 v111, a67
v_mfma_f32_16x16x32_bf16 a[68:71], a[188:191], v[200:203], a[68:71]
v_cvt_pk_bf16_f32 v12, v104, v105
v_cvt_pk_bf16_f32 v13, v106, v107
v_cvt_pk_bf16_f32 v14, v108, v109
v_cvt_pk_bf16_f32 v15, v110, v111
v_mfma_f32_16x16x32_bf16 a[80:83], a[196:199], v[200:203], a[80:83]
ds_write_b128 v251, v[12:15] offset:8192
v_accvgpr_read_b32 v112, a76
v_accvgpr_read_b32 v113, a77
v_accvgpr_read_b32 v114, a78
v_mfma_f32_16x16x32_bf16 a[80:83], a[200:203], v[212:215], a[80:83]
s_waitcnt lgkmcnt(2)
buffer_store_dwordx4 v[32:35], v250, s[16:19], s44 offen offset:128 sc1
v_accvgpr_read_b32 v115, a79
v_accvgpr_read_b32 v116, a88
v_accvgpr_read_b32 v117, a89
v_mfma_f32_16x16x32_bf16 a[92:95], a[240:243], v[212:215], a[92:95]
s_waitcnt lgkmcnt(1)
buffer_store_dwordx4 v[36:39], v250, s[16:19], s44 offen offset:3200 sc1
v_accvgpr_read_b32 v118, a90
v_accvgpr_read_b32 v119, a91
v_cvt_pk_bf16_f32 v16, v112, v113
v_mfma_f32_16x16x32_bf16 a[92:95], a[236:239], v[200:203], a[92:95]
v_cvt_pk_bf16_f32 v17, v114, v115
v_cvt_pk_bf16_f32 v18, v116, v117
v_cvt_pk_bf16_f32 v19, v118, v119
v_mfma_f32_16x16x32_bf16 a[96:99], a[156:159], v[192:195], a[96:99]
ds_write_b128 v252, v[16:19] offset:8192
v_accvgpr_read_b32 v146, a106
v_mfma_f32_16x16x32_bf16 a[96:99], a[160:163], v[204:207], a[96:99]
ds_read_b128 v[24:27], v253 offset:8192
ds_read_b128 v[28:31], v253 offset:9216
v_accvgpr_read_b32 v120, a56
v_accvgpr_read_b32 v121, a57
v_accvgpr_read_b32 v122, a58
v_accvgpr_read_b32 v123, a59
v_mfma_f32_16x16x32_bf16 a[108:111], a[168:171], v[204:207], a[108:111]
v_accvgpr_read_b32 v124, a68
v_accvgpr_read_b32 v125, a69
v_accvgpr_read_b32 v126, a70
v_accvgpr_read_b32 v127, a71
v_mfma_f32_16x16x32_bf16 a[108:111], a[164:167], v[192:195], a[108:111]
v_cvt_pk_bf16_f32 v20, v120, v121
v_cvt_pk_bf16_f32 v21, v122, v123
v_cvt_pk_bf16_f32 v22, v124, v125
v_cvt_pk_bf16_f32 v23, v126, v127
v_mfma_f32_16x16x32_bf16 a[120:123], a[172:175], v[192:195], a[120:123]
ds_write_b128 v251, v[20:23] offset:10240
v_accvgpr_read_b32 v128, a80
v_accvgpr_read_b32 v129, a81
v_accvgpr_read_b32 v130, a82
v_mfma_f32_16x16x32_bf16 a[120:123], a[176:179], v[204:207], a[120:123]
s_waitcnt lgkmcnt(2)
buffer_store_dwordx4 v[24:27], v250, s[16:19], s45 offen offset:128 sc1
v_accvgpr_read_b32 v131, a83
v_accvgpr_read_b32 v132, a92
v_accvgpr_read_b32 v133, a93
v_mfma_f32_16x16x32_bf16 a[132:135], a[232:235], v[204:207], a[132:135]
s_waitcnt lgkmcnt(1)
buffer_store_dwordx4 v[28:31], v250, s[16:19], s45 offen offset:3200 sc1
v_accvgpr_read_b32 v134, a94
v_accvgpr_read_b32 v135, a95
v_cvt_pk_bf16_f32 v12, v128, v129
v_mfma_f32_16x16x32_bf16 a[132:135], a[228:231], v[192:195], a[132:135]
v_cvt_pk_bf16_f32 v13, v130, v131
v_cvt_pk_bf16_f32 v14, v132, v133
v_cvt_pk_bf16_f32 v15, v134, v135
v_mfma_f32_16x16x32_bf16 a[100:103], a[156:159], v[196:199], a[100:103]
ds_write_b128 v252, v[12:15] offset:10240
v_accvgpr_read_b32 v147, a107
v_mfma_f32_16x16x32_bf16 a[100:103], a[160:163], v[208:211], a[100:103]
ds_read_b128 v[32:35], v253 offset:10240
ds_read_b128 v[36:39], v253 offset:11264
v_accvgpr_read_b32 v40, a96
v_accvgpr_read_b32 v41, a97
v_accvgpr_read_b32 v42, a98
v_accvgpr_read_b32 v43, a99
v_mfma_f32_16x16x32_bf16 a[112:115], a[168:171], v[208:211], a[112:115]
v_accvgpr_read_b32 v44, a108
v_accvgpr_read_b32 v45, a109
v_accvgpr_read_b32 v46, a110
v_accvgpr_read_b32 v47, a111
v_mfma_f32_16x16x32_bf16 a[112:115], a[164:167], v[196:199], a[112:115]
v_cvt_pk_bf16_f32 v16, v40, v41
v_cvt_pk_bf16_f32 v17, v42, v43
v_cvt_pk_bf16_f32 v18, v44, v45
v_cvt_pk_bf16_f32 v19, v46, v47
v_mfma_f32_16x16x32_bf16 a[124:127], a[172:175], v[196:199], a[124:127]
ds_write_b128 v251, v[16:19] offset:0
v_accvgpr_read_b32 v48, a120
v_accvgpr_read_b32 v49, a121
v_accvgpr_read_b32 v50, a122
v_mfma_f32_16x16x32_bf16 a[124:127], a[176:179], v[208:211], a[124:127]
s_waitcnt lgkmcnt(2)
buffer_store_dwordx4 v[32:35], v250, s[16:19], s46 offen offset:128 sc1
v_accvgpr_read_b32 v51, a123
v_accvgpr_read_b32 v52, a132
v_accvgpr_read_b32 v53, a133
v_mfma_f32_16x16x32_bf16 a[136:139], a[232:235], v[208:211], a[136:139]
s_waitcnt lgkmcnt(1)
buffer_store_dwordx4 v[36:39], v250, s[16:19], s46 offen offset:3200 sc1
v_accvgpr_read_b32 v54, a134
v_accvgpr_read_b32 v55, a135
v_cvt_pk_bf16_f32 v20, v48, v49
v_mfma_f32_16x16x32_bf16 a[136:139], a[228:231], v[196:199], a[136:139]
v_cvt_pk_bf16_f32 v21, v50, v51
v_cvt_pk_bf16_f32 v22, v52, v53
v_cvt_pk_bf16_f32 v23, v54, v55
v_mfma_f32_16x16x32_bf16 v[144:147], a[156:159], v[200:203], v[144:147]
ds_write_b128 v252, v[20:23] offset:0
v_mfma_f32_16x16x32_bf16 v[144:147], a[160:163], v[212:215], v[144:147]
ds_read_b128 v[24:27], v253 offset:0
ds_read_b128 v[28:31], v253 offset:1024
v_accvgpr_read_b32 v168, a100
v_accvgpr_read_b32 v169, a101
v_accvgpr_read_b32 v170, a102
v_accvgpr_read_b32 v171, a103
v_mfma_f32_16x16x32_bf16 v[148:151], a[168:171], v[212:215], v[148:151]
v_accvgpr_read_b32 v172, a112
v_accvgpr_read_b32 v173, a113
v_accvgpr_read_b32 v174, a114
v_accvgpr_read_b32 v175, a115
v_mfma_f32_16x16x32_bf16 v[148:151], a[164:167], v[200:203], v[148:151]
v_cvt_pk_bf16_f32 v12, v168, v169
v_cvt_pk_bf16_f32 v13, v170, v171
v_cvt_pk_bf16_f32 v14, v172, v173
v_cvt_pk_bf16_f32 v15, v174, v175
v_mfma_f32_16x16x32_bf16 v[152:155], a[172:175], v[200:203], v[152:155]
ds_write_b128 v251, v[12:15] offset:2048
v_accvgpr_read_b32 v176, a124
v_accvgpr_read_b32 v177, a125
v_accvgpr_read_b32 v178, a126
v_mfma_f32_16x16x32_bf16 v[152:155], a[176:179], v[212:215], v[152:155]
s_waitcnt lgkmcnt(2)
buffer_store_dwordx4 v[24:27], v250, s[16:19], s44 offen offset:256 sc1
v_accvgpr_read_b32 v179, a127
v_accvgpr_read_b32 v180, a136
v_accvgpr_read_b32 v181, a137
v_mfma_f32_16x16x32_bf16 v[156:159], a[232:235], v[212:215], v[156:159]
s_waitcnt lgkmcnt(1)
buffer_store_dwordx4 v[28:31], v250, s[16:19], s44 offen offset:3328 sc1
v_accvgpr_read_b32 v182, a138
v_accvgpr_read_b32 v183, a139
v_cvt_pk_bf16_f32 v16, v176, v177
v_mfma_f32_16x16x32_bf16 v[156:159], a[228:231], v[200:203], v[156:159]
v_cvt_pk_bf16_f32 v17, v178, v179
v_cvt_pk_bf16_f32 v18, v180, v181
v_cvt_pk_bf16_f32 v19, v182, v183
ds_write_b128 v252, v[16:19] offset:2048
ds_read_b128 v[32:35], v253 offset:2048
ds_read_b128 v[36:39], v253 offset:3072
v_cvt_pk_bf16_f32 v20, v144, v145
v_cvt_pk_bf16_f32 v21, v146, v147
v_cvt_pk_bf16_f32 v22, v148, v149
v_cvt_pk_bf16_f32 v23, v150, v151
ds_write_b128 v251, v[20:23] offset:4096
v_cvt_pk_bf16_f32 v12, v152, v153
v_cvt_pk_bf16_f32 v13, v154, v155
v_cvt_pk_bf16_f32 v14, v156, v157
v_cvt_pk_bf16_f32 v15, v158, v159
ds_write_b128 v252, v[12:15] offset:4096
ds_read_b128 v[24:27], v253 offset:4096
ds_read_b128 v[28:31], v253 offset:5120
s_waitcnt lgkmcnt(5)
buffer_store_dwordx4 v[32:35], v250, s[16:19], s45 offen offset:256 sc1
s_waitcnt lgkmcnt(4)
buffer_store_dwordx4 v[36:39], v250, s[16:19], s45 offen offset:3328 sc1
s_waitcnt lgkmcnt(1)
buffer_store_dwordx4 v[24:27], v250, s[16:19], s46 offen offset:256 sc1
s_waitcnt lgkmcnt(0)
buffer_store_dwordx4 v[28:31], v250, s[16:19], s46 offen offset:3328 sc1
s_waitcnt vmcnt(0)
s_endpgm
L_slow_p:
s_mov_b32 s26, 0
L_poll_p:
v_cmp_eq_u32 vcc, s43, v204
v_cmp_eq_u32 s[34:35], s43, v205
v_cmp_eq_u32 s[64:65], s43, v206
v_cmp_eq_u32 s[66:67], s43, v207
s_nop 3
s_and_b64 vcc, vcc, s[34:35]
s_and_b64 s[64:65], s[64:65], s[66:67]
s_and_b64 vcc, vcc, s[64:65]
s_bitset1_b64 vcc, 63
s_or_b32 vcc_lo, vcc_lo, 0x0fffffff
s_cmp_eq_u64 vcc, -1
s_cbranch_scc1 L_go_p
s_add_u32 s26, s26, 1
s_cmp_gt_u32 s26, 200000
s_cbranch_scc1 L_go_p
s_sleep 4
s_mov_b32 s27, 589824
buffer_load_dwordx4 v[204:207], v238, s[12:15], s27 offen sc1
s_waitcnt vmcnt(0)
s_branch L_poll_p
L_go_p:
s_branch L_fast_p
L_sy_p7:
ds_read_b32 v255, v252 offset:0
s_waitcnt lgkmcnt(0)
v_cmp_ne_u32 vcc, s33, v255
s_nop 4
s_cbranch_vccnz L_sy_p7
s_branch L_syb_p7
L_sy_p11:
ds_read_b32 v255, v252 offset:4
s_waitcnt lgkmcnt(0)
v_cmp_ne_u32 vcc, s33, v255
s_nop 4
s_cbranch_vccnz L_sy_p11
s_branch L_syb_p11
L_sy_p47:
ds_read_b32 v255, v252 offset:0
s_waitcnt lgkmcnt(0)
v_cmp_ne_u32 vcc, s33, v255
s_nop 4
s_cbranch_vccnz L_sy_p47
s_branch L_syb_p47
L_end_p:
)ASM"
    : "+{v0}"(tid)
    : [ka] "s"(__builtin_amdgcn_kernarg_segment_ptr()), [bid] "s"(bid), [lb] "s"(ldsbase)
    : "memory", "vcc", "scc", CLOB_V, CLOB_A, CLOB_S);
}

extern "C" void kernel_launch(void* const* d_in, const int* in_sizes, int n_in,
                              void* d_out, int out_size, void* d_ws, size_t ws_size,
                              hipStream_t stream) {
  convnext_fused_kernel<<<256, 256, 0, stream>>>((const uint16_t*)d_in[0], (const uint16_t*)d_in[1], (const uint16_t*)d_in[2], (const uint16_t*)d_in[3],
      (const uint16_t*)d_in[4], (const uint16_t*)d_in[5], (const uint16_t*)d_in[6], (uint8_t*)d_ws, (uint16_t*)d_out);
}
```
